```python
import torch

def _softmax(x, axis=-1):
    return torch.softmax(x, dim=axis)
def _gelu(x):
    return torch.nn.functional.gelu(x, approximate="tanh")
def _silu(x):
    return torch.nn.functional.silu(x)
def _sigmoid(x):
    return torch.sigmoid(x)
def _layernorm(x, axis=-1, eps=1e-5):
    m = x.mean(dim=axis, keepdim=True)
    v = x.var(dim=axis, keepdim=True, unbiased=False)
    return (x - m) / torch.sqrt(v + eps)
def _scatter_add(tgt, idx, src, axis=0):
    return tgt.clone().index_add_(axis, idx, src)
def _segment_sum(src, seg, n):
    out = torch.zeros((n,) + src.shape[1:], dtype=src.dtype)
    return out.index_add_(0, seg, src)
def _pad(x, pw):
    flat = tuple(v for pair in reversed(pw) for v in pair)
    return torch.nn.functional.pad(x, flat)

kernel_name = 'interleave_layers__sliding_local_dilated__moe_arch_mod__prompt'

def setup_inputs():

    _w = (lambda a: (lambda t: t.to(torch.bfloat16) if t.is_floating_point() else t)(torch.as_tensor(a)))
    def _A_setup():
        B, S, D = 1, 2048, 576
        return {
            "x": _w((lambda *s: torch.randn(*s, dtype=torch.bfloat16) * 0.05)(B, S, D)),
            "wqkv": _w((lambda *s: torch.randn(*s, dtype=torch.bfloat16) * 0.05)(D, 3 * D)),
            "wo": _w((lambda *s: torch.randn(*s, dtype=torch.bfloat16) * 0.05)(D, D)),
        }
    _A_in = _A_setup()

    def _B_setup():
        B, S, D, F, C = 1, 2048, 576, 4608, 1
        return {
            "x": _w((lambda *s: torch.randn(*s, dtype=torch.bfloat16) * 0.05)(B, S, D)),
            "w_r": _w((lambda *s: torch.randn(*s, dtype=torch.bfloat16) * 0.05)(D)),
            "w_gate": _w((lambda *s: torch.randn(*s, dtype=torch.bfloat16) * 0.05)(D, F)),
            "w_up": _w((lambda *s: torch.randn(*s, dtype=torch.bfloat16) * 0.05)(D, F)),
            "w_down": _w((lambda *s: torch.randn(*s, dtype=torch.bfloat16) * 0.05)(F, D)),
        }
    _B_in = _B_setup()

    return {
        "A_x": _A_in["x"],
        "A_wqkv": _A_in["wqkv"],
        "A_wo": _A_in["wo"],
        "B_x": _B_in["x"],
        "B_w_r": _B_in["w_r"],
        "B_w_gate": _B_in["w_gate"],
        "B_w_up": _B_in["w_up"],
        "B_w_down": _B_in["w_down"],
    }

def reference(A_x, A_wqkv, A_wo, B_x, B_w_r, B_w_gate, B_w_up, B_w_down):
    def _A_ref(x, wqkv, wo):
        B, S, D = x.shape
        H = 32; Dh = D // H
        W, dil = 8, 2
        qkv = (lambda x, *d: x.permute(*d))((x @ wqkv).reshape(B, S, 3, H, Dh), 2, 0, 3, 1, 4)
        q, k, v = qkv[0], qkv[1], qkv[2]
        scale = Dh ** -0.5
        pos = torch.arange(S)
        off = torch.arange(W) * dil
        idx = pos[:, None] - off[None, :]
        mask = idx >= 0
        idx_c = torch.where(mask, idx, 0).reshape(-1)
        kg = k[:, :, idx_c, :].reshape(B, H, S, W, Dh)
        vg = v[:, :, idx_c, :].reshape(B, H, S, W, Dh)
        logits = torch.einsum("bhsd,bhswd->bhsw", q, kg) * scale
        logits = torch.where(mask[None, None, :, :], logits, -1e9)
        attn = _softmax(logits, axis=-1)
        o = torch.einsum("bhsw,bhswd->bhsd", attn, vg)
        return (lambda x, *d: x.permute(*d))(o, 0, 2, 1, 3).reshape(B, S, D) @ wo

    def _B_ref(x, w_r, w_gate, w_up, w_down):
        B, S, D = x.shape
        C = 1
        r = x @ w_r
        idx = torch.argsort(-r, axis=-1)[:, :C]
        idx_e = (lambda x, n, axis: torch.repeat_interleave(x, n, dim=axis))(idx.reshape(B, C, 1), D, axis=2)
        x_sel = (lambda x, i, axis: torch.take_along_dim(x, i, dim=axis))(x, idx_e, axis=1)
        g = _silu(x_sel @ w_gate)
        u = x_sel @ w_up
        y = (g * u) @ w_down
        r_sel = _sigmoid((lambda x, i, axis: torch.take_along_dim(x, i, dim=axis))(r, idx, axis=1))
        y = y * r_sel.reshape(B, C, 1)
        b_off = (torch.arange(B) * S).reshape(B, 1)
        flat_idx = (idx + b_off).reshape(B * C)
        zero = (x * 0.0).reshape(B * S, D)
        upd = _scatter_add(zero, flat_idx, y.reshape(B * C, D), axis=0)
        return x + upd.reshape(B, S, D)

    x = A_x
    x = x + _A_ref(_layernorm(x, axis=-1), A_wqkv, A_wo)
    x = x + _B_ref(_layernorm(x, axis=-1), B_w_r, B_w_gate, B_w_up, B_w_down)
    x = x + _A_ref(_layernorm(x, axis=-1), A_wqkv, A_wo)
    x = x + _B_ref(_layernorm(x, axis=-1), B_w_r, B_w_gate, B_w_up, B_w_down)
    return x
```

```cpp
#include <hip/hip_runtime.h>
#include <stdint.h>

#define WREP 8
#define FST 32
#define XFS 32
#define NTD " nt"
#define GWM 0xAA
#define GN1 (18 - __builtin_popcount(GWM))
#define XCI 0x04208760u
#define XCN 0x02221110u
#define KDEL 0x1c0000u
#define PFA 3

#if defined(__HIP_DEVICE_COMPILE__)
#define NOPK __attribute__((target("no-packed-fp32-ops")))
#else
#define NOPK
#endif
#define DEV __device__ __forceinline__ NOPK
typedef unsigned short u16;
typedef unsigned int u32;
typedef unsigned long long u64;
#define __uint_as_float(x) __builtin_bit_cast(float, (u32)(x))
#define __float_as_uint(x) __builtin_bit_cast(u32, (float)(x))
#define fmaxf(a, b) __builtin_fmaxf((a), (b))
typedef __attribute__((ext_vector_type(2))) float f32x2;
typedef __attribute__((ext_vector_type(2))) __bf16 bf16x2;
typedef __attribute__((ext_vector_type(2))) u32 u32x2;
typedef __attribute__((ext_vector_type(4))) u32 u32x4;

extern __shared__ __attribute__((aligned(16))) char smem[];

#define SPIN_MAX 20000
#define LSPIN_MAX 400000

#define WS_BYTES 0x1400000
#define OFF_EPOCH 0
#define OFF_TMO 256
#define OFF_FLAG 0xF0000
#define OFF_GFLAG 4096
#define OFF_UV 8192
#define OFF_KEY 16384
#define OFF_KEYE 0x220000
#define OFF_YF2 0x230000
#define OFF_XFLAG 0x18000
#define OFF_XF2 0x238000
#define OFF_X2R 81920
#define OFF_STATS 0x20000
#define STATS_B 0x40000
#define OFF_CROW 0xA0000
#define OFF_CROW2 0xDB0000
#define OFF_YP 0x100000
#define OFF_YP2 0xE00000
#define YP_B 589824
#define OFF_OT 0x240000
#define OTS 20480
#define TILES_B 2621440
#define OFF_X1G 0x740000
#define OFF_HX 0x1000000
#define OFF_WI 0x980000
#define OFF_WOI 0xD00000
#define OOB 0x7FFFFF00
#define TBIAS 0x7FFF0000u

#define LDS_BYTES 163840
#define XCH 10240
#define L_KV 0
#define KVS 304
#define L_Q 24320
#define L_WOE 33536
#define L_SCR 18432
#define L_G 98048
#define L_U 125696
#define L_MISC 153344
#define L_MR (L_MISC)
#define L_BC (L_MISC + 640)
#define BC_TF 0
#define BC_WRS 4
#define BC_C2 8
#define BC_PK 12
#define BC_GC 16
#define BC_C1 20
#define BC_PC 24
#define BC_GO 28
#define BC_PST 32
#define BC_FIN 36
#define BC_WIN 40
#define BC_NIL 72
#define BC_XC 44
#define BC_ZERO 48
#define BC_WC 56
#define BC_G0 64
#define BC_FC 76
#define BC_HF 80
#define BC_MC 96
#define BC_PM 104
#define BC_CD 112
#define BC_CT 128
#define L_WR (L_MISC + 9888)
#define L_VAR (L_WR + 340)
#define L_CS (L_MISC + 928)
#define L_HROW (L_MISC + 1824)
#define L_XROW (L_MISC + 2976)
#define L_XR (L_MISC + 4128)
#define L_SCR3 (L_MISC + 5280)
#define L_MX (L_MISC + 8192)
#define L_UV (L_MISC + 9216)
#define L_FP L_HROW

#define SENT 0xFFFFFFFFu

DEV void bar() {
    asm volatile("s_waitcnt lgkmcnt(0)" ::: "memory");
    __builtin_amdgcn_s_barrier();
    asm volatile("" ::: "memory");
}
#define VMWAIT(n) asm volatile("s_waitcnt vmcnt(" #n ")" ::: "memory")
DEV void vmwaitn(const int n) { asm volatile("s_waitcnt vmcnt(%0)" :: "n"(n) : "memory"); }
DEV void vmwait8n(u32 (&d)[8], const int n) {
    asm volatile("s_waitcnt vmcnt(%8)" : "+v"(d[0]), "+v"(d[1]), "+v"(d[2]), "+v"(d[3]), "+v"(d[4]), "+v"(d[5]), "+v"(d[6]), "+v"(d[7]) : "n"(n) : "memory");
}
DEV u32x4 mk_srd(const void* p, u32 bytes) {
    u64 a = (u64)p;
    asm volatile("" : "+s"(a));
    return (u32x4){(u32)a, (u32)(a >> 32) & 0xffffu, bytes, 0x00020000u};
}
DEV void dma16g(u32x4 srd, u32 voff, u32 soff, u32 lds_dst) {
    asm volatile("s_mov_b32 m0, %0\n\ts_nop 3\n\tbuffer_load_dwordx4 %1, %2, %3 offen lds" :: "s"(lds_dst), "v"(voff), "s"(srd), "s"(soff) : "memory");
}
DEV void dma16x4_sc1(u32x4 srd, u32 voff, u32 soff, u32 lds_dst) {
    asm volatile("s_mov_b32 m0, %0\n\ts_nop 3\n\tbuffer_load_dwordx4 %1, %2, %3 offen sc1 lds\n\tbuffer_load_dwordx4 %1, %2, %3 offen offset:1024 sc1 lds\n\t"
                 "buffer_load_dwordx4 %1, %2, %3 offen offset:2048 sc1 lds\n\tbuffer_load_dwordx4 %1, %2, %3 offen offset:3072 sc1 lds"
                 :: "s"(lds_dst), "v"(voff), "s"(srd), "s"(soff) : "memory");
}
DEV void bld32(u32& d, u32x4 srd, u32 voff, u32 soff, const int imm) {
    asm volatile("s_nop 4\n\tbuffer_load_dword %0, %1, %2, %3 offen offset:%4" : "=v"(d) : "v"(voff), "s"(srd), "s"(soff), "n"(imm));
}
DEV void old8(u32 (&d)[8], u32x4 srd, u32 voff, u32 s2) {
    asm volatile("s_nop 4\n\tbuffer_load_dword %0, %8, %9, 0 offen nt\n\tbuffer_load_dword %1, %8, %9, 0 offen offset:1152 nt\n\t"
                 "buffer_load_dword %2, %8, %9, 0 offen offset:2304 nt\n\tbuffer_load_dword %3, %8, %9, 0 offen offset:3456 nt\n\t"
                 "buffer_load_dword %4, %8, %9, %10 offen nt\n\tbuffer_load_dword %5, %8, %9, %10 offen offset:1152 nt\n\t"
                 "buffer_load_dword %6, %8, %9, %10 offen offset:2304 nt\n\tbuffer_load_dword %7, %8, %9, %10 offen offset:3456 nt"
                 : "=&v"(d[0]), "=&v"(d[1]), "=&v"(d[2]), "=&v"(d[3]), "=&v"(d[4]), "=&v"(d[5]), "=&v"(d[6]), "=&v"(d[7])
                 : "v"(voff), "s"(srd), "s"(s2));
}
DEV void wld8p(u32x4 srd, u32 voff, u32 soff, int w) {
    u32 t;
    asm volatile("s_cmp_gt_u32 %4, 3\n\ts_cbranch_scc1 Lw8_%=\n\ts_setprio 3\n\ts_nop 1\n\t"
                 "buffer_load_dword v48, %1, %2, %3 offen nt\n\tbuffer_load_dword v49, %1, %2, %3 offen offset:3456 nt\n\ts_add_u32 %0, %3, 6912\n\t"
                 "buffer_load_dword v50, %1, %2, %0 offen nt\n\tbuffer_load_dword v51, %1, %2, %0 offen offset:3456 nt\n\ts_add_u32 %0, %3, 13824\n\t"
                 "buffer_load_dword v52, %1, %2, %0 offen nt\n\tbuffer_load_dword v53, %1, %2, %0 offen offset:3456 nt\n\ts_add_u32 %0, %3, 20736\n\t"
                 "buffer_load_dword v54, %1, %2, %0 offen nt\n\tbuffer_load_dword v55, %1, %2, %0 offen offset:3456 nt\n"
                 "Lw8_%=:"
                 : "=&s"(t) : "v"(voff), "s"(srd), "s"(soff), "s"(w) : "scc", "memory");
}
DEV void x_dma(u32x4 srd, u32 xv, u32 orm, u32 c, u32 n) {
    u32 z1, z2, z3, y0, y1, so, t;
    asm volatile("s_cmp_eq_u32 %8, 0\n\ts_cbranch_scc1 Lx1_%=\n\t"
                 "v_xor_b32 %2, 64, %9\n\tv_or_b32 %3, %11, %9\n\tv_add_u32 %0, 0x2000, %2\n\tv_add_u32 %1, 0x4000, %9\n\tv_or_b32 %4, %11, %0\n\tv_add_u32 %2, 0x6000, %2\n"
                 "Lx0_%=:\n\t"
                 "s_cmp_gt_u32 %7, 8\n\ts_cselect_b32 %6, 9, 0\n\ts_sub_u32 %7, %7, %6\n\ts_mul_i32 m0, %7, 10240\n\ts_lshl_b32 %5, %7, 7\n\t"
                 "s_mov_b32 exec_lo, 0xffff0000\n\tbuffer_load_dwordx4 %3, %10, %5 offen lds\n\ts_mov_b32 exec_lo, -1\n\t"
                 "buffer_load_dwordx4 %4, %10, %5 offen offset:1024 lds\n\tbuffer_load_dwordx4 %1, %10, %5 offen offset:2048 lds\n\tbuffer_load_dwordx4 %2, %10, %5 offen offset:3072 lds\n\t"
                 "s_add_u32 m0, m0, 4096\n\ts_add_u32 %5, %5, 36864\n\t"
                 "buffer_load_dwordx4 %9, %10, %5 offen lds\n\tbuffer_load_dwordx4 %0, %10, %5 offen offset:1024 lds\n\t"
                 "buffer_load_dwordx4 %1, %10, %5 offen offset:2048 lds\n\tbuffer_load_dwordx4 %2, %10, %5 offen offset:3072 lds\n\t"
                 "s_add_u32 m0, m0, 4096\n\ts_add_u32 %5, %5, 36864\n\t"
                 "buffer_load_dwordx4 %9, %10, %5 offen lds\n\tbuffer_load_dwordx4 %0, %10, %5 offen offset:1024 lds\n\t"
                 "s_add_u32 %7, %7, 1\n\ts_sub_u32 %8, %8, 1\n\ts_cmp_lg_u32 %8, 0\n\ts_cbranch_scc1 Lx0_%=\n"
                 "Lx1_%=:"
                 : "=&v"(z1), "=&v"(z2), "=&v"(z3), "=&v"(y0), "=&v"(y1), "=&s"(so), "=&s"(t), "+s"(c), "+s"(n) : "v"(xv), "s"(srd), "s"(orm) : "scc", "memory");
}
DEV void wunit_st(u32x4 srd, u32 ib, int w) {
    asm volatile("s_cmp_gt_u32 %2, 3\n\ts_cbranch_scc1 Lu_%=\n\ts_waitcnt vmcnt(0)\n\t"
                 "v_perm_b32 v56, v49, v48, %3\n\tv_perm_b32 v57, v51, v50, %3\n\tv_perm_b32 v58, v53, v52, %3\n\tv_perm_b32 v59, v55, v54, %3\n\t"
                 "buffer_store_dwordx4 v[56:59], %0, %1, 0 offen sc1\n\t"
                 "v_perm_b32 v60, v49, v48, %4\n\tv_perm_b32 v61, v51, v50, %4\n\tv_perm_b32 v62, v53, v52, %4\n\tv_perm_b32 v63, v55, v54, %4\n\t"
                 "buffer_store_dwordx4 v[60:63], %0, %1, 0 offen offset:1024 sc1\n"
                 "Lu_%=:" :: "v"(ib), "s"(srd), "s"(w), "s"(0x05040100u), "s"(0x07060302u) : "scc", "memory");
}
DEV u32 plo(u32 hi, u32 lo) { return __builtin_amdgcn_perm(hi, lo, 0x05040100u); }
DEV u32 phi(u32 hi, u32 lo) { return __builtin_amdgcn_perm(hi, lo, 0x07060302u); }
DEV void wfrag(const u32 (&d)[8], u32x4& f0, u32x4& f1) {
    f0 = (u32x4){plo(d[1], d[0]), plo(d[3], d[2]), plo(d[5], d[4]), plo(d[7], d[6])};
    f1 = (u32x4){phi(d[1], d[0]), phi(d[3], d[2]), phi(d[5], d[4]), phi(d[7], d[6])};
}
DEV u32 pk2(float a, float b) { f32x2 v = {a, b}; return __builtin_bit_cast(u32, __builtin_convertvector(v, bf16x2)); }
DEV float rbf(float a) { return (float)(__bf16)a; }
DEV float ln_rs(float var) { return __builtin_amdgcn_rcpf(rbf(__builtin_amdgcn_sqrtf(rbf(rbf(var) + 1e-5f)))); }
DEV float ln_m(float m) { return rbf(m); }
DEV u32x2 ln_next(float m, float var, float mb, float rs) {
    const float c = 1.f + rs;
    const float mb2 = ln_m(c * m - mb * rs);
    return (u32x2){__float_as_uint((mb * rs + mb2) * __builtin_amdgcn_rcpf(c)), __float_as_uint(c * ln_rs(c * c * var))};
}
DEV float lo16(u32 v) { return __uint_as_float(v << 16); }
DEV float hi16(u32 v) { return __uint_as_float(v & 0xffff0000u); }
DEV u32 san(u32 v) { return v < 0xFFFFFFFEu ? v : 0xFFFFFFFEu; }
DEV u32 umax2(u32 a, u32 b) { return a > b ? a : b; }
DEV float dot2(u32 a, u32 b, float c) {
    return __builtin_amdgcn_fdot2_f32_bf16(__builtin_bit_cast(bf16x2, a), __builtin_bit_cast(bf16x2, b), c, false);
}
DEV void gsum3(float (&t)[3]) {
    u32 a[3], c[3];
#pragma unroll
    for (int i = 0; i < 3; ++i) { a[i] = __float_as_uint(t[i]); c[i] = a[i]; }
    asm volatile("s_nop 1\n\tv_permlane16_swap_b32 %0, %3\n\tv_permlane16_swap_b32 %1, %4\n\tv_permlane16_swap_b32 %2, %5\n\t"
                 "v_add_f32 %0, %0, %3\n\tv_add_f32 %1, %1, %4\n\tv_add_f32 %2, %2, %5\n\t"
                 "v_mov_b32 %3, %0\n\tv_mov_b32 %4, %1\n\tv_mov_b32 %5, %2\n\t"
                 "v_permlane32_swap_b32 %0, %3\n\tv_permlane32_swap_b32 %1, %4\n\tv_permlane32_swap_b32 %2, %5\n\t"
                 "v_add_f32 %0, %0, %3\n\tv_add_f32 %1, %1, %4\n\tv_add_f32 %2, %2, %5"
                 : "+v"(a[0]), "+v"(a[1]), "+v"(a[2]), "+v"(c[0]), "+v"(c[1]), "+v"(c[2]));
#pragma unroll
    for (int i = 0; i < 3; ++i) t[i] = __uint_as_float(a[i]);
}
DEV float gsum(float v) {
    u32 u = __float_as_uint(v);
    auto a = __builtin_amdgcn_permlane16_swap(u, u, false, false);
    v = __uint_as_float(a[0]) + __uint_as_float(a[1]);
    u = __float_as_uint(v);
    auto c = __builtin_amdgcn_permlane32_swap(u, u, false, false);
    return __uint_as_float(c[0]) + __uint_as_float(c[1]);
}
#define DPP(v, ctrl) ((u32)__builtin_amdgcn_update_dpp(0, (int)(v), (ctrl), 0xf, 0xf, false))
DEV u32 rmax(u32 v) {
    v = umax2(v, DPP(v, 0x128)); v = umax2(v, DPP(v, 0x124)); v = umax2(v, DPP(v, 0x122)); v = umax2(v, DPP(v, 0x121));
    return v;
}
DEV float rsum(float v) {
    v += __uint_as_float(DPP(__float_as_uint(v), 0x128)); v += __uint_as_float(DPP(__float_as_uint(v), 0x124));
    v += __uint_as_float(DPP(__float_as_uint(v), 0x122)); v += __uint_as_float(DPP(__float_as_uint(v), 0x121));
    return v;
}
DEV u32 lds32(int off) { return *(const u32*)(smem + off); }
typedef __attribute__((address_space(3))) volatile u32* lds_vu32;
DEV u32 lds32v(int off) { return *(lds_vu32)(smem + off); }
DEV u32x2 lds64(int off) { return *(const u32x2*)(smem + off); }
DEV u32x4 lds128(int off) { return *(const u32x4*)(smem + off); }
DEV void sts32(int off, u32 v) { *(u32*)(smem + off) = v; }
DEV void sts32v(int off, u32 v) { *(lds_vu32)(smem + off) = v; }
DEV void sts64(int off, u32x2 v) { *(u32x2*)(smem + off) = v; }
DEV void sts128(int off, u32x4 v) { *(u32x4*)(smem + off) = v; }
DEV void lds_inc(int off, int lane) {
    (void)lane;
    asm volatile("s_mov_b64 exec, 1\n\tds_add_u32 %0, %1\n\ts_mov_b64 exec, -1" :: "v"(off), "v"(1u) : "memory");
}
DEV u32 lds_inc_rtn(int off) {
    u32 old;
    asm volatile("s_mov_b64 exec, 1\n\tds_add_rtn_u32 %0, %1, %2\n\ts_mov_b64 exec, -1\n\ts_waitcnt lgkmcnt(0)" : "=&v"(old) : "v"(off), "v"(1u) : "memory");
    return __builtin_amdgcn_readfirstlane(old);
}
#define BST128(v, off, soff) __builtin_amdgcn_raw_buffer_store_b128((v), rs, (off) + (soff), 0, 16)
#define BST64(v, off, soff) __builtin_amdgcn_raw_buffer_store_b64((v), rs, (off), (soff), 16)
#define BST32(v, off, soff) __builtin_amdgcn_raw_buffer_store_b32((v), rs, (off), (soff), 16)
#define CFENCE() asm volatile("" ::: "memory")
#define TMO(code) asm volatile("v_writelane_b32 v109, %0, 47" :: "n"(code))
#define LWAITS(off, target, code, slp) do { u32 t_, c_, f_; \
    asm volatile("s_mov_b32 %1, %5\nLl_%=:\n\tds_read_b32 %0, %3\n\ts_waitcnt lgkmcnt(0)\n\tv_readfirstlane_b32 %2, %0\n\ts_cmp_ge_u32 %2, %4\n\ts_cbranch_scc1 Ld_%=\n\t" \
                 slp "s_sub_u32 %1, %1, 1\n\ts_cmp_lg_u32 %1, 0\n\ts_cbranch_scc1 Ll_%=\n\tv_writelane_b32 v109, %6, 47\nLd_%=:" \
                 : "=&v"(t_), "=&s"(c_), "=&s"(f_) : "v"(off), "s"((u32)(target)), "n"(LSPIN_MAX), "n"(code) : "scc", "memory"); } while (0)
#define LWAIT(off, target, code) LWAITS(off, target, code, "s_sleep 1\n\t")
#define LWAIT0(off, target, code) LWAITS(off, target, code, "")

#define ONES2 0x3F803F80u
#define RG_OF(b) (((b) & 7) * 4 + (((b) >> 3) & 3))
#define CB_OF(b) ((b) >> 5)
#define IDS \
    int lane; asm volatile("v_mbcnt_lo_u32_b32 %0, -1, 0\n\tv_mbcnt_hi_u32_b32 %0, -1, %0" : "=v"(lane)); \
    int w = w_; asm volatile("" : "+s"(w)); \
    int b = b_; asm volatile("" : "+s"(b)); \
    const int rg = RG_OF(b), cb = CB_OF(b); \
    const int r16 = lane & 15, g = lane >> 4; \
    (void)rg; (void)cb; (void)r16; (void)g; (void)w;

#define CSL(w) ((w) == 3 ? 6 : ((w) == 6 ? 3 : (w)))
struct LaneC { int ka, kt, qa, qt, va, ta, l0; u32 av, tv0, ot; int sd, sstr; };
DEV LaneC lane_consts(int w_, int b_) {
    IDS
    LaneC c;
    const int rt4 = w & 3, pp = w >> 2;
    const int kvb = L_KV + 16 * rt4 * KVS + 72 * pp;
    const int qb = L_Q + (16 * rt4 + r16) * 144 + 72 * pp;
    const int trr = kvb + (4 * g + (r16 >> 2)) * KVS + 144;
    c.kt = kvb + r16 * KVS + 32; c.ka = kvb + r16 * KVS + g * 8;
    c.qa = qb + g * 8; c.qt = g == 2 ? qb + 32 : L_BC + BC_ZERO;
    c.va = trr + (r16 & 3) * 8; c.ta = trr + 32;
    c.l0 = 4096 + (8 * g + (r16 & 7)) * 16;
    c.av = lane * 16;
    c.tv0 = pp ? 7168 + g * 512 + r16 * 16 : 7168 + lane * 16;
    c.ot = 0;
    const int P = 4 * CSL(w) + g;
    c.sstr = P >= 18 ? 144 : KVS;
    c.sd = (P >= 18 ? (L_Q - 16 * 144 + (P - 18) * 16) : (L_KV + P * 16)) + r16 * c.sstr;
    c.sstr *= 16;
    return c;
}

struct MidS { u32x4 srd; u32 ob, st, tt, c0, wi, ld, wx, ldx, msk, scr, stat, x1, cr, skip, xf; };
DEV void lc_set(const LaneC& c) {
    asm volatile("v_mov_b32 v100, %0\n\t" "v_mov_b32 v101, %1\n\t" "v_mov_b32 v102, %2\n\t" "v_mov_b32 v103, %3\n\t" "v_mov_b32 v104, %4\n\t" "v_mov_b32 v105, %5\n\t" "v_mov_b32 v106, %6\n\t" "v_mov_b32 v107, %7\n\t" "v_mov_b32 v108, %8\n\t" "v_mov_b32 v110, %10\n\t" "v_mov_b32 v111, %11\n\t"
        :: "v"(c.ka), "v"(c.kt), "v"(c.qa), "v"(c.qt), "v"(c.va), "v"(c.ta), "v"(c.l0), "v"(c.av), "v"(c.tv0), "v"(c.ot), "v"(c.sd), "v"(c.sstr));
}
DEV void xt_get(u32 (&x)[10]) {
    asm volatile("v_mov_b32 %0, v88\n\t" "v_mov_b32 %1, v89\n\t" "v_mov_b32 %2, v90\n\t" "v_mov_b32 %3, v91\n\t" "v_mov_b32 %4, v92\n\t" "v_mov_b32 %5, v93\n\t" "v_mov_b32 %6, v94\n\t" "v_mov_b32 %7, v95\n\t" "v_mov_b32 %8, v96\n\t" "v_mov_b32 %9, v97\n\t"
        : "=v"(x[0]), "=v"(x[1]), "=v"(x[2]), "=v"(x[3]), "=v"(x[4]), "=v"(x[5]), "=v"(x[6]), "=v"(x[7]), "=v"(x[8]), "=v"(x[9]));
}
DEV void xt_set(const u32 (&x)[10]) {
    asm volatile("v_mov_b32 v88, %0\n\t" "v_mov_b32 v89, %1\n\t" "v_mov_b32 v90, %2\n\t" "v_mov_b32 v91, %3\n\t" "v_mov_b32 v92, %4\n\t" "v_mov_b32 v93, %5\n\t" "v_mov_b32 v94, %6\n\t" "v_mov_b32 v95, %7\n\t" "v_mov_b32 v96, %8\n\t" "v_mov_b32 v97, %9\n\t"
        :: "v"(x[0]), "v"(x[1]), "v"(x[2]), "v"(x[3]), "v"(x[4]), "v"(x[5]), "v"(x[6]), "v"(x[7]), "v"(x[8]), "v"(x[9]));
}

DEV void gemm0(u32x4 srd, int xa, u32 iv, u32 si, int (&xt)[3], u32 fo, u32 fexp, u32 cs, u64 va, u64 v0, u32& cn) {
    int xb; u32 fl, sf;
    asm volatile(
        "s_nop 4\n\t"
        "s_setprio 2\n\t"
        "buffer_load_dwordx4 v[112:115], %[iv], %[ssrd], %[si] offen offset:0 sc1\n\t"
        "buffer_load_dwordx4 v[116:119], %[iv], %[ssrd], %[si] offen offset:1024 sc1\n\t"
        "buffer_load_dwordx4 v[120:123], %[iv], %[ssrd], %[si] offen offset:2048 sc1\n\t"
        "buffer_load_dwordx4 v[124:127], %[iv], %[ssrd], %[si] offen offset:3072 sc1\n\t"
        "s_add_u32 %[si], %[si], 4096\n\t"
        "buffer_load_dwordx4 v[128:131], %[iv], %[ssrd], %[si] offen offset:0 sc1\n\t"
        "buffer_load_dwordx4 v[132:135], %[iv], %[ssrd], %[si] offen offset:1024 sc1\n\t"
        "buffer_load_dwordx4 v[136:139], %[iv], %[ssrd], %[si] offen offset:2048 sc1\n\t"
        "buffer_load_dwordx4 v[140:143], %[iv], %[ssrd], %[si] offen offset:3072 sc1\n\t"
        "s_add_u32 %[si], %[si], 4096\n\t"
        "buffer_load_dwordx4 v[144:147], %[iv], %[ssrd], %[si] offen offset:0 sc1\n\t"
        "buffer_load_dwordx4 v[148:151], %[iv], %[ssrd], %[si] offen offset:1024 sc1\n\t"
        "ds_read_b128 v[88:91], %[x0]\n\t"
        "ds_read_b128 v[92:95], %[x1]\n\t"
        "ds_read_b64 v[96:97], %[x2]\n\t"
        "v_xor_b32 %[xb], 64, %[xa]\n\t"
        "ds_read_b128 v[24:27], %[xa] offset:0\n\t"
        "ds_read_b128 v[28:31], %[xa] offset:2048\n\t"
        "ds_read_b128 v[32:35], %[xa] offset:4096\n\t"
        "ds_read_b128 v[36:39], %[xa] offset:6144\n\t"
        "ds_read_b128 v[40:43], %[xa] offset:8192\n\t"
        "ds_read_b128 v[44:47], %[xb] offset:0\n\t"
        "s_waitcnt vmcnt(8) lgkmcnt(5)\n\t"
        "v_mfma_f32_16x16x32_bf16 v[48:51], v[112:115], v[24:27], 0\n\t"
        "v_mfma_f32_16x16x32_bf16 v[68:71], v[116:119], v[24:27], 0\n\t"
        "ds_read_b128 v[24:27], %[xb] offset:2048\n\t"
        "s_waitcnt lgkmcnt(5)\n\t"
        "v_mfma_f32_16x16x32_bf16 v[52:55], v[112:115], v[28:31], 0\n\t"
        "v_mfma_f32_16x16x32_bf16 v[72:75], v[116:119], v[28:31], 0\n\t"
        "buffer_load_dwordx4 v[152:155], %[iv], %[ssrd], %[si] offen offset:2048 sc1\n\t"
        "buffer_load_dwordx4 v[156:159], %[iv], %[ssrd], %[si] offen offset:3072 sc1\n\t"
        "ds_read_b128 v[28:31], %[xb] offset:4096\n\t"
        "s_waitcnt lgkmcnt(5)\n\t"
        "v_mfma_f32_16x16x32_bf16 v[56:59], v[112:115], v[32:35], 0\n\t"
        "v_mfma_f32_16x16x32_bf16 v[76:79], v[116:119], v[32:35], 0\n\t"
        "ds_read_b128 v[32:35], %[xb] offset:6144\n\t"
        "s_waitcnt lgkmcnt(5)\n\t"
        "v_mfma_f32_16x16x32_bf16 v[60:63], v[112:115], v[36:39], 0\n\t"
        "v_mfma_f32_16x16x32_bf16 v[80:83], v[116:119], v[36:39], 0\n\t"
        "ds_read_b128 v[36:39], %[xb] offset:8192\n\t"
        "s_waitcnt lgkmcnt(5)\n\t"
        "v_mfma_f32_16x16x32_bf16 v[64:67], v[112:115], v[40:43], 0\n\t"
        "v_mfma_f32_16x16x32_bf16 v[84:87], v[116:119], v[40:43], 0\n\t"
        "ds_read_b128 v[40:43], %[xa] offset:10240\n\t"
        "s_waitcnt vmcnt(8) lgkmcnt(5)\n\t"
        "v_mfma_f32_16x16x32_bf16 v[48:51], v[120:123], v[44:47], v[48:51]\n\t"
        "v_mfma_f32_16x16x32_bf16 v[68:71], v[124:127], v[44:47], v[68:71]\n\t"
        "ds_read_b128 v[44:47], %[xa] offset:12288\n\t"
        "s_waitcnt lgkmcnt(5)\n\t"
        "v_mfma_f32_16x16x32_bf16 v[52:55], v[120:123], v[24:27], v[52:55]\n\t"
        "v_mfma_f32_16x16x32_bf16 v[72:75], v[124:127], v[24:27], v[72:75]\n\t"
        "s_add_u32 %[si], %[si], 4096\n\t"
        "buffer_load_dwordx4 v[160:163], %[iv], %[ssrd], %[si] offen offset:0 sc1\n\t"
        "buffer_load_dwordx4 v[164:167], %[iv], %[ssrd], %[si] offen offset:1024 sc1\n\t"
        "ds_read_b128 v[24:27], %[xa] offset:14336\n\t"
        "s_waitcnt lgkmcnt(5)\n\t"
        "v_mfma_f32_16x16x32_bf16 v[56:59], v[120:123], v[28:31], v[56:59]\n\t"
        "v_mfma_f32_16x16x32_bf16 v[76:79], v[124:127], v[28:31], v[76:79]\n\t"
        "ds_read_b128 v[28:31], %[xa] offset:16384\n\t"
        "s_waitcnt lgkmcnt(5)\n\t"
        "v_mfma_f32_16x16x32_bf16 v[60:63], v[120:123], v[32:35], v[60:63]\n\t"
        "v_mfma_f32_16x16x32_bf16 v[80:83], v[124:127], v[32:35], v[80:83]\n\t"
        "ds_read_b128 v[32:35], %[xa] offset:18432\n\t"
        "s_waitcnt lgkmcnt(5)\n\t"
        "v_mfma_f32_16x16x32_bf16 v[64:67], v[120:123], v[36:39], v[64:67]\n\t"
        "v_mfma_f32_16x16x32_bf16 v[84:87], v[124:127], v[36:39], v[84:87]\n\t"
        "ds_read_b128 v[36:39], %[xb] offset:10240\n\t"
        "s_waitcnt vmcnt(8) lgkmcnt(5)\n\t"
        "v_mfma_f32_16x16x32_bf16 v[48:51], v[128:131], v[40:43], v[48:51]\n\t"
        "v_mfma_f32_16x16x32_bf16 v[68:71], v[132:135], v[40:43], v[68:71]\n\t"
        "ds_read_b128 v[40:43], %[xb] offset:12288\n\t"
        "s_waitcnt lgkmcnt(5)\n\t"
        "v_mfma_f32_16x16x32_bf16 v[52:55], v[128:131], v[44:47], v[52:55]\n\t"
        "v_mfma_f32_16x16x32_bf16 v[72:75], v[132:135], v[44:47], v[72:75]\n\t"
        "buffer_load_dwordx4 v[168:171], %[iv], %[ssrd], %[si] offen offset:2048 sc1\n\t"
        "buffer_load_dwordx4 v[172:175], %[iv], %[ssrd], %[si] offen offset:3072 sc1\n\t"
        "ds_read_b128 v[44:47], %[xb] offset:14336\n\t"
        "s_waitcnt lgkmcnt(5)\n\t"
        "v_mfma_f32_16x16x32_bf16 v[56:59], v[128:131], v[24:27], v[56:59]\n\t"
        "v_mfma_f32_16x16x32_bf16 v[76:79], v[132:135], v[24:27], v[76:79]\n\t"
        "ds_read_b128 v[24:27], %[xb] offset:16384\n\t"
        "s_waitcnt lgkmcnt(5)\n\t"
        "v_mfma_f32_16x16x32_bf16 v[60:63], v[128:131], v[28:31], v[60:63]\n\t"
        "v_mfma_f32_16x16x32_bf16 v[80:83], v[132:135], v[28:31], v[80:83]\n\t"
        "ds_read_b128 v[28:31], %[xb] offset:18432\n\t"
        "s_waitcnt lgkmcnt(5)\n\t"
        "v_mfma_f32_16x16x32_bf16 v[64:67], v[128:131], v[32:35], v[64:67]\n\t"
        "v_mfma_f32_16x16x32_bf16 v[84:87], v[132:135], v[32:35], v[84:87]\n\t"
        "ds_read_b128 v[32:35], %[xa] offset:20480\n\t"
        "s_waitcnt vmcnt(8) lgkmcnt(5)\n\t"
        "v_mfma_f32_16x16x32_bf16 v[48:51], v[136:139], v[36:39], v[48:51]\n\t"
        "v_mfma_f32_16x16x32_bf16 v[68:71], v[140:143], v[36:39], v[68:71]\n\t"
        "ds_read_b128 v[36:39], %[xa] offset:22528\n\t"
        "s_waitcnt lgkmcnt(5)\n\t"
        "v_mfma_f32_16x16x32_bf16 v[52:55], v[136:139], v[40:43], v[52:55]\n\t"
        "v_mfma_f32_16x16x32_bf16 v[72:75], v[140:143], v[40:43], v[72:75]\n\t"
        "s_add_u32 %[si], %[si], 4096\n\t"
        "buffer_load_dwordx4 v[176:179], %[iv], %[ssrd], %[si] offen offset:0 sc1\n\t"
        "buffer_load_dwordx4 v[180:183], %[iv], %[ssrd], %[si] offen offset:1024 sc1\n\t"
        "ds_read_b128 v[40:43], %[xa] offset:24576\n\t"
        "s_waitcnt lgkmcnt(5)\n\t"
        "v_mfma_f32_16x16x32_bf16 v[56:59], v[136:139], v[44:47], v[56:59]\n\t"
        "v_mfma_f32_16x16x32_bf16 v[76:79], v[140:143], v[44:47], v[76:79]\n\t"
        "ds_read_b128 v[44:47], %[xa] offset:26624\n\t"
        "s_waitcnt lgkmcnt(5)\n\t"
        "v_mfma_f32_16x16x32_bf16 v[60:63], v[136:139], v[24:27], v[60:63]\n\t"
        "v_mfma_f32_16x16x32_bf16 v[80:83], v[140:143], v[24:27], v[80:83]\n\t"
        "ds_read_b128 v[24:27], %[xa] offset:28672\n\t"
        "s_waitcnt lgkmcnt(5)\n\t"
        "v_mfma_f32_16x16x32_bf16 v[64:67], v[136:139], v[28:31], v[64:67]\n\t"
        "v_mfma_f32_16x16x32_bf16 v[84:87], v[140:143], v[28:31], v[84:87]\n\t"
        "ds_read_b128 v[28:31], %[xb] offset:20480\n\t"
        "s_waitcnt vmcnt(8) lgkmcnt(5)\n\t"
        "v_mfma_f32_16x16x32_bf16 v[48:51], v[144:147], v[32:35], v[48:51]\n\t"
        "v_mfma_f32_16x16x32_bf16 v[68:71], v[148:151], v[32:35], v[68:71]\n\t"
        "ds_read_b128 v[32:35], %[xb] offset:22528\n\t"
        "s_waitcnt lgkmcnt(5)\n\t"
        "v_mfma_f32_16x16x32_bf16 v[52:55], v[144:147], v[36:39], v[52:55]\n\t"
        "v_mfma_f32_16x16x32_bf16 v[72:75], v[148:151], v[36:39], v[72:75]\n\t"
        "buffer_load_dwordx4 v[184:187], %[iv], %[ssrd], %[si] offen offset:2048 sc1\n\t"
        "buffer_load_dwordx4 v[188:191], %[iv], %[ssrd], %[si] offen offset:3072 sc1\n\t"
        "ds_read_b128 v[36:39], %[xb] offset:24576\n\t"
        "s_waitcnt lgkmcnt(5)\n\t"
        "v_mfma_f32_16x16x32_bf16 v[56:59], v[144:147], v[40:43], v[56:59]\n\t"
        "v_mfma_f32_16x16x32_bf16 v[76:79], v[148:151], v[40:43], v[76:79]\n\t"
        "ds_read_b128 v[40:43], %[xb] offset:26624\n\t"
        "s_waitcnt lgkmcnt(5)\n\t"
        "v_mfma_f32_16x16x32_bf16 v[60:63], v[144:147], v[44:47], v[60:63]\n\t"
        "v_mfma_f32_16x16x32_bf16 v[80:83], v[148:151], v[44:47], v[80:83]\n\t"
        "ds_read_b128 v[44:47], %[xb] offset:28672\n\t"
        "s_waitcnt lgkmcnt(5)\n\t"
        "v_mfma_f32_16x16x32_bf16 v[64:67], v[144:147], v[24:27], v[64:67]\n\t"
        "v_mfma_f32_16x16x32_bf16 v[84:87], v[148:151], v[24:27], v[84:87]\n\t"
        "ds_read_b128 v[24:27], %[xa] offset:30720\n\t"
        "s_waitcnt vmcnt(8) lgkmcnt(5)\n\t"
        "v_mfma_f32_16x16x32_bf16 v[48:51], v[152:155], v[28:31], v[48:51]\n\t"
        "v_mfma_f32_16x16x32_bf16 v[68:71], v[156:159], v[28:31], v[68:71]\n\t"
        "ds_read_b128 v[28:31], %[xa] offset:32768\n\t"
        "s_waitcnt lgkmcnt(5)\n\t"
        "v_mfma_f32_16x16x32_bf16 v[52:55], v[152:155], v[32:35], v[52:55]\n\t"
        "v_mfma_f32_16x16x32_bf16 v[72:75], v[156:159], v[32:35], v[72:75]\n\t"
        "s_add_u32 %[si], %[si], 4096\n\t"
        "buffer_load_dwordx4 v[192:195], %[iv], %[ssrd], %[si] offen offset:0 sc1\n\t"
        "buffer_load_dwordx4 v[196:199], %[iv], %[ssrd], %[si] offen offset:1024 sc1\n\t"
        "ds_read_b128 v[32:35], %[xa] offset:34816\n\t"
        "s_waitcnt lgkmcnt(5)\n\t"
        "v_mfma_f32_16x16x32_bf16 v[56:59], v[152:155], v[36:39], v[56:59]\n\t"
        "v_mfma_f32_16x16x32_bf16 v[76:79], v[156:159], v[36:39], v[76:79]\n\t"
        "ds_read_b128 v[36:39], %[xa] offset:36864\n\t"
        "s_waitcnt lgkmcnt(5)\n\t"
        "v_mfma_f32_16x16x32_bf16 v[60:63], v[152:155], v[40:43], v[60:63]\n\t"
        "v_mfma_f32_16x16x32_bf16 v[80:83], v[156:159], v[40:43], v[80:83]\n\t"
        "ds_read_b128 v[40:43], %[xa] offset:38912\n\t"
        "s_waitcnt lgkmcnt(5)\n\t"
        "v_mfma_f32_16x16x32_bf16 v[64:67], v[152:155], v[44:47], v[64:67]\n\t"
        "v_mfma_f32_16x16x32_bf16 v[84:87], v[156:159], v[44:47], v[84:87]\n\t"
        "ds_read_b128 v[44:47], %[xb] offset:30720\n\t"
        "s_setprio 1\n\t"
        "s_waitcnt vmcnt(8) lgkmcnt(5)\n\t"
        "v_mfma_f32_16x16x32_bf16 v[48:51], v[160:163], v[24:27], v[48:51]\n\t"
        "v_mfma_f32_16x16x32_bf16 v[68:71], v[164:167], v[24:27], v[68:71]\n\t"
        "ds_read_b128 v[24:27], %[xb] offset:32768\n\t"
        "s_waitcnt lgkmcnt(5)\n\t"
        "v_mfma_f32_16x16x32_bf16 v[52:55], v[160:163], v[28:31], v[52:55]\n\t"
        "v_mfma_f32_16x16x32_bf16 v[72:75], v[164:167], v[28:31], v[72:75]\n\t"
        "buffer_load_dwordx4 v[200:203], %[iv], %[ssrd], %[si] offen offset:2048 sc1\n\t"
        "buffer_load_dwordx4 v[204:207], %[iv], %[ssrd], %[si] offen offset:3072 sc1\n\t"
        "ds_read_b128 v[28:31], %[xb] offset:34816\n\t"
        "s_waitcnt lgkmcnt(5)\n\t"
        "v_mfma_f32_16x16x32_bf16 v[56:59], v[160:163], v[32:35], v[56:59]\n\t"
        "v_mfma_f32_16x16x32_bf16 v[76:79], v[164:167], v[32:35], v[76:79]\n\t"
        "ds_read_b128 v[32:35], %[xb] offset:36864\n\t"
        "s_waitcnt lgkmcnt(5)\n\t"
        "v_mfma_f32_16x16x32_bf16 v[60:63], v[160:163], v[36:39], v[60:63]\n\t"
        "v_mfma_f32_16x16x32_bf16 v[80:83], v[164:167], v[36:39], v[80:83]\n\t"
        "ds_read_b128 v[36:39], %[xb] offset:38912\n\t"
        "s_waitcnt lgkmcnt(5)\n\t"
        "v_mfma_f32_16x16x32_bf16 v[64:67], v[160:163], v[40:43], v[64:67]\n\t"
        "v_mfma_f32_16x16x32_bf16 v[84:87], v[164:167], v[40:43], v[84:87]\n\t"
        "ds_read_b128 v[40:43], %[xa] offset:40960\n\t"
        "v_mov_b32 %[x2], 153984\n\t"
        "v_mov_b32 %[x0], 1\n\t"
        "s_mov_b64 exec, 1\n\t"
        "ds_add_u32 %[x2], %[x0] offset:%[omc]+4\n\t"
        "s_mov_b64 exec, -1\n\t"
        "s_waitcnt vmcnt(8) lgkmcnt(6)\n\t"
        "v_mfma_f32_16x16x32_bf16 v[48:51], v[168:171], v[44:47], v[48:51]\n\t"
        "v_mfma_f32_16x16x32_bf16 v[68:71], v[172:175], v[44:47], v[68:71]\n\t"
        "ds_read_b128 v[44:47], %[xa] offset:43008\n\t"
        "s_waitcnt lgkmcnt(6)\n\t"
        "v_mfma_f32_16x16x32_bf16 v[52:55], v[168:171], v[24:27], v[52:55]\n\t"
        "v_mfma_f32_16x16x32_bf16 v[72:75], v[172:175], v[24:27], v[72:75]\n\t"
        "s_add_u32 %[si], %[si], 4096\n\t"
        "buffer_load_dwordx4 v[208:211], %[iv], %[ssrd], %[si] offen offset:0 sc1\n\t"
        "buffer_load_dwordx4 v[212:215], %[iv], %[ssrd], %[si] offen offset:1024 sc1\n\t"
        "ds_read_b128 v[24:27], %[xa] offset:45056\n\t"
        "s_waitcnt lgkmcnt(6)\n\t"
        "v_mfma_f32_16x16x32_bf16 v[56:59], v[168:171], v[28:31], v[56:59]\n\t"
        "v_mfma_f32_16x16x32_bf16 v[76:79], v[172:175], v[28:31], v[76:79]\n\t"
        "ds_read_b128 v[28:31], %[xa] offset:47104\n\t"
        "s_waitcnt lgkmcnt(6)\n\t"
        "v_mfma_f32_16x16x32_bf16 v[60:63], v[168:171], v[32:35], v[60:63]\n\t"
        "v_mfma_f32_16x16x32_bf16 v[80:83], v[172:175], v[32:35], v[80:83]\n\t"
        "ds_read_b128 v[32:35], %[xa] offset:49152\n\t"
        "s_waitcnt lgkmcnt(6)\n\t"
        "v_mfma_f32_16x16x32_bf16 v[64:67], v[168:171], v[36:39], v[64:67]\n\t"
        "v_mfma_f32_16x16x32_bf16 v[84:87], v[172:175], v[36:39], v[84:87]\n\t"
        "ds_read_b128 v[36:39], %[xb] offset:40960\n\t"
        "s_waitcnt vmcnt(8) lgkmcnt(6)\n\t"
        "v_mfma_f32_16x16x32_bf16 v[48:51], v[176:179], v[40:43], v[48:51]\n\t"
        "v_mfma_f32_16x16x32_bf16 v[68:71], v[180:183], v[40:43], v[68:71]\n\t"
        "ds_read_b128 v[40:43], %[xb] offset:43008\n\t"
        "s_waitcnt lgkmcnt(5)\n\t"
        "v_mfma_f32_16x16x32_bf16 v[52:55], v[176:179], v[44:47], v[52:55]\n\t"
        "v_mfma_f32_16x16x32_bf16 v[72:75], v[180:183], v[44:47], v[72:75]\n\t"
        "buffer_load_dwordx4 v[216:219], %[iv], %[ssrd], %[si] offen offset:2048 sc1\n\t"
        "buffer_load_dwordx4 v[220:223], %[iv], %[ssrd], %[si] offen offset:3072 sc1\n\t"
        "ds_read_b128 v[44:47], %[xb] offset:45056\n\t"
        "s_waitcnt lgkmcnt(5)\n\t"
        "v_mfma_f32_16x16x32_bf16 v[56:59], v[176:179], v[24:27], v[56:59]\n\t"
        "v_mfma_f32_16x16x32_bf16 v[76:79], v[180:183], v[24:27], v[76:79]\n\t"
        "ds_read_b128 v[24:27], %[xb] offset:47104\n\t"
        "s_waitcnt lgkmcnt(5)\n\t"
        "v_mfma_f32_16x16x32_bf16 v[60:63], v[176:179], v[28:31], v[60:63]\n\t"
        "v_mfma_f32_16x16x32_bf16 v[80:83], v[180:183], v[28:31], v[80:83]\n\t"
        "ds_read_b128 v[28:31], %[xb] offset:49152\n\t"
        "s_waitcnt lgkmcnt(5)\n\t"
        "v_mfma_f32_16x16x32_bf16 v[64:67], v[176:179], v[32:35], v[64:67]\n\t"
        "v_mfma_f32_16x16x32_bf16 v[84:87], v[180:183], v[32:35], v[84:87]\n\t"
        "v_add_u32 %[xa], 51200, %[xa]\n\t"
        "ds_read_b128 v[32:35], %[xa] offset:0\n\t"
        "s_waitcnt vmcnt(8) lgkmcnt(5)\n\t"
        "v_mfma_f32_16x16x32_bf16 v[48:51], v[184:187], v[36:39], v[48:51]\n\t"
        "v_mfma_f32_16x16x32_bf16 v[68:71], v[188:191], v[36:39], v[68:71]\n\t"
        "ds_read_b128 v[36:39], %[xa] offset:2048\n\t"
        "s_waitcnt lgkmcnt(5)\n\t"
        "v_mfma_f32_16x16x32_bf16 v[52:55], v[184:187], v[40:43], v[52:55]\n\t"
        "v_mfma_f32_16x16x32_bf16 v[72:75], v[188:191], v[40:43], v[72:75]\n\t"
        "s_add_u32 %[si], %[si], 4096\n\t"
        "buffer_load_dwordx4 v[224:227], %[iv], %[ssrd], %[si] offen offset:0 sc1\n\t"
        "buffer_load_dwordx4 v[228:231], %[iv], %[ssrd], %[si] offen offset:1024 sc1\n\t"
        "ds_read_b128 v[40:43], %[xa] offset:4096\n\t"
        "s_waitcnt lgkmcnt(5)\n\t"
        "v_mfma_f32_16x16x32_bf16 v[56:59], v[184:187], v[44:47], v[56:59]\n\t"
        "v_mfma_f32_16x16x32_bf16 v[76:79], v[188:191], v[44:47], v[76:79]\n\t"
        "ds_read_b128 v[44:47], %[xa] offset:6144\n\t"
        "s_waitcnt lgkmcnt(5)\n\t"
        "v_mfma_f32_16x16x32_bf16 v[60:63], v[184:187], v[24:27], v[60:63]\n\t"
        "v_mfma_f32_16x16x32_bf16 v[80:83], v[188:191], v[24:27], v[80:83]\n\t"
        "ds_read_b128 v[24:27], %[xa] offset:8192\n\t"
        "s_waitcnt lgkmcnt(5)\n\t"
        "v_mfma_f32_16x16x32_bf16 v[64:67], v[184:187], v[28:31], v[64:67]\n\t"
        "v_mfma_f32_16x16x32_bf16 v[84:87], v[188:191], v[28:31], v[84:87]\n\t"
        "v_add_u32 %[xb], 51200, %[xb]\n\t"
        "ds_read_b128 v[28:31], %[xb] offset:0\n\t"
        "s_waitcnt vmcnt(8) lgkmcnt(5)\n\t"
        "v_mfma_f32_16x16x32_bf16 v[48:51], v[192:195], v[32:35], v[48:51]\n\t"
        "v_mfma_f32_16x16x32_bf16 v[68:71], v[196:199], v[32:35], v[68:71]\n\t"
        "ds_read_b128 v[32:35], %[xb] offset:2048\n\t"
        "s_waitcnt lgkmcnt(5)\n\t"
        "v_mfma_f32_16x16x32_bf16 v[52:55], v[192:195], v[36:39], v[52:55]\n\t"
        "v_mfma_f32_16x16x32_bf16 v[72:75], v[196:199], v[36:39], v[72:75]\n\t"
        "buffer_load_dwordx4 v[232:235], %[iv], %[ssrd], %[si] offen offset:2048 sc1\n\t"
        "buffer_load_dwordx4 v[236:239], %[iv], %[ssrd], %[si] offen offset:3072 sc1\n\t"
        "ds_read_b128 v[36:39], %[xb] offset:4096\n\t"
        "s_waitcnt lgkmcnt(5)\n\t"
        "v_mfma_f32_16x16x32_bf16 v[56:59], v[192:195], v[40:43], v[56:59]\n\t"
        "v_mfma_f32_16x16x32_bf16 v[76:79], v[196:199], v[40:43], v[76:79]\n\t"
        "ds_read_b128 v[40:43], %[xb] offset:6144\n\t"
        "s_waitcnt lgkmcnt(5)\n\t"
        "v_mfma_f32_16x16x32_bf16 v[60:63], v[192:195], v[44:47], v[60:63]\n\t"
        "v_mfma_f32_16x16x32_bf16 v[80:83], v[196:199], v[44:47], v[80:83]\n\t"
        "ds_read_b128 v[44:47], %[xb] offset:8192\n\t"
        "s_waitcnt lgkmcnt(5)\n\t"
        "v_mfma_f32_16x16x32_bf16 v[64:67], v[192:195], v[24:27], v[64:67]\n\t"
        "v_mfma_f32_16x16x32_bf16 v[84:87], v[196:199], v[24:27], v[84:87]\n\t"
        "ds_read_b128 v[24:27], %[xa] offset:10240\n\t"
        "s_waitcnt vmcnt(8) lgkmcnt(5)\n\t"
        "v_mfma_f32_16x16x32_bf16 v[48:51], v[200:203], v[28:31], v[48:51]\n\t"
        "v_mfma_f32_16x16x32_bf16 v[68:71], v[204:207], v[28:31], v[68:71]\n\t"
        "ds_read_b128 v[28:31], %[xa] offset:12288\n\t"
        "s_waitcnt lgkmcnt(5)\n\t"
        "v_mfma_f32_16x16x32_bf16 v[52:55], v[200:203], v[32:35], v[52:55]\n\t"
        "v_mfma_f32_16x16x32_bf16 v[72:75], v[204:207], v[32:35], v[72:75]\n\t"
        "s_add_u32 %[si], %[si], 4096\n\t"
        "buffer_load_dwordx4 v[240:243], %[iv], %[ssrd], %[si] offen offset:0 sc1\n\t"
        "buffer_load_dwordx4 v[244:247], %[iv], %[ssrd], %[si] offen offset:1024 sc1\n\t"
        "ds_read_b128 v[32:35], %[xa] offset:14336\n\t"
        "s_waitcnt lgkmcnt(5)\n\t"
        "v_mfma_f32_16x16x32_bf16 v[56:59], v[200:203], v[36:39], v[56:59]\n\t"
        "v_mfma_f32_16x16x32_bf16 v[76:79], v[204:207], v[36:39], v[76:79]\n\t"
        "ds_read_b128 v[36:39], %[xa] offset:16384\n\t"
        "s_waitcnt lgkmcnt(5)\n\t"
        "v_mfma_f32_16x16x32_bf16 v[60:63], v[200:203], v[40:43], v[60:63]\n\t"
        "v_mfma_f32_16x16x32_bf16 v[80:83], v[204:207], v[40:43], v[80:83]\n\t"
        "ds_read_b128 v[40:43], %[xa] offset:18432\n\t"
        "s_waitcnt lgkmcnt(5)\n\t"
        "v_mfma_f32_16x16x32_bf16 v[64:67], v[200:203], v[44:47], v[64:67]\n\t"
        "v_mfma_f32_16x16x32_bf16 v[84:87], v[204:207], v[44:47], v[84:87]\n\t"
        "ds_read_b128 v[44:47], %[xb] offset:10240\n\t"
        "s_setprio 0\n\t"
        "s_waitcnt vmcnt(8) lgkmcnt(5)\n\t"
        "v_mfma_f32_16x16x32_bf16 v[48:51], v[208:211], v[24:27], v[48:51]\n\t"
        "v_mfma_f32_16x16x32_bf16 v[68:71], v[212:215], v[24:27], v[68:71]\n\t"
        "ds_read_b128 v[24:27], %[xb] offset:12288\n\t"
        "s_waitcnt lgkmcnt(5)\n\t"
        "v_mfma_f32_16x16x32_bf16 v[52:55], v[208:211], v[28:31], v[52:55]\n\t"
        "v_mfma_f32_16x16x32_bf16 v[72:75], v[212:215], v[28:31], v[72:75]\n\t"
        "buffer_load_dwordx4 v[248:251], %[iv], %[ssrd], %[si] offen offset:2048 sc1\n\t"
        "buffer_load_dwordx4 v[252:255], %[iv], %[ssrd], %[si] offen offset:3072 sc1\n\t"
        "ds_read_b128 v[28:31], %[xb] offset:14336\n\t"
        "s_waitcnt lgkmcnt(5)\n\t"
        "v_mfma_f32_16x16x32_bf16 v[56:59], v[208:211], v[32:35], v[56:59]\n\t"
        "v_mfma_f32_16x16x32_bf16 v[76:79], v[212:215], v[32:35], v[76:79]\n\t"
        "ds_read_b128 v[32:35], %[xb] offset:16384\n\t"
        "s_waitcnt lgkmcnt(5)\n\t"
        "v_mfma_f32_16x16x32_bf16 v[60:63], v[208:211], v[36:39], v[60:63]\n\t"
        "v_mfma_f32_16x16x32_bf16 v[80:83], v[212:215], v[36:39], v[80:83]\n\t"
        "ds_read_b128 v[36:39], %[xb] offset:18432\n\t"
        "s_waitcnt lgkmcnt(5)\n\t"
        "v_mfma_f32_16x16x32_bf16 v[64:67], v[208:211], v[40:43], v[64:67]\n\t"
        "v_mfma_f32_16x16x32_bf16 v[84:87], v[212:215], v[40:43], v[84:87]\n\t"
        "ds_read_b128 v[40:43], %[xa] offset:20480\n\t"
        "s_waitcnt vmcnt(8) lgkmcnt(5)\n\t"
        "v_mfma_f32_16x16x32_bf16 v[48:51], v[216:219], v[44:47], v[48:51]\n\t"
        "v_mfma_f32_16x16x32_bf16 v[68:71], v[220:223], v[44:47], v[68:71]\n\t"
        "ds_read_b128 v[44:47], %[xa] offset:22528\n\t"
        "s_waitcnt lgkmcnt(5)\n\t"
        "v_mfma_f32_16x16x32_bf16 v[52:55], v[216:219], v[24:27], v[52:55]\n\t"
        "v_mfma_f32_16x16x32_bf16 v[72:75], v[220:223], v[24:27], v[72:75]\n\t"
        "buffer_load_dword %[fl], %[fo], %[ssrd], 0 offen sc1\n\t"
        "ds_read_b128 v[24:27], %[xa] offset:24576\n\t"
        "s_waitcnt lgkmcnt(5)\n\t"
        "v_mfma_f32_16x16x32_bf16 v[56:59], v[216:219], v[28:31], v[56:59]\n\t"
        "v_mfma_f32_16x16x32_bf16 v[76:79], v[220:223], v[28:31], v[76:79]\n\t"
        "ds_read_b128 v[28:31], %[xa] offset:26624\n\t"
        "s_waitcnt lgkmcnt(5)\n\t"
        "v_mfma_f32_16x16x32_bf16 v[60:63], v[216:219], v[32:35], v[60:63]\n\t"
        "v_mfma_f32_16x16x32_bf16 v[80:83], v[220:223], v[32:35], v[80:83]\n\t"
        "ds_read_b128 v[32:35], %[xa] offset:28672\n\t"
        "s_waitcnt lgkmcnt(5)\n\t"
        "v_mfma_f32_16x16x32_bf16 v[64:67], v[216:219], v[36:39], v[64:67]\n\t"
        "v_mfma_f32_16x16x32_bf16 v[84:87], v[220:223], v[36:39], v[84:87]\n\t"
        "ds_read_b128 v[36:39], %[xb] offset:20480\n\t"
        "s_waitcnt vmcnt(7) lgkmcnt(5)\n\t"
        "v_mfma_f32_16x16x32_bf16 v[48:51], v[224:227], v[40:43], v[48:51]\n\t"
        "v_mfma_f32_16x16x32_bf16 v[68:71], v[228:231], v[40:43], v[68:71]\n\t"
        "ds_read_b128 v[40:43], %[xb] offset:22528\n\t"
        "s_waitcnt lgkmcnt(5)\n\t"
        "v_mfma_f32_16x16x32_bf16 v[52:55], v[224:227], v[44:47], v[52:55]\n\t"
        "v_mfma_f32_16x16x32_bf16 v[72:75], v[228:231], v[44:47], v[72:75]\n\t"
        "ds_read_b128 v[44:47], %[xb] offset:24576\n\t"
        "s_waitcnt lgkmcnt(5)\n\t"
        "v_mfma_f32_16x16x32_bf16 v[56:59], v[224:227], v[24:27], v[56:59]\n\t"
        "v_mfma_f32_16x16x32_bf16 v[76:79], v[228:231], v[24:27], v[76:79]\n\t"
        "ds_read_b128 v[24:27], %[xb] offset:26624\n\t"
        "s_waitcnt lgkmcnt(5)\n\t"
        "v_mfma_f32_16x16x32_bf16 v[60:63], v[224:227], v[28:31], v[60:63]\n\t"
        "v_mfma_f32_16x16x32_bf16 v[80:83], v[228:231], v[28:31], v[80:83]\n\t"
        "ds_read_b128 v[28:31], %[xb] offset:28672\n\t"
        "s_waitcnt lgkmcnt(5)\n\t"
        "v_mfma_f32_16x16x32_bf16 v[64:67], v[224:227], v[32:35], v[64:67]\n\t"
        "v_mfma_f32_16x16x32_bf16 v[84:87], v[228:231], v[32:35], v[84:87]\n\t"
        "ds_read_b128 v[32:35], %[xa] offset:30720\n\t"
        "s_waitcnt vmcnt(5) lgkmcnt(5)\n\t"
        "v_mfma_f32_16x16x32_bf16 v[48:51], v[232:235], v[36:39], v[48:51]\n\t"
        "v_mfma_f32_16x16x32_bf16 v[68:71], v[236:239], v[36:39], v[68:71]\n\t"
        "ds_read_b128 v[36:39], %[xa] offset:32768\n\t"
        "s_waitcnt lgkmcnt(5)\n\t"
        "v_mfma_f32_16x16x32_bf16 v[52:55], v[232:235], v[40:43], v[52:55]\n\t"
        "v_mfma_f32_16x16x32_bf16 v[72:75], v[236:239], v[40:43], v[72:75]\n\t"
        "ds_read_b128 v[40:43], %[xa] offset:34816\n\t"
        "s_waitcnt lgkmcnt(5)\n\t"
        "v_mfma_f32_16x16x32_bf16 v[56:59], v[232:235], v[44:47], v[56:59]\n\t"
        "v_mfma_f32_16x16x32_bf16 v[76:79], v[236:239], v[44:47], v[76:79]\n\t"
        "ds_read_b128 v[44:47], %[xa] offset:36864\n\t"
        "s_waitcnt lgkmcnt(5)\n\t"
        "v_mfma_f32_16x16x32_bf16 v[60:63], v[232:235], v[24:27], v[60:63]\n\t"
        "v_mfma_f32_16x16x32_bf16 v[80:83], v[236:239], v[24:27], v[80:83]\n\t"
        "ds_read_b128 v[24:27], %[xa] offset:38912\n\t"
        "s_waitcnt lgkmcnt(5)\n\t"
        "v_mfma_f32_16x16x32_bf16 v[64:67], v[232:235], v[28:31], v[64:67]\n\t"
        "v_mfma_f32_16x16x32_bf16 v[84:87], v[236:239], v[28:31], v[84:87]\n\t"
        "ds_read_b128 v[28:31], %[xb] offset:30720\n\t"
        "s_waitcnt vmcnt(3) lgkmcnt(5)\n\t"
        "v_mfma_f32_16x16x32_bf16 v[48:51], v[240:243], v[32:35], v[48:51]\n\t"
        "v_mfma_f32_16x16x32_bf16 v[68:71], v[244:247], v[32:35], v[68:71]\n\t"
        "ds_read_b128 v[32:35], %[xb] offset:32768\n\t"
        "s_waitcnt lgkmcnt(5)\n\t"
        "v_mfma_f32_16x16x32_bf16 v[52:55], v[240:243], v[36:39], v[52:55]\n\t"
        "v_mfma_f32_16x16x32_bf16 v[72:75], v[244:247], v[36:39], v[72:75]\n\t"
        "ds_read_b128 v[36:39], %[xb] offset:34816\n\t"
        "v_and_b32 %[x0], 0xf0, v107\n\t"
        "v_lshrrev_b32 %[x0], 1, %[x0]\n\t"
        "v_add_u32 %[x0], 153344, %[x0]\n\t"
        "v_lshrrev_b32 %[x1], 8, v107\n\t"
        "v_lshl_add_u32 %[x1], %[x1], 5, %[scs]\n\t"
        "s_waitcnt lgkmcnt(5)\n\t"
        "v_mfma_f32_16x16x32_bf16 v[56:59], v[240:243], v[40:43], v[56:59]\n\t"
        "v_mfma_f32_16x16x32_bf16 v[76:79], v[244:247], v[40:43], v[76:79]\n\t"
        "ds_read_b128 v[40:43], %[xb] offset:36864\n\t"
        "s_waitcnt lgkmcnt(5)\n\t"
        "v_mfma_f32_16x16x32_bf16 v[60:63], v[240:243], v[44:47], v[60:63]\n\t"
        "v_mfma_f32_16x16x32_bf16 v[80:83], v[244:247], v[44:47], v[80:83]\n\t"
        "ds_read_b128 v[44:47], %[xb] offset:38912\n\t"
        "s_waitcnt lgkmcnt(5)\n\t"
        "v_mfma_f32_16x16x32_bf16 v[64:67], v[240:243], v[24:27], v[64:67]\n\t"
        "v_mfma_f32_16x16x32_bf16 v[84:87], v[244:247], v[24:27], v[84:87]\n\t"
        "s_waitcnt vmcnt(1) lgkmcnt(4)\n\t"
        "v_mfma_f32_16x16x32_bf16 v[48:51], v[248:251], v[28:31], v[48:51]\n\t"
        "v_mfma_f32_16x16x32_bf16 v[68:71], v[252:255], v[28:31], v[68:71]\n\t"
        "s_waitcnt lgkmcnt(3)\n\t"
        "v_mfma_f32_16x16x32_bf16 v[52:55], v[248:251], v[32:35], v[52:55]\n\t"
        "v_mfma_f32_16x16x32_bf16 v[72:75], v[252:255], v[32:35], v[72:75]\n\t"
        "ds_read_b64 v[34:35], %[x2] offset:%[omc]\n\t"
        "ds_read_b64 v[24:25], %[x0] offset:0\n\t"
        "ds_read_b64 v[26:27], %[x0] offset:128\n\t"
        "ds_read_b64 v[28:29], %[x0] offset:256\n\t"
        "ds_read_b64 v[30:31], %[x0] offset:384\n\t"
        "ds_read_b64 v[32:33], %[x0] offset:512\n\t"
        "s_waitcnt lgkmcnt(8)\n\t"
        "v_mfma_f32_16x16x32_bf16 v[56:59], v[248:251], v[36:39], v[56:59]\n\t"
        "v_mfma_f32_16x16x32_bf16 v[76:79], v[252:255], v[36:39], v[76:79]\n\t"
        "s_waitcnt lgkmcnt(7)\n\t"
        "v_mfma_f32_16x16x32_bf16 v[60:63], v[248:251], v[40:43], v[60:63]\n\t"
        "v_mfma_f32_16x16x32_bf16 v[80:83], v[252:255], v[40:43], v[80:83]\n\t"
        "s_waitcnt lgkmcnt(6)\n\t"
        "v_mfma_f32_16x16x32_bf16 v[64:67], v[248:251], v[44:47], v[64:67]\n\t"
        "v_mfma_f32_16x16x32_bf16 v[84:87], v[252:255], v[44:47], v[84:87]\n\t"
        "v_mov_b32_dpp v36, v48 row_newbcast:0 row_mask:0xf bank_mask:0xf bound_ctrl:1\n\t"
        "v_mov_b32_dpp v37, v49 row_newbcast:0 row_mask:0xf bank_mask:0xf bound_ctrl:1\n\t"
        "v_mov_b32_dpp v38, v50 row_newbcast:0 row_mask:0xf bank_mask:0xf bound_ctrl:1\n\t"
        "v_mov_b32_dpp v39, v51 row_newbcast:0 row_mask:0xf bank_mask:0xf bound_ctrl:1\n\t"
        "v_mov_b32_dpp v40, v68 row_newbcast:0 row_mask:0xf bank_mask:0xf bound_ctrl:1\n\t"
        "v_mov_b32_dpp v41, v69 row_newbcast:0 row_mask:0xf bank_mask:0xf bound_ctrl:1\n\t"
        "v_mov_b32_dpp v42, v70 row_newbcast:0 row_mask:0xf bank_mask:0xf bound_ctrl:1\n\t"
        "v_mov_b32_dpp v43, v71 row_newbcast:0 row_mask:0xf bank_mask:0xf bound_ctrl:1\n\t"
        "Lm_%=:\n\t"
        "s_waitcnt vmcnt(0) lgkmcnt(0)\n\t"
        "v_readfirstlane_b32 %[sf], v34\n\t"
        "v_cmp_ne_u32 vcc, %[sexp], %[fl]\n\t"
        "s_cmp_lt_u32 %[sf], 3\n\t"
        "s_cbranch_scc1 Ln_%=\n\t"
        "s_cbranch_vccnz Ln_%=\n\t"
        "Lo_%=:\n\t"
        "s_mov_b64 exec, %[sm16]\n\t"
        "ds_write_b128 %[x1], v[36:39]\n\t"
        "ds_write_b128 %[x1], v[40:43] offset:16\n\t"
        "s_mov_b64 exec, -1\n\t"
        "v_fma_f32 v48, -v24, v36, v48\n\t"
        "v_fma_f32 v49, -v24, v37, v49\n\t"
        "v_fma_f32 v50, -v24, v38, v50\n\t"
        "v_fma_f32 v51, -v24, v39, v51\n\t"
        "v_fma_f32 v68, -v24, v40, v68\n\t"
        "v_fma_f32 v69, -v24, v41, v69\n\t"
        "v_fma_f32 v70, -v24, v42, v70\n\t"
        "v_fma_f32 v71, -v24, v43, v71\n\t"
        "v_mul_f32 v48, v25, v48\n\t"
        "v_mul_f32 v49, v25, v49\n\t"
        "v_mul_f32 v50, v25, v50\n\t"
        "v_mul_f32 v51, v25, v51\n\t"
        "v_mul_f32 v68, v25, v68\n\t"
        "v_mul_f32 v69, v25, v69\n\t"
        "v_mul_f32 v70, v25, v70\n\t"
        "v_mul_f32 v71, v25, v71\n\t"
        "v_cvt_pk_bf16_f32 v48, v48, v68\n\t"
        "v_cvt_pk_bf16_f32 v49, v49, v69\n\t"
        "v_cvt_pk_bf16_f32 v50, v50, v70\n\t"
        "v_cvt_pk_bf16_f32 v51, v51, v71\n\t"
        "v_add_u32 v68, v110, v111\n\t"
        "v_add_u32 v69, v68, v111\n\t"
        "v_add_u32 v70, v69, v111\n\t"
        "v_add_u32 v71, v70, v111\n\t"
        "v_fma_f32 v52, -v26, v36, v52\n\t"
        "v_fma_f32 v53, -v26, v37, v53\n\t"
        "v_fma_f32 v54, -v26, v38, v54\n\t"
        "v_fma_f32 v55, -v26, v39, v55\n\t"
        "v_fma_f32 v72, -v26, v40, v72\n\t"
        "v_fma_f32 v73, -v26, v41, v73\n\t"
        "v_fma_f32 v74, -v26, v42, v74\n\t"
        "v_fma_f32 v75, -v26, v43, v75\n\t"
        "v_mul_f32 v52, v27, v52\n\t"
        "v_mul_f32 v53, v27, v53\n\t"
        "v_mul_f32 v54, v27, v54\n\t"
        "v_mul_f32 v55, v27, v55\n\t"
        "v_mul_f32 v72, v27, v72\n\t"
        "v_mul_f32 v73, v27, v73\n\t"
        "v_mul_f32 v74, v27, v74\n\t"
        "v_mul_f32 v75, v27, v75\n\t"
        "v_cvt_pk_bf16_f32 v52, v52, v72\n\t"
        "v_cvt_pk_bf16_f32 v53, v53, v73\n\t"
        "v_cvt_pk_bf16_f32 v54, v54, v74\n\t"
        "v_cvt_pk_bf16_f32 v55, v55, v75\n\t"
        "v_fma_f32 v56, -v28, v36, v56\n\t"
        "v_fma_f32 v57, -v28, v37, v57\n\t"
        "v_fma_f32 v58, -v28, v38, v58\n\t"
        "v_fma_f32 v59, -v28, v39, v59\n\t"
        "v_fma_f32 v76, -v28, v40, v76\n\t"
        "v_fma_f32 v77, -v28, v41, v77\n\t"
        "v_fma_f32 v78, -v28, v42, v78\n\t"
        "v_fma_f32 v79, -v28, v43, v79\n\t"
        "v_mul_f32 v56, v29, v56\n\t"
        "v_mul_f32 v57, v29, v57\n\t"
        "v_mul_f32 v58, v29, v58\n\t"
        "v_mul_f32 v59, v29, v59\n\t"
        "v_mul_f32 v76, v29, v76\n\t"
        "v_mul_f32 v77, v29, v77\n\t"
        "v_mul_f32 v78, v29, v78\n\t"
        "v_mul_f32 v79, v29, v79\n\t"
        "v_cvt_pk_bf16_f32 v56, v56, v76\n\t"
        "v_cvt_pk_bf16_f32 v57, v57, v77\n\t"
        "v_cvt_pk_bf16_f32 v58, v58, v78\n\t"
        "v_cvt_pk_bf16_f32 v59, v59, v79\n\t"
        "v_fma_f32 v60, -v30, v36, v60\n\t"
        "v_fma_f32 v61, -v30, v37, v61\n\t"
        "v_fma_f32 v62, -v30, v38, v62\n\t"
        "v_fma_f32 v63, -v30, v39, v63\n\t"
        "v_fma_f32 v80, -v30, v40, v80\n\t"
        "v_fma_f32 v81, -v30, v41, v81\n\t"
        "v_fma_f32 v82, -v30, v42, v82\n\t"
        "v_fma_f32 v83, -v30, v43, v83\n\t"
        "v_mul_f32 v60, v31, v60\n\t"
        "v_mul_f32 v61, v31, v61\n\t"
        "v_mul_f32 v62, v31, v62\n\t"
        "v_mul_f32 v63, v31, v63\n\t"
        "v_mul_f32 v80, v31, v80\n\t"
        "v_mul_f32 v81, v31, v81\n\t"
        "v_mul_f32 v82, v31, v82\n\t"
        "v_mul_f32 v83, v31, v83\n\t"
        "v_cvt_pk_bf16_f32 v60, v60, v80\n\t"
        "v_cvt_pk_bf16_f32 v61, v61, v81\n\t"
        "v_cvt_pk_bf16_f32 v62, v62, v82\n\t"
        "v_cvt_pk_bf16_f32 v63, v63, v83\n\t"
        "v_fma_f32 v64, -v32, v36, v64\n\t"
        "v_fma_f32 v65, -v32, v37, v65\n\t"
        "v_fma_f32 v66, -v32, v38, v66\n\t"
        "v_fma_f32 v67, -v32, v39, v67\n\t"
        "v_fma_f32 v84, -v32, v40, v84\n\t"
        "v_fma_f32 v85, -v32, v41, v85\n\t"
        "v_fma_f32 v86, -v32, v42, v86\n\t"
        "v_fma_f32 v87, -v32, v43, v87\n\t"
        "v_mul_f32 v64, v33, v64\n\t"
        "v_mul_f32 v65, v33, v65\n\t"
        "v_mul_f32 v66, v33, v66\n\t"
        "v_mul_f32 v67, v33, v67\n\t"
        "v_mul_f32 v84, v33, v84\n\t"
        "v_mul_f32 v85, v33, v85\n\t"
        "v_mul_f32 v86, v33, v86\n\t"
        "v_mul_f32 v87, v33, v87\n\t"
        "v_cvt_pk_bf16_f32 v64, v64, v84\n\t"
        "v_cvt_pk_bf16_f32 v65, v65, v85\n\t"
        "v_cvt_pk_bf16_f32 v66, v66, v86\n\t"
        "v_cvt_pk_bf16_f32 v67, v67, v87\n\t"
        "Lu_%=:\n\t"
        "v_readfirstlane_b32 %[sf], v35\n\t"
        "s_cmp_ge_u32 %[sf], 7\n\t"
        "s_cbranch_scc1 Lv_%=\n\t"
        "s_cmp_lt_i32 %[scn], 1\n\t"
        "s_cbranch_scc1 Lv_%=\n\t"
        "s_sub_u32 %[scn], %[scn], 1\n\t"
        "s_sleep 4\n\t"
        "ds_read_b32 v35, %[x2] offset:%[omc]+4\n\t"
        "s_waitcnt lgkmcnt(0)\n\t"
        "s_branch Lu_%=\n\t"
        "Lv_%=:\n\t"
        "s_mov_b64 exec, %[sva]\n\t"
        "ds_write_b128 v68, v[52:55]\n\t"
        "ds_write_b128 v69, v[56:59]\n\t"
        "ds_write_b128 v70, v[60:63]\n\t"
        "ds_write_b128 v71, v[64:67]\n\t"
        "s_mov_b64 exec, %[sv0]\n\t"
        "ds_write_b128 v110, v[48:51]\n\t"
        "s_mov_b64 exec, -1\n\t"
        "s_branch Le_%=\n\t"
        "Ln_%=:\n\t"
        "s_cmp_lt_i32 %[scn], 1\n\t"
        "s_cbranch_scc1 Lo_%=\n\t"
        "s_sub_u32 %[scn], %[scn], 1\n\t"
        "buffer_load_dword %[fl], %[fo], %[ssrd], 0 offen sc1\n\t"
        "ds_read_b64 v[34:35], %[x2] offset:%[omc]\n\t"
        "ds_read_b64 v[24:25], %[x0] offset:0\n\t"
        "ds_read_b64 v[26:27], %[x0] offset:128\n\t"
        "ds_read_b64 v[28:29], %[x0] offset:256\n\t"
        "ds_read_b64 v[30:31], %[x0] offset:384\n\t"
        "ds_read_b64 v[32:33], %[x0] offset:512\n\t"
        "s_waitcnt vmcnt(0)\n\t"
        "s_branch Lm_%=\n\t"
        "Le_%=:\n\t"
        : [xa] "+v"(xa), [xb] "=&v"(xb), [si] "+s"(si), [fl] "=&v"(fl), [sf] "=&s"(sf), [scn] "+s"(cn), [x0] "+v"(xt[0]), [x1] "+v"(xt[1]), [x2] "+v"(xt[2])
        : [ssrd] "s"(srd), [iv] "v"(iv), [fo] "v"(fo), [sexp] "s"(fexp), [scs] "s"(cs), [sva] "s"(va), [sv0] "s"(v0), [sm16] "s"(0x0001000100010001ull), [omc] "n"(BC_MC)
        : "memory", "scc", "vcc", "v255", "v24", "v25", "v26", "v27", "v28", "v29", "v30", "v31", "v32", "v33", "v34", "v35", "v36", "v37", "v38", "v39", "v40", "v41", "v42", "v43", "v44", "v45", "v46", "v47");
}

DEV void gemm1a(u32x4 srd, u32 ho, u32 hi, u32 tag, int fb, u32 cs, u64 va, u64 v0, u32& sf, u32& cn) {
    int xa, ma; u32 one, fl;
    asm volatile(
        "v_add_u32 %[xa], 36864, v107\n\t"
        "v_mov_b32 %[one], 1\n\t"
        "v_lshrrev_b32 %[fl], 8, v107\n\t"
        "v_lshl_add_u32 %[fl], %[fl], 5, %[scs]\n\t"
        "ds_read_b128 v[48:51], %[fl] offset:0\n\t"
        "ds_read_b128 v[68:71], %[fl] offset:16\n\t"
        "v_and_b32 %[ma], 0xf0, v107\n\t"
        "v_lshrrev_b32 %[ma], 1, %[ma]\n\t"
        "v_add_u32 %[ma], 153344, %[ma]\n\t"
        "v_mov_b32 v81, %[stag]\n\t"
        "v_mov_b32 v86, %[stag]\n\t"
        "ds_read_b128 v[24:27], v107 offset:0\n\t"
        "ds_read_b128 v[28:31], v107 offset:1024\n\t"
        "ds_read_b128 v[32:35], v107 offset:2048\n\t"
        "ds_read_b128 v[36:39], v107 offset:3072\n\t"
        "ds_read_b32 %[fl], %[fb] offset:%[og0]\n\t"
        "ds_read_b128 v[40:43], v107 offset:4096\n\t"
        "ds_read_b128 v[44:47], v107 offset:5120\n\t"
        "s_setprio 3\n\t"
        "s_waitcnt lgkmcnt(6)\n\t"
        "v_mfma_f32_16x16x32_bf16 v[72:75], v[112:115], v[24:27], 0\n\t"
        "v_mfma_f32_16x16x32_bf16 v[76:79], v[116:119], v[24:27], 0\n\t"
        "ds_read_b128 v[24:27], v107 offset:6144\n\t"
        "s_waitcnt lgkmcnt(6)\n\t"
        "v_mfma_f32_16x16x32_bf16 v[72:75], v[120:123], v[28:31], v[72:75]\n\t"
        "v_mfma_f32_16x16x32_bf16 v[76:79], v[124:127], v[28:31], v[76:79]\n\t"
        "ds_read_b128 v[28:31], v107 offset:7168\n\t"
        "s_waitcnt lgkmcnt(6)\n\t"
        "v_mfma_f32_16x16x32_bf16 v[72:75], v[128:131], v[32:35], v[72:75]\n\t"
        "v_mfma_f32_16x16x32_bf16 v[76:79], v[132:135], v[32:35], v[76:79]\n\t"
        "ds_read_b128 v[32:35], v107 offset:8192\n\t"
        "s_waitcnt lgkmcnt(6)\n\t"
        "v_mfma_f32_16x16x32_bf16 v[72:75], v[136:139], v[36:39], v[72:75]\n\t"
        "v_mfma_f32_16x16x32_bf16 v[76:79], v[140:143], v[36:39], v[76:79]\n\t"
        "s_waitcnt lgkmcnt(5)\n\t"
        "Lc9_%=:\n\t"
        "v_readfirstlane_b32 %[sf], %[fl]\n\t"
        "s_cmp_lt_u32 %[sf], 2\n\t"
        "s_cbranch_scc1 Ls9_%=\n\t"
        "Lg9_%=:\n\t"
        "ds_read_b128 v[36:39], v107 offset:9216\n\t"
        "s_waitcnt lgkmcnt(5)\n\t"
        "v_mfma_f32_16x16x32_bf16 v[72:75], v[144:147], v[40:43], v[72:75]\n\t"
        "v_mfma_f32_16x16x32_bf16 v[76:79], v[148:151], v[40:43], v[76:79]\n\t"
        "ds_read_b128 v[40:43], v107 offset:10240\n\t"
        "s_waitcnt lgkmcnt(5)\n\t"
        "v_mfma_f32_16x16x32_bf16 v[72:75], v[152:155], v[44:47], v[72:75]\n\t"
        "v_mfma_f32_16x16x32_bf16 v[76:79], v[156:159], v[44:47], v[76:79]\n\t"
        "ds_read_b128 v[44:47], v107 offset:11264\n\t"
        "s_waitcnt lgkmcnt(5)\n\t"
        "v_mfma_f32_16x16x32_bf16 v[72:75], v[160:163], v[24:27], v[72:75]\n\t"
        "v_mfma_f32_16x16x32_bf16 v[76:79], v[164:167], v[24:27], v[76:79]\n\t"
        "ds_read_b128 v[24:27], v107 offset:12288\n\t"
        "s_waitcnt lgkmcnt(5)\n\t"
        "v_mfma_f32_16x16x32_bf16 v[72:75], v[168:171], v[28:31], v[72:75]\n\t"
        "v_mfma_f32_16x16x32_bf16 v[76:79], v[172:175], v[28:31], v[76:79]\n\t"
        "ds_read_b32 %[fl], %[fb] offset:%[og0]\n\t"
        "ds_read_b128 v[28:31], v107 offset:13312\n\t"
        "s_waitcnt lgkmcnt(6)\n\t"
        "v_mfma_f32_16x16x32_bf16 v[72:75], v[176:179], v[32:35], v[72:75]\n\t"
        "v_mfma_f32_16x16x32_bf16 v[76:79], v[180:183], v[32:35], v[76:79]\n\t"
        "ds_read_b128 v[32:35], v107 offset:14336\n\t"
        "s_waitcnt lgkmcnt(6)\n\t"
        "v_mfma_f32_16x16x32_bf16 v[72:75], v[184:187], v[36:39], v[72:75]\n\t"
        "v_mfma_f32_16x16x32_bf16 v[76:79], v[188:191], v[36:39], v[76:79]\n\t"
        "ds_read_b128 v[36:39], v107 offset:15360\n\t"
        "s_waitcnt lgkmcnt(6)\n\t"
        "v_mfma_f32_16x16x32_bf16 v[72:75], v[192:195], v[40:43], v[72:75]\n\t"
        "v_mfma_f32_16x16x32_bf16 v[76:79], v[196:199], v[40:43], v[76:79]\n\t"
        "ds_read_b128 v[40:43], v107 offset:16384\n\t"
        "s_waitcnt lgkmcnt(6)\n\t"
        "v_mfma_f32_16x16x32_bf16 v[72:75], v[200:203], v[44:47], v[72:75]\n\t"
        "v_mfma_f32_16x16x32_bf16 v[76:79], v[204:207], v[44:47], v[76:79]\n\t"
        "ds_read_b128 v[44:47], v107 offset:17408\n\t"
        "s_waitcnt lgkmcnt(6)\n\t"
        "v_mfma_f32_16x16x32_bf16 v[72:75], v[208:211], v[24:27], v[72:75]\n\t"
        "v_mfma_f32_16x16x32_bf16 v[76:79], v[212:215], v[24:27], v[76:79]\n\t"
        "s_waitcnt lgkmcnt(5)\n\t"
        "Lc18_%=:\n\t"
        "v_readfirstlane_b32 %[sf], %[fl]\n\t"
        "s_cmp_lt_u32 %[sf], 3\n\t"
        "s_cbranch_scc1 Ls18_%=\n\t"
        "Lg18_%=:\n\t"
        "ds_read_b128 v[24:27], v107 offset:18432\n\t"
        "s_waitcnt lgkmcnt(5)\n\t"
        "v_mfma_f32_16x16x32_bf16 v[72:75], v[216:219], v[28:31], v[72:75]\n\t"
        "v_mfma_f32_16x16x32_bf16 v[76:79], v[220:223], v[28:31], v[76:79]\n\t"
        "ds_read_b128 v[28:31], v107 offset:19456\n\t"
        "ds_read_b64 v[12:13], %[fb] offset:%[omc]\n\t"
        "ds_read_b64 v[14:15], %[ma] offset:512\n\t"
        "s_waitcnt lgkmcnt(7)\n\t"
        "v_mfma_f32_16x16x32_bf16 v[72:75], v[224:227], v[32:35], v[72:75]\n\t"
        "v_mfma_f32_16x16x32_bf16 v[76:79], v[228:231], v[32:35], v[76:79]\n\t"
        "ds_read_b128 v[32:35], v107 offset:20480\n\t"
        "s_waitcnt lgkmcnt(7)\n\t"
        "v_mfma_f32_16x16x32_bf16 v[72:75], v[232:235], v[36:39], v[72:75]\n\t"
        "v_mfma_f32_16x16x32_bf16 v[76:79], v[236:239], v[36:39], v[76:79]\n\t"
        "ds_read_b128 v[36:39], v107 offset:21504\n\t"
        "s_waitcnt lgkmcnt(7)\n\t"
        "v_mfma_f32_16x16x32_bf16 v[72:75], v[240:243], v[40:43], v[72:75]\n\t"
        "v_mfma_f32_16x16x32_bf16 v[76:79], v[244:247], v[40:43], v[76:79]\n\t"
        "ds_read_b32 %[fl], %[fb] offset:%[og0]\n\t"
        "ds_read_b128 v[40:43], v107 offset:22528\n\t"
        "s_waitcnt lgkmcnt(8)\n\t"
        "v_mfma_f32_16x16x32_bf16 v[72:75], v[248:251], v[44:47], v[72:75]\n\t"
        "v_mfma_f32_16x16x32_bf16 v[76:79], v[252:255], v[44:47], v[76:79]\n\t"
        "ds_read_b128 v[44:47], v107 offset:23552\n\t"
        "s_waitcnt lgkmcnt(8)\n\t"
        "v_mfma_f32_16x16x32_bf16 v[52:55], v[112:115], v[24:27], 0\n\t"
        "v_mfma_f32_16x16x32_bf16 v[56:59], v[116:119], v[24:27], 0\n\t"
        "ds_read_b128 v[24:27], v107 offset:24576\n\t"
        "s_waitcnt lgkmcnt(8)\n\t"
        "v_mfma_f32_16x16x32_bf16 v[52:55], v[120:123], v[28:31], v[52:55]\n\t"
        "s_waitcnt lgkmcnt(7)\n\t"
        "s_waitcnt lgkmcnt(6)\n\t"
        "Lm_%=:\n\t"
        "v_readfirstlane_b32 %[sf], v12\n\t"
        "s_cmp_lt_u32 %[sf], 4\n\t"
        "s_cbranch_scc1 Ln_%=\n\t"
        "Lo_%=:\n\t"
        "v_mfma_f32_16x16x32_bf16 v[56:59], v[124:127], v[28:31], v[56:59]\n\t"
        "ds_read_b128 v[28:31], v107 offset:25600\n\t"
        "s_waitcnt lgkmcnt(6)\n\t"
        "v_mfma_f32_16x16x32_bf16 v[52:55], v[128:131], v[32:35], v[52:55]\n\t"
        "v_fma_f32 v72, -v14, v48, v72\n\t"
        "v_fma_f32 v73, -v14, v49, v73\n\t"
        "v_fma_f32 v74, -v14, v50, v74\n\t"
        "v_fma_f32 v75, -v14, v51, v75\n\t"
        "v_fma_f32 v76, -v14, v68, v76\n\t"
        "v_fma_f32 v77, -v14, v69, v77\n\t"
        "v_mfma_f32_16x16x32_bf16 v[56:59], v[132:135], v[32:35], v[56:59]\n\t"
        "v_fma_f32 v78, -v14, v70, v78\n\t"
        "v_fma_f32 v79, -v14, v71, v79\n\t"
        "v_mul_f32 v72, v15, v72\n\t"
        "v_mul_f32 v73, v15, v73\n\t"
        "v_mul_f32 v74, v15, v74\n\t"
        "v_mul_f32 v75, v15, v75\n\t"
        "ds_read_b128 v[32:35], v107 offset:26624\n\t"
        "s_waitcnt lgkmcnt(6)\n\t"
        "v_mfma_f32_16x16x32_bf16 v[52:55], v[136:139], v[36:39], v[52:55]\n\t"
        "v_mul_f32 v76, v15, v76\n\t"
        "v_mul_f32 v77, v15, v77\n\t"
        "v_mul_f32 v78, v15, v78\n\t"
        "v_mul_f32 v79, v15, v79\n\t"
        "v_cvt_pk_bf16_f32 v82, v72, v76\n\t"
        "v_cvt_pk_bf16_f32 v83, v73, v77\n\t"
        "v_mfma_f32_16x16x32_bf16 v[56:59], v[140:143], v[36:39], v[56:59]\n\t"
        "v_cvt_pk_bf16_f32 v84, v74, v78\n\t"
        "v_cvt_pk_bf16_f32 v85, v75, v79\n\t"
        "s_cmp_eq_u64 %[sv0], 0\n\t"
        "s_cbranch_scc1 Lh0_%=\n\t"
        "buffer_store_dwordx4 v[80:83], v107, %[ssrd], %[sho] offen sc1\n\t"
        "buffer_store_dwordx4 v[84:87], v107, %[ssrd], %[sho] offen offset:1024 sc1\n\t"
        "Lh0_%=:\n\t"
        "s_waitcnt lgkmcnt(5)\n\t"
        "Lc27_%=:\n\t"
        "v_readfirstlane_b32 %[sf], %[fl]\n\t"
        "s_cmp_lt_u32 %[sf], 4\n\t"
        "s_cbranch_scc1 Ls27_%=\n\t"
        "Lg27_%=:\n\t"
        "ds_read_b128 v[36:39], v107 offset:27648\n\t"
        "s_waitcnt lgkmcnt(5)\n\t"
        "v_mfma_f32_16x16x32_bf16 v[52:55], v[144:147], v[40:43], v[52:55]\n\t"
        "v_mfma_f32_16x16x32_bf16 v[56:59], v[148:151], v[40:43], v[56:59]\n\t"
        "ds_read_b128 v[40:43], v107 offset:28672\n\t"
        "s_waitcnt lgkmcnt(5)\n\t"
        "v_mfma_f32_16x16x32_bf16 v[52:55], v[152:155], v[44:47], v[52:55]\n\t"
        "v_mfma_f32_16x16x32_bf16 v[56:59], v[156:159], v[44:47], v[56:59]\n\t"
        "ds_read_b128 v[44:47], v107 offset:29696\n\t"
        "s_setprio 2\n\t"
        "s_waitcnt lgkmcnt(5)\n\t"
        "v_mfma_f32_16x16x32_bf16 v[52:55], v[160:163], v[24:27], v[52:55]\n\t"
        "v_mfma_f32_16x16x32_bf16 v[56:59], v[164:167], v[24:27], v[56:59]\n\t"
        "ds_read_b128 v[24:27], v107 offset:30720\n\t"
        "s_waitcnt lgkmcnt(5)\n\t"
        "v_mfma_f32_16x16x32_bf16 v[52:55], v[168:171], v[28:31], v[52:55]\n\t"
        "v_mfma_f32_16x16x32_bf16 v[56:59], v[172:175], v[28:31], v[56:59]\n\t"
        "ds_read_b32 %[fl], %[fb] offset:%[og0]\n\t"
        "ds_read_b128 v[28:31], v107 offset:31744\n\t"
        "s_waitcnt lgkmcnt(6)\n\t"
        "v_mfma_f32_16x16x32_bf16 v[52:55], v[176:179], v[32:35], v[52:55]\n\t"
        "v_mfma_f32_16x16x32_bf16 v[56:59], v[180:183], v[32:35], v[56:59]\n\t"
        "ds_read_b128 v[32:35], v107 offset:32768\n\t"
        "s_waitcnt lgkmcnt(6)\n\t"
        "v_mfma_f32_16x16x32_bf16 v[52:55], v[184:187], v[36:39], v[52:55]\n\t"
        "v_mfma_f32_16x16x32_bf16 v[56:59], v[188:191], v[36:39], v[56:59]\n\t"
        "ds_read_b128 v[36:39], v107 offset:33792\n\t"
        "s_waitcnt lgkmcnt(6)\n\t"
        "v_mfma_f32_16x16x32_bf16 v[52:55], v[192:195], v[40:43], v[52:55]\n\t"
        "v_mfma_f32_16x16x32_bf16 v[56:59], v[196:199], v[40:43], v[56:59]\n\t"
        "ds_read_b128 v[40:43], v107 offset:34816\n\t"
        "s_waitcnt lgkmcnt(6)\n\t"
        "v_mfma_f32_16x16x32_bf16 v[52:55], v[200:203], v[44:47], v[52:55]\n\t"
        "v_mfma_f32_16x16x32_bf16 v[56:59], v[204:207], v[44:47], v[56:59]\n\t"
        "ds_read_b128 v[44:47], v107 offset:35840\n\t"
        "s_waitcnt lgkmcnt(6)\n\t"
        "v_mfma_f32_16x16x32_bf16 v[52:55], v[208:211], v[24:27], v[52:55]\n\t"
        "v_mfma_f32_16x16x32_bf16 v[56:59], v[212:215], v[24:27], v[56:59]\n\t"
        "s_waitcnt lgkmcnt(5)\n\t"
        "Lc36_%=:\n\t"
        "v_readfirstlane_b32 %[sf], %[fl]\n\t"
        "s_cmp_lt_u32 %[sf], 5\n\t"
        "s_cbranch_scc1 Ls36_%=\n\t"
        "Lg36_%=:\n\t"
        "s_mov_b64 exec, 1\n\t"
        "ds_add_u32 %[fb], %[one] offset:%[omc]+4\n\t"
        "s_mov_b64 exec, -1\n\t"
        "ds_read_b128 v[24:27], %[xa] offset:0\n\t"
        "s_waitcnt lgkmcnt(6)\n\t"
        "v_mfma_f32_16x16x32_bf16 v[52:55], v[216:219], v[28:31], v[52:55]\n\t"
        "v_mfma_f32_16x16x32_bf16 v[56:59], v[220:223], v[28:31], v[56:59]\n\t"
        "ds_read_b128 v[28:31], %[xa] offset:1024\n\t"
        "ds_read_b64 v[14:15], %[ma] offset:128\n\t"
        "s_waitcnt lgkmcnt(7)\n\t"
        "v_mfma_f32_16x16x32_bf16 v[52:55], v[224:227], v[32:35], v[52:55]\n\t"
        "v_mfma_f32_16x16x32_bf16 v[56:59], v[228:231], v[32:35], v[56:59]\n\t"
        "ds_read_b128 v[32:35], %[xa] offset:2048\n\t"
        "s_waitcnt lgkmcnt(7)\n\t"
        "v_mfma_f32_16x16x32_bf16 v[52:55], v[232:235], v[36:39], v[52:55]\n\t"
        "v_mfma_f32_16x16x32_bf16 v[56:59], v[236:239], v[36:39], v[56:59]\n\t"
        "ds_read_b128 v[36:39], %[xa] offset:3072\n\t"
        "s_waitcnt lgkmcnt(7)\n\t"
        "v_mfma_f32_16x16x32_bf16 v[52:55], v[240:243], v[40:43], v[52:55]\n\t"
        "v_mfma_f32_16x16x32_bf16 v[56:59], v[244:247], v[40:43], v[56:59]\n\t"
        "ds_read_b32 %[fl], %[fb] offset:%[og0]\n\t"
        "ds_read_b128 v[40:43], %[xa] offset:4096\n\t"
        "s_waitcnt lgkmcnt(8)\n\t"
        "v_mfma_f32_16x16x32_bf16 v[52:55], v[248:251], v[44:47], v[52:55]\n\t"
        "v_mfma_f32_16x16x32_bf16 v[56:59], v[252:255], v[44:47], v[56:59]\n\t"
        "ds_read_b128 v[44:47], %[xa] offset:5120\n\t"
        "s_waitcnt lgkmcnt(7)\n\t"
        "v_mfma_f32_16x16x32_bf16 v[72:75], v[112:115], v[24:27], 0\n\t"
        "v_mfma_f32_16x16x32_bf16 v[76:79], v[116:119], v[24:27], 0\n\t"
        "ds_read_b128 v[24:27], %[xa] offset:6144\n\t"
        "s_waitcnt lgkmcnt(7)\n\t"
        "v_mfma_f32_16x16x32_bf16 v[72:75], v[120:123], v[28:31], v[72:75]\n\t"
        "v_mfma_f32_16x16x32_bf16 v[76:79], v[124:127], v[28:31], v[76:79]\n\t"
        "s_waitcnt lgkmcnt(6)\n\t"
        "ds_read_b128 v[28:31], %[xa] offset:7168\n\t"
        "s_waitcnt lgkmcnt(6)\n\t"
        "v_mfma_f32_16x16x32_bf16 v[72:75], v[128:131], v[32:35], v[72:75]\n\t"
        "v_fma_f32 v52, -v14, v48, v52\n\t"
        "v_fma_f32 v53, -v14, v49, v53\n\t"
        "v_mfma_f32_16x16x32_bf16 v[76:79], v[132:135], v[32:35], v[76:79]\n\t"
        "v_fma_f32 v54, -v14, v50, v54\n\t"
        "v_fma_f32 v55, -v14, v51, v55\n\t"
        "ds_read_b128 v[32:35], %[xa] offset:8192\n\t"
        "s_waitcnt lgkmcnt(6)\n\t"
        "v_mfma_f32_16x16x32_bf16 v[72:75], v[136:139], v[36:39], v[72:75]\n\t"
        "v_fma_f32 v56, -v14, v68, v56\n\t"
        "v_fma_f32 v57, -v14, v69, v57\n\t"
        "v_mfma_f32_16x16x32_bf16 v[76:79], v[140:143], v[36:39], v[76:79]\n\t"
        "v_fma_f32 v58, -v14, v70, v58\n\t"
        "v_fma_f32 v59, -v14, v71, v59\n\t"
        "s_waitcnt lgkmcnt(5)\n\t"
        "Lc45_%=:\n\t"
        "v_readfirstlane_b32 %[sf], %[fl]\n\t"
        "s_cmp_lt_u32 %[sf], 6\n\t"
        "s_cbranch_scc1 Ls45_%=\n\t"
        "Lg45_%=:\n\t"
        "ds_read_b128 v[36:39], %[xa] offset:9216\n\t"
        "s_waitcnt lgkmcnt(5)\n\t"
        "v_mfma_f32_16x16x32_bf16 v[72:75], v[144:147], v[40:43], v[72:75]\n\t"
        "v_mul_f32 v52, v15, v52\n\t"
        "v_mul_f32 v53, v15, v53\n\t"
        "v_mfma_f32_16x16x32_bf16 v[76:79], v[148:151], v[40:43], v[76:79]\n\t"
        "v_mul_f32 v54, v15, v54\n\t"
        "v_mul_f32 v55, v15, v55\n\t"
        "ds_read_b128 v[40:43], %[xa] offset:10240\n\t"
        "s_waitcnt lgkmcnt(5)\n\t"
        "v_mfma_f32_16x16x32_bf16 v[72:75], v[152:155], v[44:47], v[72:75]\n\t"
        "v_mul_f32 v56, v15, v56\n\t"
        "v_mul_f32 v57, v15, v57\n\t"
        "v_mfma_f32_16x16x32_bf16 v[76:79], v[156:159], v[44:47], v[76:79]\n\t"
        "v_mul_f32 v58, v15, v58\n\t"
        "v_mul_f32 v59, v15, v59\n\t"
        "ds_read_b128 v[44:47], %[xa] offset:11264\n\t"
        "s_waitcnt lgkmcnt(5)\n\t"
        "v_mfma_f32_16x16x32_bf16 v[72:75], v[160:163], v[24:27], v[72:75]\n\t"
        "v_cvt_pk_bf16_f32 v60, v52, v56\n\t"
        "v_cvt_pk_bf16_f32 v61, v53, v57\n\t"
        "v_mfma_f32_16x16x32_bf16 v[76:79], v[164:167], v[24:27], v[76:79]\n\t"
        "v_cvt_pk_bf16_f32 v62, v54, v58\n\t"
        "v_cvt_pk_bf16_f32 v63, v55, v59\n\t"
        "ds_read_b128 v[24:27], %[xa] offset:12288\n\t"
        "s_waitcnt lgkmcnt(5)\n\t"
        "v_mfma_f32_16x16x32_bf16 v[72:75], v[168:171], v[28:31], v[72:75]\n\t"
        "v_mfma_f32_16x16x32_bf16 v[76:79], v[172:175], v[28:31], v[76:79]\n\t"
        "ds_read_b32 %[fl], %[fb] offset:%[og0]\n\t"
        "ds_read_b128 v[28:31], %[xa] offset:13312\n\t"
        "s_waitcnt lgkmcnt(6)\n\t"
        "v_mfma_f32_16x16x32_bf16 v[72:75], v[176:179], v[32:35], v[72:75]\n\t"
        "v_mfma_f32_16x16x32_bf16 v[76:79], v[180:183], v[32:35], v[76:79]\n\t"
        "ds_read_b128 v[32:35], %[xa] offset:14336\n\t"
        "s_waitcnt lgkmcnt(6)\n\t"
        "v_mfma_f32_16x16x32_bf16 v[72:75], v[184:187], v[36:39], v[72:75]\n\t"
        "v_mfma_f32_16x16x32_bf16 v[76:79], v[188:191], v[36:39], v[76:79]\n\t"
        "ds_read_b128 v[36:39], %[xa] offset:15360\n\t"
        "s_waitcnt lgkmcnt(6)\n\t"
        "v_mfma_f32_16x16x32_bf16 v[72:75], v[192:195], v[40:43], v[72:75]\n\t"
        "v_mfma_f32_16x16x32_bf16 v[76:79], v[196:199], v[40:43], v[76:79]\n\t"
        "ds_read_b128 v[40:43], %[xa] offset:16384\n\t"
        "s_waitcnt lgkmcnt(6)\n\t"
        "v_mfma_f32_16x16x32_bf16 v[72:75], v[200:203], v[44:47], v[72:75]\n\t"
        "v_mfma_f32_16x16x32_bf16 v[76:79], v[204:207], v[44:47], v[76:79]\n\t"
        "ds_read_b128 v[44:47], %[xa] offset:17408\n\t"
        "s_setprio 1\n\t"
        "s_waitcnt lgkmcnt(6)\n\t"
        "v_mfma_f32_16x16x32_bf16 v[72:75], v[208:211], v[24:27], v[72:75]\n\t"
        "v_mfma_f32_16x16x32_bf16 v[76:79], v[212:215], v[24:27], v[76:79]\n\t"
        "s_waitcnt lgkmcnt(5)\n\t"
        "Lc54_%=:\n\t"
        "v_readfirstlane_b32 %[sf], %[fl]\n\t"
        "s_cmp_lt_u32 %[sf], 7\n\t"
        "s_cbranch_scc1 Ls54_%=\n\t"
        "Lg54_%=:\n\t"
        "s_mov_b64 exec, 1\n\t"
        "ds_add_u32 %[fb], %[one] offset:%[oc2]\n\t"
        "s_mov_b64 exec, -1\n\t"
        "ds_read_b128 v[24:27], %[xa] offset:18432\n\t"
        "s_waitcnt lgkmcnt(6)\n\t"
        "v_mfma_f32_16x16x32_bf16 v[72:75], v[216:219], v[28:31], v[72:75]\n\t"
        "v_mfma_f32_16x16x32_bf16 v[76:79], v[220:223], v[28:31], v[76:79]\n\t"
        "ds_read_b128 v[28:31], %[xa] offset:19456\n\t"
        "ds_read_b64 v[14:15], %[ma] offset:256\n\t"
        "s_waitcnt lgkmcnt(7)\n\t"
        "v_mfma_f32_16x16x32_bf16 v[72:75], v[224:227], v[32:35], v[72:75]\n\t"
        "v_mfma_f32_16x16x32_bf16 v[76:79], v[228:231], v[32:35], v[76:79]\n\t"
        "ds_read_b128 v[32:35], %[xa] offset:20480\n\t"
        "s_waitcnt lgkmcnt(7)\n\t"
        "v_mfma_f32_16x16x32_bf16 v[72:75], v[232:235], v[36:39], v[72:75]\n\t"
        "v_mfma_f32_16x16x32_bf16 v[76:79], v[236:239], v[36:39], v[76:79]\n\t"
        "ds_read_b128 v[36:39], %[xa] offset:21504\n\t"
        "s_waitcnt lgkmcnt(7)\n\t"
        "v_mfma_f32_16x16x32_bf16 v[72:75], v[240:243], v[40:43], v[72:75]\n\t"
        "v_mfma_f32_16x16x32_bf16 v[76:79], v[244:247], v[40:43], v[76:79]\n\t"
        "ds_read_b32 %[fl], %[fb] offset:%[og0]\n\t"
        "ds_read_b128 v[40:43], %[xa] offset:22528\n\t"
        "s_waitcnt lgkmcnt(8)\n\t"
        "v_mfma_f32_16x16x32_bf16 v[72:75], v[248:251], v[44:47], v[72:75]\n\t"
        "v_mfma_f32_16x16x32_bf16 v[76:79], v[252:255], v[44:47], v[76:79]\n\t"
        "ds_read_b128 v[44:47], %[xa] offset:23552\n\t"
        "s_waitcnt lgkmcnt(7)\n\t"
        "v_mfma_f32_16x16x32_bf16 v[52:55], v[112:115], v[24:27], 0\n\t"
        "v_mfma_f32_16x16x32_bf16 v[56:59], v[116:119], v[24:27], 0\n\t"
        "ds_read_b128 v[24:27], %[xa] offset:24576\n\t"
        "s_waitcnt lgkmcnt(7)\n\t"
        "v_mfma_f32_16x16x32_bf16 v[52:55], v[120:123], v[28:31], v[52:55]\n\t"
        "v_mfma_f32_16x16x32_bf16 v[56:59], v[124:127], v[28:31], v[56:59]\n\t"
        "s_waitcnt lgkmcnt(6)\n\t"
        "ds_read_b128 v[28:31], %[xa] offset:25600\n\t"
        "s_waitcnt lgkmcnt(6)\n\t"
        "v_mfma_f32_16x16x32_bf16 v[52:55], v[128:131], v[32:35], v[52:55]\n\t"
        "v_fma_f32 v72, -v14, v48, v72\n\t"
        "v_fma_f32 v73, -v14, v49, v73\n\t"
        "v_mfma_f32_16x16x32_bf16 v[56:59], v[132:135], v[32:35], v[56:59]\n\t"
        "v_fma_f32 v74, -v14, v50, v74\n\t"
        "v_fma_f32 v75, -v14, v51, v75\n\t"
        "ds_read_b128 v[32:35], %[xa] offset:26624\n\t"
        "s_waitcnt lgkmcnt(6)\n\t"
        "v_mfma_f32_16x16x32_bf16 v[52:55], v[136:139], v[36:39], v[52:55]\n\t"
        "v_fma_f32 v76, -v14, v68, v76\n\t"
        "v_fma_f32 v77, -v14, v69, v77\n\t"
        "v_mfma_f32_16x16x32_bf16 v[56:59], v[140:143], v[36:39], v[56:59]\n\t"
        "v_fma_f32 v78, -v14, v70, v78\n\t"
        "v_fma_f32 v79, -v14, v71, v79\n\t"
        "s_waitcnt lgkmcnt(5)\n\t"
        "Lc63_%=:\n\t"
        "v_readfirstlane_b32 %[sf], %[fl]\n\t"
        "s_cmp_lt_u32 %[sf], 8\n\t"
        "s_cbranch_scc1 Ls63_%=\n\t"
        "Lg63_%=:\n\t"
        "ds_read_b128 v[36:39], %[xa] offset:27648\n\t"
        "s_waitcnt lgkmcnt(5)\n\t"
        "v_mfma_f32_16x16x32_bf16 v[52:55], v[144:147], v[40:43], v[52:55]\n\t"
        "v_mul_f32 v72, v15, v72\n\t"
        "v_mul_f32 v73, v15, v73\n\t"
        "v_mfma_f32_16x16x32_bf16 v[56:59], v[148:151], v[40:43], v[56:59]\n\t"
        "v_mul_f32 v74, v15, v74\n\t"
        "v_mul_f32 v75, v15, v75\n\t"
        "ds_read_b32 v13, %[fb] offset:%[omc]+4\n\t"
        "s_cmp_eq_u64 %[sv0], 0\n\t"
        "s_cbranch_scc1 Lh1_%=\n\t"
        "buffer_load_dwordx4 v[16:19], v107, %[ssrd], %[shi] offen sc1\n\t"
        "buffer_load_dwordx4 v[20:23], v107, %[ssrd], %[shi] offen offset:1024 sc1\n\t"
        "Lh1_%=:\n\t"
        "ds_read_b128 v[40:43], %[xa] offset:28672\n\t"
        "s_waitcnt lgkmcnt(6)\n\t"
        "v_mfma_f32_16x16x32_bf16 v[52:55], v[152:155], v[44:47], v[52:55]\n\t"
        "v_mul_f32 v76, v15, v76\n\t"
        "v_mul_f32 v77, v15, v77\n\t"
        "v_mfma_f32_16x16x32_bf16 v[56:59], v[156:159], v[44:47], v[56:59]\n\t"
        "v_mul_f32 v78, v15, v78\n\t"
        "v_mul_f32 v79, v15, v79\n\t"
        "ds_read_b128 v[44:47], %[xa] offset:29696\n\t"
        "s_waitcnt lgkmcnt(6)\n\t"
        "v_mfma_f32_16x16x32_bf16 v[52:55], v[160:163], v[24:27], v[52:55]\n\t"
        "v_cvt_pk_bf16_f32 v64, v72, v76\n\t"
        "v_cvt_pk_bf16_f32 v65, v73, v77\n\t"
        "v_mfma_f32_16x16x32_bf16 v[56:59], v[164:167], v[24:27], v[56:59]\n\t"
        "v_cvt_pk_bf16_f32 v66, v74, v78\n\t"
        "v_cvt_pk_bf16_f32 v67, v75, v79\n\t"
        "ds_read_b128 v[24:27], %[xa] offset:30720\n\t"
        "s_waitcnt lgkmcnt(6)\n\t"
        "v_mfma_f32_16x16x32_bf16 v[52:55], v[168:171], v[28:31], v[52:55]\n\t"
        "v_add_u32 v72, v110, v111\n\t"
        "v_add_u32 v73, v72, v111\n\t"
        "v_mfma_f32_16x16x32_bf16 v[56:59], v[172:175], v[28:31], v[56:59]\n\t"
        "v_add_u32 v74, v73, v111\n\t"
        "v_add_u32 v75, v74, v111\n\t"
        "ds_read_b128 v[28:31], %[xa] offset:31744\n\t"
        "s_setprio 0\n\t"
        "s_waitcnt lgkmcnt(6)\n\t"
        "v_mfma_f32_16x16x32_bf16 v[52:55], v[176:179], v[32:35], v[52:55]\n\t"
        "v_mfma_f32_16x16x32_bf16 v[56:59], v[180:183], v[32:35], v[56:59]\n\t"
        "ds_read_b128 v[32:35], %[xa] offset:32768\n\t"
        "s_waitcnt lgkmcnt(6)\n\t"
        "v_mfma_f32_16x16x32_bf16 v[52:55], v[184:187], v[36:39], v[52:55]\n\t"
        "v_mfma_f32_16x16x32_bf16 v[56:59], v[188:191], v[36:39], v[56:59]\n\t"
        "s_waitcnt lgkmcnt(5)\n\t"
        "Lu_%=:\n\t"
        "v_readfirstlane_b32 %[sf], v13\n\t"
        "s_cmp_lt_u32 %[sf], 14\n\t"
        "s_cbranch_scc1 Lw_%=\n\t"
        "Lv_%=:\n\t"
        "s_mov_b64 exec, %[sva]\n\t"
        "ds_write_b128 v75, v[82:85]\n\t"
        "ds_write_b128 v72, v[60:63]\n\t"
        "ds_write_b128 v73, v[64:67]\n\t"
        "s_mov_b64 exec, -1\n\t"
        "ds_read_b128 v[36:39], %[xa] offset:33792\n\t"
        "s_waitcnt lgkmcnt(8)\n\t"
        "v_mfma_f32_16x16x32_bf16 v[52:55], v[192:195], v[40:43], v[52:55]\n\t"
        "v_mfma_f32_16x16x32_bf16 v[56:59], v[196:199], v[40:43], v[56:59]\n\t"
        "ds_read_b128 v[40:43], %[xa] offset:34816\n\t"
        "s_waitcnt lgkmcnt(8)\n\t"
        "v_mfma_f32_16x16x32_bf16 v[52:55], v[200:203], v[44:47], v[52:55]\n\t"
        "v_mfma_f32_16x16x32_bf16 v[56:59], v[204:207], v[44:47], v[56:59]\n\t"
        "ds_read_b128 v[44:47], %[xa] offset:35840\n\t"
        "s_waitcnt lgkmcnt(8)\n\t"
        "v_mfma_f32_16x16x32_bf16 v[52:55], v[208:211], v[24:27], v[52:55]\n\t"
        "v_mfma_f32_16x16x32_bf16 v[56:59], v[212:215], v[24:27], v[56:59]\n\t"
        "s_mov_b64 exec, 1\n\t"
        "ds_add_u32 %[fb], %[one] offset:%[oc1]\n\t"
        "s_mov_b64 exec, -1\n\t"
        "s_waitcnt lgkmcnt(8)\n\t"
        "v_mfma_f32_16x16x32_bf16 v[52:55], v[216:219], v[28:31], v[52:55]\n\t"
        "v_mfma_f32_16x16x32_bf16 v[56:59], v[220:223], v[28:31], v[56:59]\n\t"
        "ds_read_b64 v[14:15], %[ma] offset:384\n\t"
        "s_waitcnt lgkmcnt(8)\n\t"
        "v_mfma_f32_16x16x32_bf16 v[52:55], v[224:227], v[32:35], v[52:55]\n\t"
        "v_mfma_f32_16x16x32_bf16 v[56:59], v[228:231], v[32:35], v[56:59]\n\t"
        "s_waitcnt lgkmcnt(4)\n\t"
        "v_mfma_f32_16x16x32_bf16 v[52:55], v[232:235], v[36:39], v[52:55]\n\t"
        "v_mfma_f32_16x16x32_bf16 v[56:59], v[236:239], v[36:39], v[56:59]\n\t"
        "ds_read_b32 v12, %[fb] offset:%[ogo]\n\t"
        "s_waitcnt lgkmcnt(4)\n\t"
        "v_mfma_f32_16x16x32_bf16 v[52:55], v[240:243], v[40:43], v[52:55]\n\t"
        "v_mfma_f32_16x16x32_bf16 v[56:59], v[244:247], v[40:43], v[56:59]\n\t"
        "s_waitcnt lgkmcnt(3)\n\t"
        "v_mfma_f32_16x16x32_bf16 v[52:55], v[248:251], v[44:47], v[52:55]\n\t"
        "v_mfma_f32_16x16x32_bf16 v[56:59], v[252:255], v[44:47], v[56:59]\n\t"
        "s_waitcnt lgkmcnt(0)\n\t"
        "v_readfirstlane_b32 %[sf], v12\n\t"
        "s_nop 6\n\t"
        "v_fma_f32 v52, -v14, v48, v52\n\t"
        "v_fma_f32 v53, -v14, v49, v53\n\t"
        "v_fma_f32 v54, -v14, v50, v54\n\t"
        "v_fma_f32 v55, -v14, v51, v55\n\t"
        "v_fma_f32 v56, -v14, v68, v56\n\t"
        "v_fma_f32 v57, -v14, v69, v57\n\t"
        "v_fma_f32 v58, -v14, v70, v58\n\t"
        "v_fma_f32 v59, -v14, v71, v59\n\t"
        "v_mul_f32 v52, v15, v52\n\t"
        "v_mul_f32 v53, v15, v53\n\t"
        "v_mul_f32 v54, v15, v54\n\t"
        "v_mul_f32 v55, v15, v55\n\t"
        "v_mul_f32 v56, v15, v56\n\t"
        "v_mul_f32 v57, v15, v57\n\t"
        "v_mul_f32 v58, v15, v58\n\t"
        "v_mul_f32 v59, v15, v59\n\t"
        "v_cvt_pk_bf16_f32 v52, v52, v56\n\t"
        "v_cvt_pk_bf16_f32 v53, v53, v57\n\t"
        "v_cvt_pk_bf16_f32 v54, v54, v58\n\t"
        "v_cvt_pk_bf16_f32 v55, v55, v59\n\t"
        "s_mov_b64 exec, %[sva]\n\t"
        "ds_write_b128 v74, v[52:55]\n\t"
        "s_mov_b64 exec, -1\n\t"
        "s_cmp_eq_u64 %[sv0], 0\n\t"
        "s_cbranch_scc1 Lhe_%=\n\t"
        "Lhw_%=:\n\t"
        "s_waitcnt vmcnt(0)\n\t"
        "v_xor_b32 v16, %[stag], v17\n\t"
        "v_xor_b32 v23, %[stag], v22\n\t"
        "v_or_b32 v16, v16, v23\n\t"
        "v_cmp_ne_u32 vcc, 0, v16\n\t"
        "s_cbranch_vccnz Lhr_%=\n\t"
        "Lhg_%=:\n\t"
        "s_mov_b64 exec, %[sv0]\n\t"
        "ds_write_b128 v110, v[18:21]\n\t"
        "s_mov_b64 exec, -1\n\t"
        "Lhe_%=:\n\t"
        "s_branch Le_%=\n\t"
        "Lhr_%=:\n\t"
        "s_cmp_lt_i32 %[scn], 1\n\t"
        "s_cbranch_scc1 Lhg_%=\n\t"
        "s_sub_u32 %[scn], %[scn], 16\n\t"
        "buffer_load_dwordx4 v[16:19], v107, %[ssrd], %[shi] offen sc1\n\t"
        "buffer_load_dwordx4 v[20:23], v107, %[ssrd], %[shi] offen offset:1024 sc1\n\t"
        "s_waitcnt vmcnt(0)\n\t"
        "s_branch Lhw_%=\n\t"
        "Lw_%=:\n\t"
        "s_cmp_lt_i32 %[scn], 1\n\t"
        "s_cbranch_scc1 Lv_%=\n\t"
        "s_sub_u32 %[scn], %[scn], 1\n\t"
        "s_sleep 2\n\t"
        "ds_read_b32 v13, %[fb] offset:%[omc]+4\n\t"
        "s_waitcnt lgkmcnt(0)\n\t"
        "s_branch Lu_%=\n\t"
        "Ls9_%=:\n\t"
        "s_cmp_lt_i32 %[scn], 1\n\t"
        "s_cbranch_scc1 Lg9_%=\n\t"
        "s_sub_u32 %[scn], %[scn], 1\n\t"
        "s_sleep 1\n\t"
        "ds_read_b32 %[fl], %[fb] offset:%[og0]\n\t"
        "s_waitcnt lgkmcnt(0)\n\t"
        "s_branch Lc9_%=\n\t"
        "Ls18_%=:\n\t"
        "s_cmp_lt_i32 %[scn], 1\n\t"
        "s_cbranch_scc1 Lg18_%=\n\t"
        "s_sub_u32 %[scn], %[scn], 1\n\t"
        "s_sleep 1\n\t"
        "ds_read_b32 %[fl], %[fb] offset:%[og0]\n\t"
        "s_waitcnt lgkmcnt(0)\n\t"
        "s_branch Lc18_%=\n\t"
        "Ls27_%=:\n\t"
        "s_cmp_lt_i32 %[scn], 1\n\t"
        "s_cbranch_scc1 Lg27_%=\n\t"
        "s_sub_u32 %[scn], %[scn], 1\n\t"
        "s_sleep 1\n\t"
        "ds_read_b32 %[fl], %[fb] offset:%[og0]\n\t"
        "s_waitcnt lgkmcnt(0)\n\t"
        "s_branch Lc27_%=\n\t"
        "Ls36_%=:\n\t"
        "s_cmp_lt_i32 %[scn], 1\n\t"
        "s_cbranch_scc1 Lg36_%=\n\t"
        "s_sub_u32 %[scn], %[scn], 1\n\t"
        "s_sleep 1\n\t"
        "ds_read_b32 %[fl], %[fb] offset:%[og0]\n\t"
        "s_waitcnt lgkmcnt(0)\n\t"
        "s_branch Lc36_%=\n\t"
        "Ls45_%=:\n\t"
        "s_cmp_lt_i32 %[scn], 1\n\t"
        "s_cbranch_scc1 Lg45_%=\n\t"
        "s_sub_u32 %[scn], %[scn], 1\n\t"
        "s_sleep 1\n\t"
        "ds_read_b32 %[fl], %[fb] offset:%[og0]\n\t"
        "s_waitcnt lgkmcnt(0)\n\t"
        "s_branch Lc45_%=\n\t"
        "Ls54_%=:\n\t"
        "s_cmp_lt_i32 %[scn], 1\n\t"
        "s_cbranch_scc1 Lg54_%=\n\t"
        "s_sub_u32 %[scn], %[scn], 1\n\t"
        "s_sleep 1\n\t"
        "ds_read_b32 %[fl], %[fb] offset:%[og0]\n\t"
        "s_waitcnt lgkmcnt(0)\n\t"
        "s_branch Lc54_%=\n\t"
        "Ls63_%=:\n\t"
        "s_cmp_lt_i32 %[scn], 1\n\t"
        "s_cbranch_scc1 Lg63_%=\n\t"
        "s_sub_u32 %[scn], %[scn], 1\n\t"
        "s_sleep 1\n\t"
        "ds_read_b32 %[fl], %[fb] offset:%[og0]\n\t"
        "s_waitcnt lgkmcnt(0)\n\t"
        "s_branch Lc63_%=\n\t"
        "Ln_%=:\n\t"
        "s_cmp_lt_i32 %[scn], 1\n\t"
        "s_cbranch_scc1 Lo_%=\n\t"
        "s_sub_u32 %[scn], %[scn], 1\n\t"
        "s_sleep 1\n\t"
        "ds_read_b64 v[12:13], %[fb] offset:%[omc]\n\t"
        "ds_read_b64 v[14:15], %[ma] offset:512\n\t"
        "s_waitcnt lgkmcnt(0)\n\t"
        "s_branch Lm_%=\n\t"
        "Le_%=:\n\t"
        : [xa] "=&v"(xa), [ma] "=&v"(ma), [one] "=&v"(one), [fl] "=&v"(fl), [sf] "=&s"(sf), [scn] "+s"(cn)
        : [ssrd] "s"(srd), [sho] "s"(ho), [shi] "s"(hi), [stag] "s"(tag), [fb] "v"(fb), [scs] "s"(cs), [sva] "s"(va), [sv0] "s"(v0), [ogo] "n"(BC_GO), [omc] "n"(BC_MC), [oc1] "n"(BC_C1), [oc2] "n"(BC_C2), [og0] "n"(BC_G0)
        : "memory", "scc", "vcc", "v12", "v13", "v14", "v15", "v16", "v17", "v18", "v19", "v20", "v21", "v22", "v23", "v24", "v25", "v26", "v27", "v28", "v29", "v30", "v31", "v32", "v33", "v34", "v35", "v36", "v37", "v38", "v39", "v40", "v41", "v42", "v43", "v44", "v45", "v46", "v47");
}

DEV void gather(u32x4 srd, u32 base) {
    u32 so, a, t;
    asm volatile(
        "s_mov_b32 m0, 0\n\t"
        "s_add_u32 %[so], %[sb], 55296\n\t"
        "v_mov_b32 %[a], 153984\n\t"
        "s_nop 0\n\t"
        "buffer_load_dwordx4 v107, %[ssrd], %[so] offen offset:0 sc1 lds\n\t"
        "buffer_load_dwordx4 v107, %[ssrd], %[so] offen offset:1024 sc1 lds\n\t"
        "buffer_load_dwordx4 v107, %[ssrd], %[so] offen offset:2048 sc1 lds\n\t"
        "buffer_load_dwordx4 v107, %[ssrd], %[so] offen offset:3072 sc1 lds\n\t"
        "s_mov_b32 m0, 4096\n\t"
        "s_add_u32 %[so], %[so], 4096\n\t"
        "buffer_load_dwordx4 v107, %[ssrd], %[so] offen offset:0 sc1 lds\n\t"
        "buffer_load_dwordx4 v107, %[ssrd], %[so] offen offset:1024 sc1 lds\n\t"
        "buffer_load_dwordx4 v107, %[ssrd], %[so] offen offset:2048 sc1 lds\n\t"
        "buffer_load_dwordx4 v107, %[ssrd], %[so] offen offset:3072 sc1 lds\n\t"
        "s_mov_b32 m0, 8192\n\t"
        "s_add_u32 %[so], %[so], 4096\n\t"
        "buffer_load_dwordx4 v107, %[ssrd], %[so] offen offset:0 sc1 lds\n\t"
        "buffer_load_dwordx4 v107, %[ssrd], %[so] offen offset:1024 sc1 lds\n\t"
        "buffer_load_dwordx4 v107, %[ssrd], %[so] offen offset:2048 sc1 lds\n\t"
        "buffer_load_dwordx4 v107, %[ssrd], %[so] offen offset:3072 sc1 lds\n\t"
        "s_mov_b32 m0, 12288\n\t"
        "s_add_u32 %[so], %[so], 4096\n\t"
        "buffer_load_dwordx4 v107, %[ssrd], %[so] offen offset:0 sc1 lds\n\t"
        "buffer_load_dwordx4 v107, %[ssrd], %[so] offen offset:1024 sc1 lds\n\t"
        "buffer_load_dwordx4 v107, %[ssrd], %[so] offen offset:2048 sc1 lds\n\t"
        "buffer_load_dwordx4 v107, %[ssrd], %[so] offen offset:3072 sc1 lds\n\t"
        "s_mov_b32 m0, 16384\n\t"
        "s_add_u32 %[so], %[so], 4096\n\t"
        "buffer_load_dwordx4 v107, %[ssrd], %[so] offen offset:0 sc1 lds\n\t"
        "buffer_load_dwordx4 v107, %[ssrd], %[so] offen offset:1024 sc1 lds\n\t"
        "s_mov_b32 m0, 18432\n\t"
        "s_mov_b32 %[so], %[sb]\n\t"
        "buffer_load_dwordx4 v107, %[ssrd], %[so] offen offset:0 sc1 lds\n\t"
        "buffer_load_dwordx4 v107, %[ssrd], %[so] offen offset:1024 sc1 lds\n\t"
        "buffer_load_dwordx4 v107, %[ssrd], %[so] offen offset:2048 sc1 lds\n\t"
        "buffer_load_dwordx4 v107, %[ssrd], %[so] offen offset:3072 sc1 lds\n\t"
        "s_mov_b32 m0, 22528\n\t"
        "s_add_u32 %[so], %[so], 4096\n\t"
        "buffer_load_dwordx4 v107, %[ssrd], %[so] offen offset:0 sc1 lds\n\t"
        "buffer_load_dwordx4 v107, %[ssrd], %[so] offen offset:1024 sc1 lds\n\t"
        "buffer_load_dwordx4 v107, %[ssrd], %[so] offen offset:2048 sc1 lds\n\t"
        "buffer_load_dwordx4 v107, %[ssrd], %[so] offen offset:3072 sc1 lds\n\t"
        "s_mov_b32 m0, 26624\n\t"
        "s_add_u32 %[so], %[so], 4096\n\t"
        "buffer_load_dwordx4 v107, %[ssrd], %[so] offen offset:0 sc1 lds\n\t"
        "buffer_load_dwordx4 v107, %[ssrd], %[so] offen offset:1024 sc1 lds\n\t"
        "buffer_load_dwordx4 v107, %[ssrd], %[so] offen offset:2048 sc1 lds\n\t"
        "buffer_load_dwordx4 v107, %[ssrd], %[so] offen offset:3072 sc1 lds\n\t"
        "s_mov_b32 m0, 30720\n\t"
        "s_add_u32 %[so], %[so], 4096\n\t"
        "buffer_load_dwordx4 v107, %[ssrd], %[so] offen offset:0 sc1 lds\n\t"
        "buffer_load_dwordx4 v107, %[ssrd], %[so] offen offset:1024 sc1 lds\n\t"
        "buffer_load_dwordx4 v107, %[ssrd], %[so] offen offset:2048 sc1 lds\n\t"
        "buffer_load_dwordx4 v107, %[ssrd], %[so] offen offset:3072 sc1 lds\n\t"
        "s_mov_b32 m0, 34816\n\t"
        "s_add_u32 %[so], %[so], 4096\n\t"
        "buffer_load_dwordx4 v107, %[ssrd], %[so] offen offset:0 sc1 lds\n\t"
        "buffer_load_dwordx4 v107, %[ssrd], %[so] offen offset:1024 sc1 lds\n\t"
        "v_mov_b32 %[t], 1\n\t"
        "s_waitcnt vmcnt(27)\n\t"
        "ds_write_b32 %[a], %[t] offset:%[og0]\n\t"
        "buffer_load_dwordx4 v107, %[ssrd], %[so] offen offset:2048 sc1 lds\n\t"
        "buffer_load_dwordx4 v107, %[ssrd], %[so] offen offset:3072 sc1 lds\n\t"
        "s_mov_b32 m0, 38912\n\t"
        "s_add_u32 %[so], %[so], 4096\n\t"
        "buffer_load_dwordx4 v107, %[ssrd], %[so] offen offset:0 sc1 lds\n\t"
        "buffer_load_dwordx4 v107, %[ssrd], %[so] offen offset:1024 sc1 lds\n\t"
        "buffer_load_dwordx4 v107, %[ssrd], %[so] offen offset:2048 sc1 lds\n\t"
        "buffer_load_dwordx4 v107, %[ssrd], %[so] offen offset:3072 sc1 lds\n\t"
        "s_mov_b32 m0, 43008\n\t"
        "s_add_u32 %[so], %[so], 4096\n\t"
        "buffer_load_dwordx4 v107, %[ssrd], %[so] offen offset:0 sc1 lds\n\t"
        "buffer_load_dwordx4 v107, %[ssrd], %[so] offen offset:1024 sc1 lds\n\t"
        "buffer_load_dwordx4 v107, %[ssrd], %[so] offen offset:2048 sc1 lds\n\t"
        "v_mov_b32 %[t], 2\n\t"
        "s_waitcnt vmcnt(27)\n\t"
        "ds_write_b32 %[a], %[t] offset:%[og0]\n\t"
        "buffer_load_dwordx4 v107, %[ssrd], %[so] offen offset:3072 sc1 lds\n\t"
        "s_mov_b32 m0, 47104\n\t"
        "s_add_u32 %[so], %[so], 4096\n\t"
        "buffer_load_dwordx4 v107, %[ssrd], %[so] offen offset:0 sc1 lds\n\t"
        "buffer_load_dwordx4 v107, %[ssrd], %[so] offen offset:1024 sc1 lds\n\t"
        "buffer_load_dwordx4 v107, %[ssrd], %[so] offen offset:2048 sc1 lds\n\t"
        "buffer_load_dwordx4 v107, %[ssrd], %[so] offen offset:3072 sc1 lds\n\t"
        "s_mov_b32 m0, 51200\n\t"
        "s_add_u32 %[so], %[so], 4096\n\t"
        "buffer_load_dwordx4 v107, %[ssrd], %[so] offen offset:0 sc1 lds\n\t"
        "buffer_load_dwordx4 v107, %[ssrd], %[so] offen offset:1024 sc1 lds\n\t"
        "buffer_load_dwordx4 v107, %[ssrd], %[so] offen offset:2048 sc1 lds\n\t"
        "buffer_load_dwordx4 v107, %[ssrd], %[so] offen offset:3072 sc1 lds\n\t"
        "v_mov_b32 %[t], 3\n\t"
        "s_waitcnt vmcnt(27)\n\t"
        "ds_write_b32 %[a], %[t] offset:%[og0]\n\t"
        "s_mov_b32 m0, 55296\n\t"
        "s_add_u32 %[so], %[so], 4096\n\t"
        "buffer_load_dwordx4 v107, %[ssrd], %[so] offen offset:0 sc1 lds\n\t"
        "buffer_load_dwordx4 v107, %[ssrd], %[so] offen offset:1024 sc1 lds\n\t"
        "buffer_load_dwordx4 v107, %[ssrd], %[so] offen offset:2048 sc1 lds\n\t"
        "buffer_load_dwordx4 v107, %[ssrd], %[so] offen offset:3072 sc1 lds\n\t"
        "s_mov_b32 m0, 59392\n\t"
        "s_add_u32 %[so], %[so], 4096\n\t"
        "buffer_load_dwordx4 v107, %[ssrd], %[so] offen offset:0 sc1 lds\n\t"
        "buffer_load_dwordx4 v107, %[ssrd], %[so] offen offset:1024 sc1 lds\n\t"
        "buffer_load_dwordx4 v107, %[ssrd], %[so] offen offset:2048 sc1 lds\n\t"
        "buffer_load_dwordx4 v107, %[ssrd], %[so] offen offset:3072 sc1 lds\n\t"
        "s_mov_b32 m0, 63488\n\t"
        "s_add_u32 %[so], %[so], 4096\n\t"
        "buffer_load_dwordx4 v107, %[ssrd], %[so] offen offset:0 sc1 lds\n\t"
        "v_mov_b32 %[t], 4\n\t"
        "s_waitcnt vmcnt(27)\n\t"
        "ds_write_b32 %[a], %[t] offset:%[og0]\n\t"
        "buffer_load_dwordx4 v107, %[ssrd], %[so] offen offset:1024 sc1 lds\n\t"
        "buffer_load_dwordx4 v107, %[ssrd], %[so] offen offset:2048 sc1 lds\n\t"
        "buffer_load_dwordx4 v107, %[ssrd], %[so] offen offset:3072 sc1 lds\n\t"
        "s_mov_b32 m0, 67584\n\t"
        "s_add_u32 %[so], %[so], 4096\n\t"
        "buffer_load_dwordx4 v107, %[ssrd], %[so] offen offset:0 sc1 lds\n\t"
        "buffer_load_dwordx4 v107, %[ssrd], %[so] offen offset:1024 sc1 lds\n\t"
        "buffer_load_dwordx4 v107, %[ssrd], %[so] offen offset:2048 sc1 lds\n\t"
        "buffer_load_dwordx4 v107, %[ssrd], %[so] offen offset:3072 sc1 lds\n\t"
        "s_mov_b32 m0, 71680\n\t"
        "s_add_u32 %[so], %[so], 4096\n\t"
        "buffer_load_dwordx4 v107, %[ssrd], %[so] offen offset:0 sc1 lds\n\t"
        "buffer_load_dwordx4 v107, %[ssrd], %[so] offen offset:1024 sc1 lds\n\t"
        "v_mov_b32 %[t], 5\n\t"
        "s_waitcnt vmcnt(27)\n\t"
        "ds_write_b32 %[a], %[t] offset:%[og0]\n\t"
        "v_mov_b32 %[t], 6\n\t"
        "s_waitcnt vmcnt(18)\n\t"
        "ds_write_b32 %[a], %[t] offset:%[og0]\n\t"
        "v_mov_b32 %[t], 7\n\t"
        "s_waitcnt vmcnt(9)\n\t"
        "ds_write_b32 %[a], %[t] offset:%[og0]\n\t"
        "v_mov_b32 %[t], 8\n\t"
        "s_waitcnt vmcnt(0)\n\t"
        "ds_write_b32 %[a], %[t] offset:%[og0]\n\t"
        : [so] "=&s"(so), [a] "=&v"(a), [t] "=&v"(t) : [ssrd] "s"(srd), [sb] "s"(base), [og0] "n"(BC_G0) : "memory", "scc");
}

DEV void gemm1b(int fb, int xr, int xq, u32 w1, u64 va, u64 v0, u32& cn) {
    u32 sf, st; u64 mw, mx;
    asm volatile(
        "v_lshrrev_b32 v36, 8, v107\n\t"
        "v_mov_b32 v37, %[lxr]\n\t"
        "v_lshl_add_u32 v36, v36, 4, v37\n\t"
        "v_lshrrev_b32 v37, 4, v111\n\t"
        "Lw_%=:\n\t"
        "ds_read_b32 v34, %[fb] offset:%[ogo]\n\t"
        "s_sub_u32 %[scn], %[scn], 1\n\t"
        "s_waitcnt lgkmcnt(0)\n\t"
        "v_readfirstlane_b32 %[sf], v34\n\t"
        "s_cmp_eq_u32 %[sf], 1\n\t"
        "s_cbranch_scc1 Le_%=\n\t"
        "s_cmp_lt_i32 %[scn], 1\n\t"
        "s_cbranch_scc1 Le_%=\n\t"
        "s_cmp_lg_u32 %[sf], 0\n\t"
        "s_cbranch_scc1 Lg_%=\n\t"
        "s_sleep 1\n\t"
        "s_branch Lw_%=\n\t"
        "Lg_%=:\n\t"
        "ds_read_b128 v[52:55], v36 offset:0\n\t"
        "ds_read_b128 v[56:59], v36 offset:64\n\t"
        "ds_read_b128 v[60:63], v36 offset:128\n\t"
        "ds_read_b128 v[64:67], v36 offset:192\n\t"
        "ds_read_b128 v[72:75], v36 offset:256\n\t"
        "ds_read_b128 v[76:79], v36 offset:320\n\t"
        "ds_read_b128 v[80:83], v36 offset:384\n\t"
        "ds_read_b128 v[84:87], v36 offset:448\n\t"
        "s_lshr_b32 %[sf], %[sf], 8\n\t"
        "s_and_b32 %[st], %[sf], 15\n\t"
        "s_lshl_b64 %[smx], %[sm16], %[st]\n\t"
        "s_cmp_ge_u32 %[sf], 16\n\t"
        "s_cselect_b64 %[smw], %[sva], %[sv0]\n\t"
        "s_lshr_b32 %[st], %[sf], 4\n\t"
        "s_cmp_eq_u32 %[st], %[sw1]\n\t"
        "s_cselect_b64 %[smx], %[smx], 0\n\t"
        "s_and_b64 %[smw], %[smw], %[sm16]\n\t"
        "s_cbranch_scc0 Le_%=\n\t"
        "s_waitcnt lgkmcnt(7)\n\t"
        "v_mfma_f32_16x16x32_bf16 v[24:27], v[112:115], v[52:55], 0\n\t"
        "v_mfma_f32_16x16x32_bf16 v[28:31], v[116:119], v[52:55], 0\n\t"
        "ds_read_b128 v[52:55], v36 offset:512\n\t"
        "s_waitcnt lgkmcnt(7)\n\t"
        "v_mfma_f32_16x16x32_bf16 v[24:27], v[120:123], v[56:59], v[24:27]\n\t"
        "v_mfma_f32_16x16x32_bf16 v[28:31], v[124:127], v[56:59], v[28:31]\n\t"
        "ds_read_b128 v[56:59], v36 offset:576\n\t"
        "v_mad_u32_u24 v37, v37, %[sf], v110\n\t"
        "s_waitcnt lgkmcnt(7)\n\t"
        "v_mfma_f32_16x16x32_bf16 v[24:27], v[128:131], v[60:63], v[24:27]\n\t"
        "v_mfma_f32_16x16x32_bf16 v[28:31], v[132:135], v[60:63], v[28:31]\n\t"
        "ds_read_b128 v[60:63], v36 offset:640\n\t"
        "s_waitcnt lgkmcnt(7)\n\t"
        "v_mfma_f32_16x16x32_bf16 v[24:27], v[136:139], v[64:67], v[24:27]\n\t"
        "v_mfma_f32_16x16x32_bf16 v[28:31], v[140:143], v[64:67], v[28:31]\n\t"
        "ds_read_b128 v[64:67], v36 offset:704\n\t"
        "s_waitcnt lgkmcnt(7)\n\t"
        "v_mfma_f32_16x16x32_bf16 v[24:27], v[144:147], v[72:75], v[24:27]\n\t"
        "v_mfma_f32_16x16x32_bf16 v[28:31], v[148:151], v[72:75], v[28:31]\n\t"
        "ds_read_b128 v[72:75], v36 offset:768\n\t"
        "s_waitcnt lgkmcnt(7)\n\t"
        "v_mfma_f32_16x16x32_bf16 v[24:27], v[152:155], v[76:79], v[24:27]\n\t"
        "v_mfma_f32_16x16x32_bf16 v[28:31], v[156:159], v[76:79], v[28:31]\n\t"
        "ds_read_b128 v[76:79], v36 offset:832\n\t"
        "s_waitcnt lgkmcnt(7)\n\t"
        "v_mfma_f32_16x16x32_bf16 v[24:27], v[160:163], v[80:83], v[24:27]\n\t"
        "v_mfma_f32_16x16x32_bf16 v[28:31], v[164:167], v[80:83], v[28:31]\n\t"
        "ds_read_b128 v[80:83], v36 offset:896\n\t"
        "s_waitcnt lgkmcnt(7)\n\t"
        "v_mfma_f32_16x16x32_bf16 v[24:27], v[168:171], v[84:87], v[24:27]\n\t"
        "v_mfma_f32_16x16x32_bf16 v[28:31], v[172:175], v[84:87], v[28:31]\n\t"
        "ds_read_b128 v[84:87], v36 offset:960\n\t"
        "s_waitcnt lgkmcnt(7)\n\t"
        "v_mfma_f32_16x16x32_bf16 v[24:27], v[176:179], v[52:55], v[24:27]\n\t"
        "v_mfma_f32_16x16x32_bf16 v[28:31], v[180:183], v[52:55], v[28:31]\n\t"
        "ds_read_b128 v[52:55], v36 offset:1024\n\t"
        "s_waitcnt lgkmcnt(7)\n\t"
        "v_mfma_f32_16x16x32_bf16 v[24:27], v[184:187], v[56:59], v[24:27]\n\t"
        "v_mfma_f32_16x16x32_bf16 v[28:31], v[188:191], v[56:59], v[28:31]\n\t"
        "ds_read_b128 v[56:59], v36 offset:1088\n\t"
        "s_waitcnt lgkmcnt(7)\n\t"
        "v_mfma_f32_16x16x32_bf16 v[24:27], v[192:195], v[60:63], v[24:27]\n\t"
        "v_mfma_f32_16x16x32_bf16 v[28:31], v[196:199], v[60:63], v[28:31]\n\t"
        "s_waitcnt lgkmcnt(6)\n\t"
        "v_mfma_f32_16x16x32_bf16 v[24:27], v[200:203], v[64:67], v[24:27]\n\t"
        "v_mfma_f32_16x16x32_bf16 v[28:31], v[204:207], v[64:67], v[28:31]\n\t"
        "s_waitcnt lgkmcnt(5)\n\t"
        "v_mfma_f32_16x16x32_bf16 v[24:27], v[208:211], v[72:75], v[24:27]\n\t"
        "v_mfma_f32_16x16x32_bf16 v[28:31], v[212:215], v[72:75], v[28:31]\n\t"
        "ds_read_b32 v34, %[fb] offset:%[ops]\n\t"
        "ds_read_b64 v[32:33], %[fb] offset:%[opm]\n\t"
        "s_waitcnt lgkmcnt(6)\n\t"
        "v_mfma_f32_16x16x32_bf16 v[24:27], v[216:219], v[76:79], v[24:27]\n\t"
        "v_mfma_f32_16x16x32_bf16 v[28:31], v[220:223], v[76:79], v[28:31]\n\t"
        "s_waitcnt lgkmcnt(5)\n\t"
        "v_mfma_f32_16x16x32_bf16 v[24:27], v[224:227], v[80:83], v[24:27]\n\t"
        "v_mfma_f32_16x16x32_bf16 v[28:31], v[228:231], v[80:83], v[28:31]\n\t"
        "s_waitcnt lgkmcnt(4)\n\t"
        "v_mfma_f32_16x16x32_bf16 v[24:27], v[232:235], v[84:87], v[24:27]\n\t"
        "v_mfma_f32_16x16x32_bf16 v[28:31], v[236:239], v[84:87], v[28:31]\n\t"
        "s_waitcnt lgkmcnt(3)\n\t"
        "v_mfma_f32_16x16x32_bf16 v[24:27], v[240:243], v[52:55], v[24:27]\n\t"
        "v_mfma_f32_16x16x32_bf16 v[28:31], v[244:247], v[52:55], v[28:31]\n\t"
        "s_waitcnt lgkmcnt(2)\n\t"
        "v_mfma_f32_16x16x32_bf16 v[24:27], v[248:251], v[56:59], v[24:27]\n\t"
        "v_mfma_f32_16x16x32_bf16 v[28:31], v[252:255], v[56:59], v[28:31]\n\t"
        "Lm_%=:\n\t"
        "s_waitcnt lgkmcnt(0)\n\t"
        "v_readfirstlane_b32 %[st], v34\n\t"
        "s_cmp_ge_u32 %[st], %[sf]\n\t"
        "s_cbranch_scc1 Lo_%=\n\t"
        "s_cmp_lt_i32 %[scn], 1\n\t"
        "s_cbranch_scc1 Lo_%=\n\t"
        "s_sub_u32 %[scn], %[scn], 1\n\t"
        "ds_read_b32 v34, %[fb] offset:%[ops]\n\t"
        "ds_read_b64 v[32:33], %[fb] offset:%[opm]\n\t"
        "s_branch Lm_%=\n\t"
        "Lo_%=:\n\t"
        "s_mov_b64 exec, %[smx]\n\t"
        "ds_read_b128 v[88:91], %[xr]\n\t"
        "ds_read_b128 v[92:95], %[xr] offset:64\n\t"
        "ds_read_b64 v[96:97], %[xq]\n\t"
        "s_mov_b64 exec, -1\n\t"
        "v_fma_f32 v24, -v32, v48, v24\n\t"
        "v_fma_f32 v25, -v32, v49, v25\n\t"
        "v_fma_f32 v26, -v32, v50, v26\n\t"
        "v_fma_f32 v27, -v32, v51, v27\n\t"
        "v_fma_f32 v28, -v32, v68, v28\n\t"
        "v_fma_f32 v29, -v32, v69, v29\n\t"
        "v_fma_f32 v30, -v32, v70, v30\n\t"
        "v_fma_f32 v31, -v32, v71, v31\n\t"
        "v_mul_f32 v24, v33, v24\n\t"
        "v_mul_f32 v25, v33, v25\n\t"
        "v_mul_f32 v26, v33, v26\n\t"
        "v_mul_f32 v27, v33, v27\n\t"
        "v_mul_f32 v28, v33, v28\n\t"
        "v_mul_f32 v29, v33, v29\n\t"
        "v_mul_f32 v30, v33, v30\n\t"
        "v_mul_f32 v31, v33, v31\n\t"
        "v_cvt_pk_bf16_f32 v24, v24, v28\n\t"
        "v_cvt_pk_bf16_f32 v25, v25, v29\n\t"
        "v_cvt_pk_bf16_f32 v26, v26, v30\n\t"
        "v_cvt_pk_bf16_f32 v27, v27, v31\n\t"
        "s_mov_b64 exec, %[smw]\n\t"
        "ds_write_b128 v37, v[24:27]\n\t"
        "s_mov_b64 exec, -1\n\t"
        "Le_%=:\n\t"
        "s_waitcnt lgkmcnt(0)\n\t"
        : [sf] "=&s"(sf), [st] "=&s"(st), [smw] "=&s"(mw), [smx] "=&s"(mx), [scn] "+s"(cn)
        : [fb] "v"(fb), [xr] "v"(xr), [xq] "v"(xq), [sw1] "s"(w1), [sva] "s"(va), [sv0] "s"(v0), [sm16] "s"(0x0001000100010001ull), [ogo] "n"(BC_GO), [ops] "n"(BC_PST),
          [opm] "n"(BC_PM), [lxr] "n"(L_XR)
        : "memory", "scc", "v24", "v25", "v26", "v27", "v28", "v29", "v30", "v31", "v32", "v33", "v34", "v35", "v36", "v37");
}

DEV void mid(const MidS& a, u32 tag, u64 gb, u32 gl, u32 wt, u32& cn) {
    u32 ob4, ob4x, cd;
    asm volatile(
        "s_mov_b32 m0, %[sldx]\n\t"
        "s_nop 3\n\t"
        "s_waitcnt lgkmcnt(0)\n\t"
        "s_barrier\n\t"
        "ds_read_b64 v[36:37], v102 offset:0\n\t"
        "ds_read_b32 v38, v103 offset:0\n\t"
        "ds_read_b64 v[20:21], v100 offset:0\n\t"
        "ds_read_b32 v22, v101 offset:0\n\t"
        "ds_read_b64 v[24:25], v100 offset:4864\n\t"
        "ds_read_b32 v26, v101 offset:4864\n\t"
        "ds_read_b64 v[40:41], v102 offset:40\n\t"
        "ds_read_b32 v42, v103 offset:4\n\t"
        "ds_read_b64 v[28:29], v100 offset:40\n\t"
        "ds_read_b32 v30, v101 offset:4\n\t"
        "ds_read_b64 v[32:33], v100 offset:4904\n\t"
        "ds_read_b32 v34, v101 offset:4868\n\t"
        "ds_read_b64_tr_b16 v[52:53], v105\n\t"
        "ds_read_b64_tr_b16 v[54:55], v105 offset:4864\n\t"
        "s_bitcmp1_b32 %[sskip], 0\n\t"
        "s_cbranch_scc1 Le0_%=\n\t"
        "buffer_load_dwordx4 v107, %[ssrd], %[swx] offen sc1 lds\n\t"
        "buffer_load_dwordx4 v107, %[ssrd], %[swx] offen offset:1024 sc1 lds\n\t"
        "Le0_%=:\n\t"
        "s_mov_b32 s92, 0xaaaaaaaa\n\t"
        "s_mov_b32 s93, 0xaaaaaaaa\n\t"
        "s_mov_b32 s94, 0xcccccccc\n\t"
        "s_mov_b32 s95, 0xcccccccc\n\t"
        "s_mov_b32 s96, 0xfff0ffff\n\t"
        "s_mov_b32 s97, 0xf000ff00\n\t"
        "s_mov_b32 s98, 0xffc0fffc\n\t"
        "s_mov_b32 s99, 0xc000fc00\n\t"
        "v_mov_b32 v23, 0\n\t"
        "v_mov_b32 v27, 0\n\t"
        "v_mov_b32 v31, 0\n\t"
        "v_mov_b32 v35, 0\n\t"
        "v_mov_b32 v39, 0\n\t"
        "v_mov_b32 v43, 0\n\t"
        "v_mov_b32 v60, %[sc0]\n\t"
        "v_mov_b32 v61, %[sc0]\n\t"
        "v_mov_b32 v62, %[sc0]\n\t"
        "v_mov_b32 v63, %[sc0]\n\t"
        "v_mov_b32 v68, %[sc0]\n\t"
        "v_mov_b32 v69, %[sc0]\n\t"
        "v_mov_b32 v70, %[sc0]\n\t"
        "v_mov_b32 v71, %[sc0]\n\t"
        "s_waitcnt lgkmcnt(10)\n\t"
        "v_mfma_f32_16x16x32_bf16 v[60:63], v[20:23], v[36:39], v[60:63]\n\t"
        "ds_read_b64_tr_b16 v[44:45], v104\n\t"
        "ds_read_b64_tr_b16 v[46:47], v104 offset:4864\n\t"
        "s_waitcnt lgkmcnt(10)\n\t"
        "v_mfma_f32_16x16x32_bf16 v[64:67], v[24:27], v[36:39], 0\n\t"
        "ds_read_b64_tr_b16 v[48:49], v104 offset:40\n\t"
        "ds_read_b64_tr_b16 v[50:51], v104 offset:4904\n\t"
        "s_waitcnt lgkmcnt(8)\n\t"
        "v_mfma_f32_16x16x32_bf16 v[68:71], v[28:31], v[40:43], v[68:71]\n\t"
        "s_waitcnt lgkmcnt(6)\n\t"
        "v_mfma_f32_16x16x32_bf16 v[72:75], v[32:35], v[40:43], 0\n\t"
        "s_cbranch_scc1 Le1_%=\n\t"
        "buffer_load_dwordx4 v107, %[ssrd], %[swx] offen offset:2048 sc1 lds\n\t"
        "buffer_load_dwordx4 v107, %[ssrd], %[swx] offen offset:3072 sc1 lds\n\t"
        "Le1_%=:\n\t"
        "s_andn2_b32 %[sc], 0x100, %[smsk]\n\t"
        "v_lshrrev_b32 v16, 4, v107\n\t"
        "s_add_u32 %[sc], %[sc], 162560\n\t"
        "v_and_b32 v16, 0x30, v16\n\t"
        "v_mov_b32 v8, %[sc]\n\t"
        "v_add_u32 v6, %[sc], v16\n\t"
        "v_add_u32 v12, 155168, v107\n\t"
        "ds_read_b64 v[4:5], v6 offset:8\n\t"
        "ds_read_b64 v[6:7], v6 offset:80\n\t"
        "ds_read_b96 v[8:10], v8 offset:64\n\t"
        "ds_read_b96 v[12:14], v12\n\t"
        "v_mov_b32 v56, 0x0c0c0100\n\t"
        "v_mov_b32 v57, 0x01000c0c\n\t"
        "v_mov_b32 v58, 0x0c0c0c0c\n\t"
        "v_cndmask_b32 v56, v56, v57, s[92:93]\n\t"
        "v_add_u32 v57, 0x0202, v56\n\t"
        "v_add_u32 v59, 0x02020000, v56\n\t"
        "v_cndmask_b32 v57, v57, v59, s[92:93]\n\t"
        "v_cndmask_b32 v76, v56, v58, s[96:97]\n\t"
        "v_cndmask_b32 v78, v58, v56, s[96:97]\n\t"
        "v_cndmask_b32 v77, v57, v58, s[98:99]\n\t"
        "v_cndmask_b32 v79, v58, v57, s[98:99]\n\t"
        "v_mov_b32 v17, 0x3f803f80\n\t"
        "v_cndmask_b32 v28, v60, v61, s[92:93]\n\t"
        "v_cndmask_b32 v36, v68, v69, s[92:93]\n\t"
        "v_cndmask_b32 v29, v62, v63, s[92:93]\n\t"
        "v_cndmask_b32 v37, v70, v71, s[92:93]\n\t"
        "v_cndmask_b32 v30, v64, v65, s[92:93]\n\t"
        "v_cndmask_b32 v38, v72, v73, s[92:93]\n\t"
        "v_cndmask_b32 v31, v66, v67, s[92:93]\n\t"
        "v_cndmask_b32 v39, v74, v75, s[92:93]\n\t"
        "v_cndmask_b32 v28, v28, v30, s[96:97]\n\t"
        "v_cndmask_b32 v36, v36, v38, s[96:97]\n\t"
        "v_cndmask_b32 v29, v29, v31, s[98:99]\n\t"
        "v_cndmask_b32 v37, v37, v39, s[98:99]\n\t"
        "v_max_f32 v30, v28, v29\n\t"
        "v_max_f32 v38, v36, v37\n\t"
        "v_max_f32 v31, v28, v29\n\t"
        "v_max_f32 v39, v36, v37\n\t"
        "s_waitcnt lgkmcnt(8)\n\t"
        "v_permlane16_swap_b32 v30, v31\n\t"
        "v_permlane16_swap_b32 v38, v39\n\t"
        "v_max_f32 v32, v30, v31\n\t"
        "v_max_f32 v40, v38, v39\n\t"
        "v_max_f32 v33, v30, v31\n\t"
        "v_max_f32 v41, v38, v39\n\t"
        "v_cndmask_b32 v56, v17, v52, s[94:95]\n\t"
        "v_permlane32_swap_b32 v32, v33\n\t"
        "v_permlane32_swap_b32 v40, v41\n\t"
        "v_max_f32 v30, v32, v33\n\t"
        "v_max_f32 v38, v40, v41\n\t"
        "v_sub_f32 v28, v28, v30\n\t"
        "v_sub_f32 v36, v36, v38\n\t"
        "v_sub_f32 v29, v29, v30\n\t"
        "v_sub_f32 v37, v37, v38\n\t"
        "v_mul_f32 v28, 0x3eae1a93, v28\n\t"
        "v_mul_f32 v36, 0x3eae1a93, v36\n\t"
        "v_mul_f32 v29, 0x3eae1a93, v29\n\t"
        "v_mul_f32 v37, 0x3eae1a93, v37\n\t"
        "v_exp_f32 v28, v28\n\t"
        "v_exp_f32 v36, v36\n\t"
        "v_exp_f32 v29, v29\n\t"
        "v_exp_f32 v37, v37\n\t"
        "v_cndmask_b32 v57, v17, v53, s[94:95]\n\t"
        "v_cndmask_b32 v58, v17, v54, s[94:95]\n\t"
        "v_cndmask_b32 v59, v17, v55, s[94:95]\n\t"
        "v_cndmask_b32 v52, v52, v17, s[94:95]\n\t"
        "v_cndmask_b32 v53, v53, v17, s[94:95]\n\t"
        "v_cndmask_b32 v54, v54, v17, s[94:95]\n\t"
        "v_cndmask_b32 v55, v55, v17, s[94:95]\n\t"
        "v_cvt_pk_bf16_f32 v28, v28, v29\n\t"
        "v_cvt_pk_bf16_f32 v36, v36, v37\n\t"
        "v_perm_b32 v20, v28, v28, v76\n\t"
        "v_perm_b32 v21, v28, v28, v77\n\t"
        "v_perm_b32 v22, v28, v28, v78\n\t"
        "v_perm_b32 v23, v28, v28, v79\n\t"
        "v_perm_b32 v24, v36, v36, v76\n\t"
        "v_perm_b32 v25, v36, v36, v77\n\t"
        "v_perm_b32 v26, v36, v36, v78\n\t"
        "v_perm_b32 v27, v36, v36, v79\n\t"
        "s_waitcnt lgkmcnt(0)\n\t"
        "v_mfma_f32_16x16x32_bf16 v[64:67], v[52:55], v[20:23], 0\n\t"
        "v_mfma_f32_16x16x32_bf16 v[60:63], v[44:47], v[20:23], 0\n\t"
        "v_mfma_f32_16x16x32_bf16 v[72:75], v[56:59], v[24:27], 0\n\t"
        "v_mfma_f32_16x16x32_bf16 v[68:71], v[48:51], v[24:27], 0\n\t"
        "v_add_u32 v16, 163232, v16\n\t"
        "v_mov_b32 v39, 163424\n\t"
        "s_mov_b32 vcc_lo, 0x22222222\n\t"
        "s_mov_b32 vcc_hi, 0x22222222\n\t"
        "v_cndmask_b32 v16, v39, v16, vcc\n\t"
        "ds_read_b128 v[28:31], v16 offset:0\n\t"
        "ds_read_b128 v[32:35], v16 offset:64\n\t"
        "ds_read_b128 v[40:43], v16 offset:128\n\t"
        "ds_read_b32 v39, v39 offset:144\n\t"
        "v_mov_b32 v86, 0\n\t"
        "v_mov_b32 v87, 0\n\t"
        "v_rcp_f32 v36, v66\n\t"
        "s_nop 0\n\t"
        "v_mul_f32 v60, v60, v36\n\t"
        "v_mul_f32 v61, v61, v36\n\t"
        "v_mul_f32 v62, v62, v36\n\t"
        "v_mul_f32 v63, v63, v36\n\t"
        "v_rcp_f32 v37, v72\n\t"
        "v_cvt_pk_bf16_f32 v80, v60, v61\n\t"
        "v_cvt_pk_bf16_f32 v81, v62, v63\n\t"
        "v_mul_f32 v68, v68, v37\n\t"
        "v_mul_f32 v69, v69, v37\n\t"
        "v_mul_f32 v70, v70, v37\n\t"
        "v_mul_f32 v71, v71, v37\n\t"
        "v_min_u32 v80, -2, v80\n\t"
        "v_min_u32 v81, -2, v81\n\t"
        "v_cvt_pk_bf16_f32 v82, v68, v69\n\t"
        "v_cvt_pk_bf16_f32 v83, v70, v71\n\t"
        "v_mul_f32 v64, v64, v36\n\t"
        "v_mul_f32 v65, v65, v36\n\t"
        "v_min_u32 v82, -2, v82\n\t"
        "v_min_u32 v83, -2, v83\n\t"
        "buffer_store_dwordx4 v[80:83], v107, %[ssrd], %[sst] offen sc1\n\t"
        "v_mul_f32 v74, v74, v37\n\t"
        "v_mul_f32 v75, v75, v37\n\t"
        "v_mfma_f32_16x16x32_bf16 v[52:55], v[80:83], v[80:83], 0\n\t"
        "v_cvt_pk_bf16_f32 v84, v64, v65\n\t"
        "v_cvt_pk_bf16_f32 v85, v74, v75\n\t"
        "v_mfma_f32_16x16x32_bf16 v[44:47], v[4:7], v[80:83], 0\n\t"
        "v_min_u32 v84, -2, v84\n\t"
        "v_min_u32 v85, -2, v85\n\t"
        "v_mov_b32 v11, 0\n\t"
        "s_cmp_eq_u32 %[smsk], 0\n\t"
        "v_mov_b32 v98, 0\n\t"
        "v_mov_b32 v99, 0\n\t"
        "v_mfma_f32_16x16x32_bf16 v[44:47], v[8:11], v[84:87], v[44:47]\n\t"
        "v_cndmask_b32 v52, v52, v53, s[92:93]\n\t"
        "v_cndmask_b32 v54, v54, v55, s[92:93]\n\t"
        "s_waitcnt lgkmcnt(0)\n\t"
        "s_cbranch_scc1 Lf1_%=\n\t"
        "v_mfma_f32_16x16x32_bf16 v[56:59], v[28:31], v[88:91], 0\n\t"
        "v_mfma_f32_16x16x32_bf16 v[56:59], v[32:35], v[92:95], v[56:59]\n\t"
        "v_mfma_f32_16x16x32_bf16 v[56:59], v[40:43], v[96:99], v[56:59]\n\t"
        "v_cndmask_b32 v52, v52, v54, s[94:95]\n\t"
        "v_and_b32 v17, 0xf0, v107\n\t"
        "v_mul_f32 v52, v52, v10\n\t"
        "s_mov_b32 exec_lo, 0xf0000f\n\t"
        "s_mov_b32 exec_hi, 0xf0000f00\n\t"
        "v_min_u32 v83, -2, v52\n\t"
        "s_nop 1\n\t"
        "v_fma_f32 v56, -v39, v56, v57\n\t"
        "v_add_f32 v44, v44, v56\n\t"
        "v_min_u32 v82, -2, v44\n\t"
        "buffer_store_dwordx4 v[82:85], v17, %[ssrd], %[stt] offen sc1\n\t"
        "s_branch Lf2_%=\n\t"
        "Lf1_%=:\n\t"
        "v_cndmask_b32 v52, v52, v54, s[94:95]\n\t"
        "v_and_b32 v17, 0xf0, v107\n\t"
        "v_mul_f32 v52, v52, v10\n\t"
        "s_mov_b32 exec_lo, 0xf0000f\n\t"
        "s_mov_b32 exec_hi, 0xf0000f00\n\t"
        "v_min_u32 v87, -2, v52\n\t"
        "v_min_u32 v86, -2, v44\n\t"
        "buffer_store_dwordx4 v[84:87], v17, %[ssrd], %[stt] offen sc1\n\t"
        "s_setprio 1\n\t"
        "Lf2_%=:\n\t"
        "s_mov_b64 exec, -1\n\t"
        "s_mov_b64 s[96:97], %[sgb]\n\t"
        "s_mov_b32 s98, 589824\n\t"
        "s_mov_b32 s99, 0x20000\n\t"
        "s_mov_b32 m0, %[sgl]\n\t"
        "s_add_u32 %[sob4], %[sob], 4096\n\t"
        "s_add_u32 %[sob4x], %[swi], 4096\n\t"
        "s_cmp_eq_u32 %[sgl], 0\n\t"
        "s_waitcnt vmcnt(2)\n\t"
        "s_barrier\n\t"
        "s_cbranch_scc1 L5_%=\n\t"
        "buffer_load_dwordx4 v12, s[96:99], 0 offen offset:0 lds\n\t"
        "buffer_load_dwordx4 v13, s[96:99], 0 offen offset:1024 lds\n\t"
        "buffer_load_dwordx4 v14, s[96:99], 0 offen offset:2048 lds\n\t"
        "L5_%=:\n\t"
        "s_mov_b32 m0, %[sld]\n\t"
        "s_cmp_eq_u32 %[sskip], 1\n\t"
        "s_cbranch_scc1 L6_%=\n\t"
        "buffer_load_dwordx4 v107, %[ssrd], %[swi] offen offset:0 sc1 lds\n\t"
        "buffer_load_dwordx4 v107, %[ssrd], %[swi] offen offset:1024 sc1 lds\n\t"
        "buffer_load_dwordx4 v107, %[ssrd], %[swi] offen offset:2048 sc1 lds\n\t"
        "buffer_load_dwordx4 v107, %[ssrd], %[swi] offen offset:3072 sc1 lds\n\t"
        "s_add_u32 m0, m0, 4096\n\t"
        "s_andn2_b32 %[sc], 0x1800, %[sstat]\n\t"
        "buffer_load_dwordx4 v107, %[ssrd], %[sob4x] offen offset:0 sc1 lds\n\t"
        "buffer_load_dwordx4 v107, %[ssrd], %[sob4x] offen offset:1024 sc1 lds\n\t"
        "s_cbranch_scc1 L6_%=\n\t"
        "buffer_load_dwordx4 v107, %[ssrd], %[sob4x] offen offset:2048 sc1 lds\n\t"
        "L6_%=:\n\t"
        "s_cmp_eq_u32 %[smsk], 0\n\t"
        "s_cbranch_scc0 Lk0_%=\n\t"
        "s_cmp_lg_u32 %[sskip], 1\n\t"
        "s_cbranch_scc1 L9_%=\n\t"
        "s_sleep 3\n\t"
        "L9_%=:\n\t"
        "s_cmp_ge_u32 %[sld], 18432\n\t"
        "s_cselect_b32 s98, 1, 0\n\t"
        "buffer_load_dwordx4 v[4:7], v108, %[ssrd], %[sob] offen offset:0 sc1\n\t"
        "buffer_load_dwordx4 v[8:11], v108, %[ssrd], %[sob] offen offset:256 sc1\n\t"
        "buffer_load_dwordx4 v[68:71], v108, %[ssrd], %[sob] offen offset:2048 sc1\n\t"
        "buffer_load_dwordx4 v[72:75], v108, %[ssrd], %[sob] offen offset:2304 sc1\n\t"
        "buffer_load_dwordx4 v[40:43], v107, %[ssrd], %[sob] offen offset:0 sc1\n\t"
        "buffer_load_dwordx4 v[44:47], v107, %[ssrd], %[sob] offen offset:1024 sc1\n\t"
        "buffer_load_dwordx4 v[48:51], v107, %[ssrd], %[sob] offen offset:2048 sc1\n\t"
        "buffer_load_dwordx4 v[52:55], v107, %[ssrd], %[sob] offen offset:3072 sc1\n\t"
        "buffer_load_dwordx4 v[56:59], v107, %[ssrd], %[sob4] offen offset:0 sc1\n\t"
        "buffer_load_dwordx4 v[60:63], v107, %[ssrd], %[sob4] offen offset:1024 sc1\n\t"
        "buffer_load_dwordx4 v[64:67], v107, %[ssrd], %[sob4] offen offset:2048 sc1\n\t"
        "s_lshr_b32 s96, %[sstat], 9\n\t"
        "s_and_b32 s96, s96, 12\n\t"
        "v_bfe_u32 v13, v107, 4, 4\n\t"
        "s_add_u32 s97, s96, 153984\n\t"
        "v_mov_b32 v3, s97\n\t"
        "s_lshl_b32 s96, s96, 4\n\t"
        "s_add_u32 s96, s96, 163572\n\t"
        "v_lshl_add_u32 v12, v13, 2, s96\n\t"
        "ds_read_b32 v12, v12\n\t"
        "s_lshr_b32 s96, %[sstat], 7\n\t"
        "s_and_b32 s96, s96, 0x7f0\n\t"
        "s_xor_b32 s96, s96, 0x400\n\t"
        "v_xnor_b32 v13, s96, v13\n\t"
        "s_movk_i32 s96, 0x7ff\n\t"
        "s_lshl_b32 s97, %[swt], 6\n\t"
        "v_lshl_add_u32 v2, v107, 6, %[sxf]\n\t"
        "v_mov_b32 v15, 153984\n\t"
        "v_mov_b32 v16, 1\n\t"
        "v_add_u32 v18, 75008, v107\n\t"
        "v_add_u32 v14, 75008, v106\n\t"
        "ds_read_b128 v[76:79], v18 offset:0\n\t"
        "ds_read_b128 v[80:83], v18 offset:1024\n\t"
        "ds_read_b128 v[84:87], v18 offset:2048\n\t"
        "ds_read_b128 v[88:91], v18 offset:3072\n\t"
        "ds_read_b128 v[92:95], v14 offset:0\n\t"
        "ds_read_b128 v[96:99], v18 offset:4608\n\t"
        "s_waitcnt vmcnt(11)\n\t"
        "s_mov_b64 exec, 1\n\t"
        "ds_add_u32 v15, v16 offset:60\n\t"
        "s_mov_b64 exec, -1\n\t"
        "s_waitcnt vmcnt(6)\n\t"
        "v_max3_u32 v17, v40, v41, v42\n\t"
        "v_max3_u32 v19, v4, v5, v10\n\t"
        "v_max3_u32 v17, v17, v6, v7\n\t"
        "v_max3_u32 v19, v19, v11, v68\n\t"
        "v_max3_u32 v17, v17, v8, v9\n\t"
        "v_max3_u32 v19, v19, v69, v74\n\t"
        "v_max3_u32 v17, v17, v70, v71\n\t"
        "v_max3_u32 v19, v19, v75, v73\n\t"
        "v_max3_u32 v17, v17, v72, v43\n\t"
        "v_max_u32 v17, v17, v19\n\t"
        "v_cmp_eq_u32 vcc, -1, v17\n\t"
        "s_cbranch_vccnz Lx01_%=\n\t"
        "Ld01_%=:\n\t"
        "s_waitcnt lgkmcnt(6)\n\t"
        "v_mfma_f32_16x16x32_bf16 v[20:23], v[76:79], v[40:43], 0\n\t"
        "v_add_f32 v4, v4, v10\n\t"
        "v_add_f32 v5, v5, v11\n\t"
        "v_add_f32 v68, v68, v74\n\t"
        "v_add_f32 v69, v69, v75\n\t"
        "v_add_f32 v10, v4, v68\n\t"
        "v_add_f32 v4, v4, v68\n\t"
        "v_add_f32 v11, v5, v69\n\t"
        "v_add_f32 v5, v5, v69\n\t"
        "v_permlane16_swap_b32 v10, v4\n\t"
        "s_nop 0\n\t"
        "v_permlane16_swap_b32 v11, v5\n\t"
        "v_add_f32 v68, v10, v4\n\t"
        "v_add_f32 v4, v10, v4\n\t"
        "v_add_f32 v69, v11, v5\n\t"
        "v_add_f32 v5, v11, v5\n\t"
        "ds_read_b128 v[76:79], v18 offset:5632\n\t"
        "s_waitcnt lgkmcnt(6)\n\t"
        "v_mfma_f32_16x16x32_bf16 v[24:27], v[80:83], v[40:43], 0\n\t"
        "v_permlane32_swap_b32 v69, v5\n\t"
        "v_permlane32_swap_b32 v68, v4\n\t"
        "v_add_f32 v5, v69, v5\n\t"
        "v_add_f32 v5, v5, v12\n\t"
        "v_rsq_f32 v5, v5\n\t"
        "v_add_f32 v4, v68, v4\n\t"
        "v_mul_f32 v4, v4, v5\n\t"
        "v_ashrrev_i32 v5, 31, v4\n\t"
        "v_or_b32 v5, 0x80000000, v5\n\t"
        "v_xor_b32 v4, v4, v5\n\t"
        "v_bfi_b32 v4, s96, v13, v4\n\t"
        "v_mov_b32 v5, %[stag]\n\t"
        "ds_read_b128 v[80:83], v18 offset:6656\n\t"
        "s_waitcnt lgkmcnt(6)\n\t"
        "v_mfma_f32_16x16x32_bf16 v[28:31], v[84:87], v[40:43], 0\n\t"
        "s_waitcnt vmcnt(5)\n\t"
        "v_max3_u32 v17, v44, v45, v46\n\t"
        "v_max_u32 v17, v17, v47\n\t"
        "v_cmp_eq_u32 vcc, -1, v17\n\t"
        "v_max_u32_dpp v4, v4, v4 row_ror:8 row_mask:0xf bank_mask:0xf\n\t"
        "s_nop 1\n\t"
        "v_max_u32_dpp v4, v4, v4 row_ror:4 row_mask:0xf bank_mask:0xf\n\t"
        "s_nop 1\n\t"
        "v_max_u32_dpp v4, v4, v4 row_ror:2 row_mask:0xf bank_mask:0xf\n\t"
        "s_nop 1\n\t"
        "v_max_u32_dpp v4, v4, v4 row_ror:1 row_mask:0xf bank_mask:0xf\n\t"
        "s_mov_b64 exec, 15\n\t"
        "buffer_store_dwordx2 v[4:5], v2, %[ssrd], 0 offen sc1\n\t"
        "v_and_or_b32 v10, v4, 15, s97\n\t"
        "ds_write_b32 v3, v10 offset:112\n\t"
        "s_mov_b64 exec, -1\n\t"
        "ds_read_b128 v[84:87], v18 offset:7680\n\t"
        "s_waitcnt lgkmcnt(7)\n\t"
        "v_mfma_f32_16x16x32_bf16 v[32:35], v[88:91], v[40:43], 0\n\t"
        "ds_read_b128 v[88:91], v14 offset:4608\n\t"
        "s_waitcnt lgkmcnt(7)\n\t"
        "v_mfma_f32_16x16x32_bf16 v[36:39], v[92:95], v[40:43], 0\n\t"
        "s_cbranch_vccnz Lx11_%=\n\t"
        "Ld11_%=:\n\t"
        "ds_read_b32 v16, v15 offset:60\n\t"
        "ds_read_b128 v[92:95], v18 offset:9216\n\t"
        "ds_read_b128 v[40:43], v18 offset:10240\n\t"
        "s_waitcnt lgkmcnt(9)\n\t"
        "v_mfma_f32_16x16x32_bf16 v[20:23], v[96:99], v[44:47], v[20:23]\n\t"
        "ds_read_b128 v[96:99], v18 offset:11264\n\t"
        "s_waitcnt lgkmcnt(8)\n\t"
        "v_mfma_f32_16x16x32_bf16 v[24:27], v[76:79], v[44:47], v[24:27]\n\t"
        "ds_read_b128 v[76:79], v18 offset:12288\n\t"
        "s_waitcnt lgkmcnt(8)\n\t"
        "v_mfma_f32_16x16x32_bf16 v[28:31], v[80:83], v[44:47], v[28:31]\n\t"
        "s_waitcnt vmcnt(5)\n\t"
        "v_max3_u32 v17, v48, v49, v50\n\t"
        "v_max_u32 v17, v17, v51\n\t"
        "v_cmp_eq_u32 vcc, -1, v17\n\t"
        "ds_read_b128 v[80:83], v14 offset:9216\n\t"
        "s_waitcnt lgkmcnt(7)\n\t"
        "v_mfma_f32_16x16x32_bf16 v[32:35], v[84:87], v[44:47], v[32:35]\n\t"
        "s_waitcnt lgkmcnt(5)\n\t"
        "Lwc10_%=:\n\t"
        "v_readfirstlane_b32 %[sob4x], v16\n\t"
        "s_cmp_lt_u32 %[sob4x], %[swt]\n\t"
        "s_cbranch_scc1 Lws10_%=\n\t"
        "Lwg10_%=:\n\t"
        "ds_read_b128 v[84:87], v18 offset:13824\n\t"
        "v_mfma_f32_16x16x32_bf16 v[36:39], v[88:91], v[44:47], v[36:39]\n\t"
        "s_cbranch_vccnz Lx21_%=\n\t"
        "Ld21_%=:\n\t"
        "ds_read_b128 v[88:91], v18 offset:14848\n\t"
        "ds_read_b128 v[44:47], v18 offset:15872\n\t"
        "s_waitcnt lgkmcnt(7)\n\t"
        "v_mfma_f32_16x16x32_bf16 v[20:23], v[92:95], v[48:51], v[20:23]\n\t"
        "ds_read_b128 v[92:95], v18 offset:16896\n\t"
        "s_waitcnt lgkmcnt(7)\n\t"
        "v_mfma_f32_16x16x32_bf16 v[24:27], v[40:43], v[48:51], v[24:27]\n\t"
        "ds_read_b128 v[40:43], v14 offset:13824\n\t"
        "s_waitcnt lgkmcnt(7)\n\t"
        "v_mfma_f32_16x16x32_bf16 v[28:31], v[96:99], v[48:51], v[28:31]\n\t"
        "s_waitcnt vmcnt(4)\n\t"
        "v_max3_u32 v17, v52, v53, v54\n\t"
        "v_max_u32 v17, v17, v55\n\t"
        "v_cmp_eq_u32 vcc, -1, v17\n\t"
        "ds_read_b128 v[96:99], v18 offset:18432\n\t"
        "s_waitcnt lgkmcnt(7)\n\t"
        "v_mfma_f32_16x16x32_bf16 v[32:35], v[76:79], v[48:51], v[32:35]\n\t"
        "ds_read_b128 v[76:79], v18 offset:19456\n\t"
        "s_waitcnt lgkmcnt(7)\n\t"
        "v_mfma_f32_16x16x32_bf16 v[36:39], v[80:83], v[48:51], v[36:39]\n\t"
        "s_cbranch_vccnz Lx31_%=\n\t"
        "Ld31_%=:\n\t"
        "ds_read_b128 v[80:83], v18 offset:20480\n\t"
        "ds_read_b128 v[48:51], v18 offset:21504\n\t"
        "s_waitcnt lgkmcnt(8)\n\t"
        "v_mfma_f32_16x16x32_bf16 v[20:23], v[84:87], v[52:55], v[20:23]\n\t"
        "ds_read_b128 v[84:87], v14 offset:18432\n\t"
        "s_waitcnt lgkmcnt(8)\n\t"
        "v_mfma_f32_16x16x32_bf16 v[24:27], v[88:91], v[52:55], v[24:27]\n\t"
        "ds_read_b128 v[88:91], v107 offset:0\n\t"
        "s_waitcnt lgkmcnt(8)\n\t"
        "v_mfma_f32_16x16x32_bf16 v[28:31], v[44:47], v[52:55], v[28:31]\n\t"
        "s_waitcnt vmcnt(3)\n\t"
        "v_max3_u32 v17, v56, v57, v58\n\t"
        "v_max_u32 v17, v17, v59\n\t"
        "v_cmp_eq_u32 vcc, -1, v17\n\t"
        "ds_read_b128 v[44:47], v107 offset:1024\n\t"
        "s_waitcnt lgkmcnt(8)\n\t"
        "v_mfma_f32_16x16x32_bf16 v[32:35], v[92:95], v[52:55], v[32:35]\n\t"
        "ds_read_b128 v[92:95], v107 offset:2048\n\t"
        "s_waitcnt lgkmcnt(8)\n\t"
        "v_mfma_f32_16x16x32_bf16 v[36:39], v[40:43], v[52:55], v[36:39]\n\t"
        "s_cbranch_vccnz Lx41_%=\n\t"
        "Ld41_%=:\n\t"
        "ds_read_b128 v[40:43], v107 offset:3072\n\t"
        "s_waitcnt lgkmcnt(8)\n\t"
        "v_mfma_f32_16x16x32_bf16 v[20:23], v[96:99], v[56:59], v[20:23]\n\t"
        "ds_read_b128 v[52:55], v106 offset:0\n\t"
        "s_waitcnt lgkmcnt(8)\n\t"
        "v_mfma_f32_16x16x32_bf16 v[24:27], v[76:79], v[56:59], v[24:27]\n\t"
        "ds_read_b128 v[96:99], v107 offset:4608\n\t"
        "s_waitcnt lgkmcnt(8)\n\t"
        "v_mfma_f32_16x16x32_bf16 v[28:31], v[80:83], v[56:59], v[28:31]\n\t"
        "s_waitcnt vmcnt(2)\n\t"
        "v_max3_u32 v17, v60, v61, v62\n\t"
        "v_max_u32 v17, v17, v63\n\t"
        "v_cmp_eq_u32 vcc, -1, v17\n\t"
        "ds_read_b128 v[76:79], v107 offset:5632\n\t"
        "s_waitcnt lgkmcnt(8)\n\t"
        "v_mfma_f32_16x16x32_bf16 v[32:35], v[48:51], v[56:59], v[32:35]\n\t"
        "ds_read_b128 v[80:83], v107 offset:6656\n\t"
        "s_waitcnt lgkmcnt(8)\n\t"
        "v_mfma_f32_16x16x32_bf16 v[36:39], v[84:87], v[56:59], v[36:39]\n\t"
        "s_cbranch_vccnz Lx51_%=\n\t"
        "Ld51_%=:\n\t"
        "ds_read_b128 v[48:51], v107 offset:7680\n\t"
        "s_waitcnt lgkmcnt(8)\n\t"
        "v_mfma_f32_16x16x32_bf16 v[20:23], v[88:91], v[60:63], v[20:23]\n\t"
        "ds_read_b128 v[84:87], v106 offset:4608\n\t"
        "s_waitcnt lgkmcnt(8)\n\t"
        "v_mfma_f32_16x16x32_bf16 v[24:27], v[44:47], v[60:63], v[24:27]\n\t"
        "ds_read_b128 v[56:59], v107 offset:9216\n\t"
        "s_waitcnt lgkmcnt(8)\n\t"
        "v_mfma_f32_16x16x32_bf16 v[28:31], v[92:95], v[60:63], v[28:31]\n\t"
        "s_waitcnt vmcnt(1)\n\t"
        "v_max3_u32 v17, v64, v65, v66\n\t"
        "v_max_u32 v17, v17, v67\n\t"
        "v_cmp_eq_u32 vcc, -1, v17\n\t"
        "ds_read_b128 v[88:91], v107 offset:10240\n\t"
        "s_waitcnt lgkmcnt(8)\n\t"
        "v_mfma_f32_16x16x32_bf16 v[32:35], v[40:43], v[60:63], v[32:35]\n\t"
        "ds_read_b128 v[44:47], v107 offset:11264\n\t"
        "s_waitcnt lgkmcnt(8)\n\t"
        "v_mfma_f32_16x16x32_bf16 v[36:39], v[52:55], v[60:63], v[36:39]\n\t"
        "s_cbranch_vccnz Lx61_%=\n\t"
        "Ld61_%=:\n\t"
        "ds_read_b128 v[92:95], v107 offset:12288\n\t"
        "s_waitcnt lgkmcnt(8)\n\t"
        "v_mfma_f32_16x16x32_bf16 v[20:23], v[96:99], v[64:67], v[20:23]\n\t"
        "ds_read_b128 v[40:43], v106 offset:9216\n\t"
        "s_waitcnt lgkmcnt(8)\n\t"
        "v_mfma_f32_16x16x32_bf16 v[24:27], v[76:79], v[64:67], v[24:27]\n\t"
        "ds_read_b128 v[52:55], v107 offset:13824\n\t"
        "s_waitcnt lgkmcnt(8)\n\t"
        "v_mfma_f32_16x16x32_bf16 v[28:31], v[80:83], v[64:67], v[28:31]\n\t"
        "ds_read_b128 v[60:63], v107 offset:14848\n\t"
        "s_waitcnt lgkmcnt(8)\n\t"
        "v_mfma_f32_16x16x32_bf16 v[32:35], v[48:51], v[64:67], v[32:35]\n\t"
        "ds_read_b128 v[96:99], v107 offset:15872\n\t"
        "s_waitcnt lgkmcnt(8)\n\t"
        "v_mfma_f32_16x16x32_bf16 v[36:39], v[84:87], v[64:67], v[36:39]\n\t"
        "ds_read_b128 v[76:79], v107 offset:16896\n\t"
        "s_waitcnt lgkmcnt(8)\n\t"
        "v_mfma_f32_16x16x32_bf16 v[20:23], v[56:59], v[6:9], v[20:23]\n\t"
        "ds_read_b128 v[80:83], v106 offset:13824\n\t"
        "s_waitcnt lgkmcnt(8)\n\t"
        "v_mfma_f32_16x16x32_bf16 v[24:27], v[88:91], v[6:9], v[24:27]\n\t"
        "s_waitcnt lgkmcnt(7)\n\t"
        "v_mfma_f32_16x16x32_bf16 v[28:31], v[44:47], v[6:9], v[28:31]\n\t"
        "s_waitcnt lgkmcnt(6)\n\t"
        "v_mfma_f32_16x16x32_bf16 v[32:35], v[92:95], v[6:9], v[32:35]\n\t"
        "s_waitcnt lgkmcnt(5)\n\t"
        "v_mfma_f32_16x16x32_bf16 v[36:39], v[40:43], v[6:9], v[36:39]\n\t"
        "s_waitcnt lgkmcnt(4)\n\t"
        "v_mfma_f32_16x16x32_bf16 v[20:23], v[52:55], v[70:73], v[20:23]\n\t"
        "s_waitcnt lgkmcnt(3)\n\t"
        "v_mfma_f32_16x16x32_bf16 v[24:27], v[60:63], v[70:73], v[24:27]\n\t"
        "v_add_u32 v18, %[sscr], v107\n\t"
        "s_waitcnt lgkmcnt(2)\n\t"
        "v_mfma_f32_16x16x32_bf16 v[28:31], v[96:99], v[70:73], v[28:31]\n\t"
        "s_waitcnt lgkmcnt(1)\n\t"
        "v_mfma_f32_16x16x32_bf16 v[32:35], v[76:79], v[70:73], v[32:35]\n\t"
        "s_waitcnt lgkmcnt(0)\n\t"
        "v_mfma_f32_16x16x32_bf16 v[36:39], v[80:83], v[70:73], v[36:39]\n\t"
        "v_mov_b32 v16, 1\n\t"
        "ds_write_b128 v18, v[20:23] offset:0\n\t"
        "ds_write_b128 v18, v[24:27] offset:1024\n\t"
        "s_nop 0\n\t"
        "ds_write_b128 v18, v[28:31] offset:2048\n\t"
        "s_nop 0\n\t"
        "ds_write_b128 v18, v[32:35] offset:3072\n\t"
        "s_mov_b32 exec_hi, 0\n\t"
        "ds_write_b128 v18, v[36:39] offset:4096\n\t"
        "s_mov_b32 exec_lo, 1\n\t"
        "ds_add_u32 v15, v16 offset:24\n\t"
        "s_mov_b64 exec, -1\n\t"
        "s_branch Lend2_%=\n\t"
        "Lx01_%=:\n\t"
        "s_cmp_eq_u32 %[scn], 0\n\t"
        "s_cbranch_scc1 Ld01_%=\n\t"
        "s_sub_u32 %[scn], %[scn], 1\n\t"
        "buffer_load_dwordx4 v[4:7], v108, %[ssrd], %[sob] offen offset:0 sc1\n\t"
        "buffer_load_dwordx4 v[8:11], v108, %[ssrd], %[sob] offen offset:256 sc1\n\t"
        "buffer_load_dwordx4 v[68:71], v108, %[ssrd], %[sob] offen offset:2048 sc1\n\t"
        "buffer_load_dwordx4 v[72:75], v108, %[ssrd], %[sob] offen offset:2304 sc1\n\t"
        "buffer_load_dwordx4 v[40:43], v107, %[ssrd], %[sob] offen offset:0 sc1\n\t"
        "buffer_load_dwordx4 v[44:47], v107, %[ssrd], %[sob] offen offset:1024 sc1\n\t"
        "buffer_load_dwordx4 v[48:51], v107, %[ssrd], %[sob] offen offset:2048 sc1\n\t"
        "buffer_load_dwordx4 v[52:55], v107, %[ssrd], %[sob] offen offset:3072 sc1\n\t"
        "buffer_load_dwordx4 v[56:59], v107, %[ssrd], %[sob4] offen offset:0 sc1\n\t"
        "buffer_load_dwordx4 v[60:63], v107, %[ssrd], %[sob4] offen offset:1024 sc1\n\t"
        "buffer_load_dwordx4 v[64:67], v107, %[ssrd], %[sob4] offen offset:2048 sc1\n\t"
        "s_mov_b32 s98, 1\n\t"
        "s_waitcnt vmcnt(6)\n\t"
        "v_max3_u32 v17, v40, v41, v42\n\t"
        "v_max3_u32 v19, v4, v5, v10\n\t"
        "v_max3_u32 v17, v17, v6, v7\n\t"
        "v_max3_u32 v19, v19, v11, v68\n\t"
        "v_max3_u32 v17, v17, v8, v9\n\t"
        "v_max3_u32 v19, v19, v69, v74\n\t"
        "v_max3_u32 v17, v17, v70, v71\n\t"
        "v_max3_u32 v19, v19, v75, v73\n\t"
        "v_max3_u32 v17, v17, v72, v43\n\t"
        "v_max_u32 v17, v17, v19\n\t"
        "v_cmp_eq_u32 vcc, -1, v17\n\t"
        "s_cbranch_vccnz Lx01_%=\n\t"
        "s_branch Ld01_%=\n\t"
        "Lx11_%=:\n\t"
        "s_cmp_eq_u32 %[scn], 0\n\t"
        "s_cbranch_scc1 Ld11_%=\n\t"
        "s_sub_u32 %[scn], %[scn], 1\n\t"
        "buffer_load_dwordx4 v[44:47], v107, %[ssrd], %[sob] offen offset:1024 sc1\n\t"
        "buffer_load_dwordx4 v[48:51], v107, %[ssrd], %[sob] offen offset:2048 sc1\n\t"
        "buffer_load_dwordx4 v[52:55], v107, %[ssrd], %[sob] offen offset:3072 sc1\n\t"
        "buffer_load_dwordx4 v[56:59], v107, %[ssrd], %[sob4] offen offset:0 sc1\n\t"
        "buffer_load_dwordx4 v[60:63], v107, %[ssrd], %[sob4] offen offset:1024 sc1\n\t"
        "buffer_load_dwordx4 v[64:67], v107, %[ssrd], %[sob4] offen offset:2048 sc1\n\t"
        "s_mov_b32 s98, 1\n\t"
        "s_mov_b64 exec, 15\n\t"
        "buffer_store_dwordx2 v[4:5], v2, %[ssrd], 0 offen sc1\n\t"
        "s_mov_b64 exec, -1\n\t"
        "s_waitcnt vmcnt(6)\n\t"
        "v_max3_u32 v17, v44, v45, v46\n\t"
        "v_max_u32 v17, v17, v47\n\t"
        "v_cmp_eq_u32 vcc, -1, v17\n\t"
        "s_cbranch_vccnz Lx11_%=\n\t"
        "s_branch Ld11_%=\n\t"
        "Lx21_%=:\n\t"
        "s_cmp_eq_u32 %[scn], 0\n\t"
        "s_cbranch_scc1 Ld21_%=\n\t"
        "s_sub_u32 %[scn], %[scn], 1\n\t"
        "buffer_load_dwordx4 v[48:51], v107, %[ssrd], %[sob] offen offset:2048 sc1\n\t"
        "buffer_load_dwordx4 v[52:55], v107, %[ssrd], %[sob] offen offset:3072 sc1\n\t"
        "buffer_load_dwordx4 v[56:59], v107, %[ssrd], %[sob4] offen offset:0 sc1\n\t"
        "buffer_load_dwordx4 v[60:63], v107, %[ssrd], %[sob4] offen offset:1024 sc1\n\t"
        "buffer_load_dwordx4 v[64:67], v107, %[ssrd], %[sob4] offen offset:2048 sc1\n\t"
        "s_mov_b32 s98, 1\n\t"
        "s_mov_b64 exec, 15\n\t"
        "buffer_store_dwordx2 v[4:5], v2, %[ssrd], 0 offen sc1\n\t"
        "s_mov_b64 exec, -1\n\t"
        "s_waitcnt vmcnt(5)\n\t"
        "v_max3_u32 v17, v48, v49, v50\n\t"
        "v_max_u32 v17, v17, v51\n\t"
        "v_cmp_eq_u32 vcc, -1, v17\n\t"
        "s_cbranch_vccnz Lx21_%=\n\t"
        "s_branch Ld21_%=\n\t"
        "Lx31_%=:\n\t"
        "s_cmp_eq_u32 %[scn], 0\n\t"
        "s_cbranch_scc1 Ld31_%=\n\t"
        "s_sub_u32 %[scn], %[scn], 1\n\t"
        "buffer_load_dwordx4 v[52:55], v107, %[ssrd], %[sob] offen offset:3072 sc1\n\t"
        "buffer_load_dwordx4 v[56:59], v107, %[ssrd], %[sob4] offen offset:0 sc1\n\t"
        "buffer_load_dwordx4 v[60:63], v107, %[ssrd], %[sob4] offen offset:1024 sc1\n\t"
        "buffer_load_dwordx4 v[64:67], v107, %[ssrd], %[sob4] offen offset:2048 sc1\n\t"
        "s_mov_b32 s98, 1\n\t"
        "s_mov_b64 exec, 15\n\t"
        "buffer_store_dwordx2 v[4:5], v2, %[ssrd], 0 offen sc1\n\t"
        "s_mov_b64 exec, -1\n\t"
        "s_waitcnt vmcnt(4)\n\t"
        "v_max3_u32 v17, v52, v53, v54\n\t"
        "v_max_u32 v17, v17, v55\n\t"
        "v_cmp_eq_u32 vcc, -1, v17\n\t"
        "s_cbranch_vccnz Lx31_%=\n\t"
        "s_branch Ld31_%=\n\t"
        "Lx41_%=:\n\t"
        "s_cmp_eq_u32 %[scn], 0\n\t"
        "s_cbranch_scc1 Ld41_%=\n\t"
        "s_sub_u32 %[scn], %[scn], 1\n\t"
        "buffer_load_dwordx4 v[56:59], v107, %[ssrd], %[sob4] offen offset:0 sc1\n\t"
        "buffer_load_dwordx4 v[60:63], v107, %[ssrd], %[sob4] offen offset:1024 sc1\n\t"
        "buffer_load_dwordx4 v[64:67], v107, %[ssrd], %[sob4] offen offset:2048 sc1\n\t"
        "s_mov_b32 s98, 1\n\t"
        "s_mov_b64 exec, 15\n\t"
        "buffer_store_dwordx2 v[4:5], v2, %[ssrd], 0 offen sc1\n\t"
        "s_mov_b64 exec, -1\n\t"
        "s_waitcnt vmcnt(3)\n\t"
        "v_max3_u32 v17, v56, v57, v58\n\t"
        "v_max_u32 v17, v17, v59\n\t"
        "v_cmp_eq_u32 vcc, -1, v17\n\t"
        "s_cbranch_vccnz Lx41_%=\n\t"
        "s_branch Ld41_%=\n\t"
        "Lx51_%=:\n\t"
        "s_cmp_eq_u32 %[scn], 0\n\t"
        "s_cbranch_scc1 Ld51_%=\n\t"
        "s_sub_u32 %[scn], %[scn], 1\n\t"
        "buffer_load_dwordx4 v[60:63], v107, %[ssrd], %[sob4] offen offset:1024 sc1\n\t"
        "buffer_load_dwordx4 v[64:67], v107, %[ssrd], %[sob4] offen offset:2048 sc1\n\t"
        "s_mov_b32 s98, 1\n\t"
        "s_mov_b64 exec, 15\n\t"
        "buffer_store_dwordx2 v[4:5], v2, %[ssrd], 0 offen sc1\n\t"
        "s_mov_b64 exec, -1\n\t"
        "s_waitcnt vmcnt(2)\n\t"
        "v_max3_u32 v17, v60, v61, v62\n\t"
        "v_max_u32 v17, v17, v63\n\t"
        "v_cmp_eq_u32 vcc, -1, v17\n\t"
        "s_cbranch_vccnz Lx51_%=\n\t"
        "s_branch Ld51_%=\n\t"
        "Lx61_%=:\n\t"
        "s_cmp_eq_u32 %[scn], 0\n\t"
        "s_cbranch_scc1 Ld61_%=\n\t"
        "s_sub_u32 %[scn], %[scn], 1\n\t"
        "buffer_load_dwordx4 v[64:67], v107, %[ssrd], %[sob4] offen offset:2048 sc1\n\t"
        "s_mov_b32 s98, 1\n\t"
        "s_mov_b64 exec, 15\n\t"
        "buffer_store_dwordx2 v[4:5], v2, %[ssrd], 0 offen sc1\n\t"
        "s_mov_b64 exec, -1\n\t"
        "s_waitcnt vmcnt(1)\n\t"
        "v_max3_u32 v17, v64, v65, v66\n\t"
        "v_max_u32 v17, v17, v67\n\t"
        "v_cmp_eq_u32 vcc, -1, v17\n\t"
        "s_cbranch_vccnz Lx61_%=\n\t"
        "s_branch Ld61_%=\n\t"
        "Lws10_%=:\n\t"
        "s_cmp_eq_u32 %[scn], 0\n\t"
        "s_cbranch_scc1 Lwg10_%=\n\t"
        "s_sub_u32 %[scn], %[scn], 1\n\t"
        "s_sleep 1\n\t"
        "ds_read_b32 v16, v15 offset:60\n\t"
        "s_waitcnt lgkmcnt(0)\n\t"
        "s_branch Lwc10_%=\n\t"
        "Lx00_%=:\n\t"
        "s_cmp_eq_u32 %[scn], 0\n\t"
        "s_cbranch_scc1 Ld00_%=\n\t"
        "s_sub_u32 %[scn], %[scn], 1\n\t"
        "buffer_load_dwordx4 v[40:43], v107, %[ssrd], %[sob] offen offset:0 sc1\n\t"
        "buffer_load_dwordx4 v[44:47], v107, %[ssrd], %[sob] offen offset:1024 sc1\n\t"
        "buffer_load_dwordx4 v[48:51], v107, %[ssrd], %[sob] offen offset:2048 sc1\n\t"
        "buffer_load_dwordx4 v[52:55], v107, %[ssrd], %[sob] offen offset:3072 sc1\n\t"
        "buffer_load_dwordx4 v[56:59], v107, %[ssrd], %[sob4] offen offset:0 sc1\n\t"
        "buffer_load_dwordx4 v[60:63], v107, %[ssrd], %[sob4] offen offset:1024 sc1\n\t"
        "buffer_load_dwordx4 v[64:67], v107, %[ssrd], %[sob4] offen offset:2048 sc1\n\t"
        "buffer_load_dwordx4 v[6:9], v108, %[ssrd], %[sob] offen offset:0 sc1\n\t"
        "buffer_load_dwordx4 v[70:73], v108, %[ssrd], %[sob] offen offset:1024 sc1\n\t"
        "s_waitcnt vmcnt(8)\n\t"
        "v_max3_u32 v17, v40, v41, v42\n\t"
        "v_max_u32 v17, v17, v43\n\t"
        "v_cmp_eq_u32 vcc, -1, v17\n\t"
        "s_cbranch_vccnz Lx00_%=\n\t"
        "s_branch Ld00_%=\n\t"
        "Lx10_%=:\n\t"
        "s_cmp_eq_u32 %[scn], 0\n\t"
        "s_cbranch_scc1 Ld10_%=\n\t"
        "s_sub_u32 %[scn], %[scn], 1\n\t"
        "buffer_load_dwordx4 v[44:47], v107, %[ssrd], %[sob] offen offset:1024 sc1\n\t"
        "buffer_load_dwordx4 v[48:51], v107, %[ssrd], %[sob] offen offset:2048 sc1\n\t"
        "buffer_load_dwordx4 v[52:55], v107, %[ssrd], %[sob] offen offset:3072 sc1\n\t"
        "buffer_load_dwordx4 v[56:59], v107, %[ssrd], %[sob4] offen offset:0 sc1\n\t"
        "buffer_load_dwordx4 v[60:63], v107, %[ssrd], %[sob4] offen offset:1024 sc1\n\t"
        "buffer_load_dwordx4 v[64:67], v107, %[ssrd], %[sob4] offen offset:2048 sc1\n\t"
        "buffer_load_dwordx4 v[6:9], v108, %[ssrd], %[sob] offen offset:0 sc1\n\t"
        "buffer_load_dwordx4 v[70:73], v108, %[ssrd], %[sob] offen offset:1024 sc1\n\t"
        "s_waitcnt vmcnt(7)\n\t"
        "v_max3_u32 v17, v44, v45, v46\n\t"
        "v_max_u32 v17, v17, v47\n\t"
        "v_cmp_eq_u32 vcc, -1, v17\n\t"
        "s_cbranch_vccnz Lx10_%=\n\t"
        "s_branch Ld10_%=\n\t"
        "Lx20_%=:\n\t"
        "s_cmp_eq_u32 %[scn], 0\n\t"
        "s_cbranch_scc1 Ld20_%=\n\t"
        "s_sub_u32 %[scn], %[scn], 1\n\t"
        "buffer_load_dwordx4 v[48:51], v107, %[ssrd], %[sob] offen offset:2048 sc1\n\t"
        "buffer_load_dwordx4 v[52:55], v107, %[ssrd], %[sob] offen offset:3072 sc1\n\t"
        "buffer_load_dwordx4 v[56:59], v107, %[ssrd], %[sob4] offen offset:0 sc1\n\t"
        "buffer_load_dwordx4 v[60:63], v107, %[ssrd], %[sob4] offen offset:1024 sc1\n\t"
        "buffer_load_dwordx4 v[64:67], v107, %[ssrd], %[sob4] offen offset:2048 sc1\n\t"
        "buffer_load_dwordx4 v[6:9], v108, %[ssrd], %[sob] offen offset:0 sc1\n\t"
        "buffer_load_dwordx4 v[70:73], v108, %[ssrd], %[sob] offen offset:1024 sc1\n\t"
        "s_waitcnt vmcnt(6)\n\t"
        "v_max3_u32 v17, v48, v49, v50\n\t"
        "v_max_u32 v17, v17, v51\n\t"
        "v_cmp_eq_u32 vcc, -1, v17\n\t"
        "s_cbranch_vccnz Lx20_%=\n\t"
        "s_branch Ld20_%=\n\t"
        "Lx30_%=:\n\t"
        "s_cmp_eq_u32 %[scn], 0\n\t"
        "s_cbranch_scc1 Ld30_%=\n\t"
        "s_sub_u32 %[scn], %[scn], 1\n\t"
        "buffer_load_dwordx4 v[52:55], v107, %[ssrd], %[sob] offen offset:3072 sc1\n\t"
        "buffer_load_dwordx4 v[56:59], v107, %[ssrd], %[sob4] offen offset:0 sc1\n\t"
        "buffer_load_dwordx4 v[60:63], v107, %[ssrd], %[sob4] offen offset:1024 sc1\n\t"
        "buffer_load_dwordx4 v[64:67], v107, %[ssrd], %[sob4] offen offset:2048 sc1\n\t"
        "buffer_load_dwordx4 v[6:9], v108, %[ssrd], %[sob] offen offset:0 sc1\n\t"
        "buffer_load_dwordx4 v[70:73], v108, %[ssrd], %[sob] offen offset:1024 sc1\n\t"
        "s_waitcnt vmcnt(5)\n\t"
        "v_max3_u32 v17, v52, v53, v54\n\t"
        "v_max_u32 v17, v17, v55\n\t"
        "v_cmp_eq_u32 vcc, -1, v17\n\t"
        "s_cbranch_vccnz Lx30_%=\n\t"
        "s_branch Ld30_%=\n\t"
        "Lx40_%=:\n\t"
        "s_cmp_eq_u32 %[scn], 0\n\t"
        "s_cbranch_scc1 Ld40_%=\n\t"
        "s_sub_u32 %[scn], %[scn], 1\n\t"
        "buffer_load_dwordx4 v[56:59], v107, %[ssrd], %[sob4] offen offset:0 sc1\n\t"
        "buffer_load_dwordx4 v[60:63], v107, %[ssrd], %[sob4] offen offset:1024 sc1\n\t"
        "buffer_load_dwordx4 v[64:67], v107, %[ssrd], %[sob4] offen offset:2048 sc1\n\t"
        "buffer_load_dwordx4 v[6:9], v108, %[ssrd], %[sob] offen offset:0 sc1\n\t"
        "buffer_load_dwordx4 v[70:73], v108, %[ssrd], %[sob] offen offset:1024 sc1\n\t"
        "s_waitcnt vmcnt(4)\n\t"
        "v_max3_u32 v17, v56, v57, v58\n\t"
        "v_max_u32 v17, v17, v59\n\t"
        "v_cmp_eq_u32 vcc, -1, v17\n\t"
        "s_cbranch_vccnz Lx40_%=\n\t"
        "s_branch Ld40_%=\n\t"
        "Lx50_%=:\n\t"
        "s_cmp_eq_u32 %[scn], 0\n\t"
        "s_cbranch_scc1 Ld50_%=\n\t"
        "s_sub_u32 %[scn], %[scn], 1\n\t"
        "buffer_load_dwordx4 v[60:63], v107, %[ssrd], %[sob4] offen offset:1024 sc1\n\t"
        "buffer_load_dwordx4 v[64:67], v107, %[ssrd], %[sob4] offen offset:2048 sc1\n\t"
        "buffer_load_dwordx4 v[6:9], v108, %[ssrd], %[sob] offen offset:0 sc1\n\t"
        "buffer_load_dwordx4 v[70:73], v108, %[ssrd], %[sob] offen offset:1024 sc1\n\t"
        "s_waitcnt vmcnt(3)\n\t"
        "v_max3_u32 v17, v60, v61, v62\n\t"
        "v_max_u32 v17, v17, v63\n\t"
        "v_cmp_eq_u32 vcc, -1, v17\n\t"
        "s_cbranch_vccnz Lx50_%=\n\t"
        "s_branch Ld50_%=\n\t"
        "Lx60_%=:\n\t"
        "s_cmp_eq_u32 %[scn], 0\n\t"
        "s_cbranch_scc1 Ld60_%=\n\t"
        "s_sub_u32 %[scn], %[scn], 1\n\t"
        "buffer_load_dwordx4 v[64:67], v107, %[ssrd], %[sob4] offen offset:2048 sc1\n\t"
        "buffer_load_dwordx4 v[6:9], v108, %[ssrd], %[sob] offen offset:0 sc1\n\t"
        "buffer_load_dwordx4 v[70:73], v108, %[ssrd], %[sob] offen offset:1024 sc1\n\t"
        "s_waitcnt vmcnt(2)\n\t"
        "v_max3_u32 v17, v64, v65, v66\n\t"
        "v_max_u32 v17, v17, v67\n\t"
        "v_cmp_eq_u32 vcc, -1, v17\n\t"
        "s_cbranch_vccnz Lx60_%=\n\t"
        "s_branch Ld60_%=\n\t"
        "Lx70_%=:\n\t"
        "s_cmp_eq_u32 %[scn], 0\n\t"
        "s_cbranch_scc1 Ld70_%=\n\t"
        "s_sub_u32 %[scn], %[scn], 1\n\t"
        "buffer_load_dwordx4 v[6:9], v108, %[ssrd], %[sob] offen offset:0 sc1\n\t"
        "buffer_load_dwordx4 v[70:73], v108, %[ssrd], %[sob] offen offset:1024 sc1\n\t"
        "s_waitcnt vmcnt(1)\n\t"
        "v_max3_u32 v17, v6, v7, v8\n\t"
        "v_max_u32 v17, v17, v9\n\t"
        "v_cmp_eq_u32 vcc, -1, v17\n\t"
        "s_cbranch_vccnz Lx70_%=\n\t"
        "s_branch Ld70_%=\n\t"
        "Lx80_%=:\n\t"
        "s_cmp_eq_u32 %[scn], 0\n\t"
        "s_cbranch_scc1 Ld80_%=\n\t"
        "s_sub_u32 %[scn], %[scn], 1\n\t"
        "buffer_load_dwordx4 v[70:73], v108, %[ssrd], %[sob] offen offset:1024 sc1\n\t"
        "s_waitcnt vmcnt(0)\n\t"
        "v_max3_u32 v17, v70, v71, v72\n\t"
        "v_max_u32 v17, v17, v73\n\t"
        "v_cmp_eq_u32 vcc, -1, v17\n\t"
        "s_cbranch_vccnz Lx80_%=\n\t"
        "s_branch Ld80_%=\n\t"
        "Lws00_%=:\n\t"
        "s_cmp_eq_u32 %[scn], 0\n\t"
        "s_cbranch_scc1 Lwg00_%=\n\t"
        "s_sub_u32 %[scn], %[scn], 1\n\t"
        "s_sleep 1\n\t"
        "ds_read_b32 v16, v15 offset:56\n\t"
        "s_waitcnt lgkmcnt(0)\n\t"
        "s_branch Lwc00_%=\n\t"
        "Lcw_%=:\n\t"
        "s_cmp_eq_u32 %[scn], 0\n\t"
        "s_cbranch_scc1 Lcg_%=\n\t"
        "s_sub_u32 %[scn], %[scn], 1\n\t"
        "ds_read_b32 v13, v12 offset:112\n\t"
        "s_waitcnt lgkmcnt(0)\n\t"
        "v_readfirstlane_b32 %[sc], v13\n\t"
        "s_branch Lc_%=\n\t"
        "Lk0_%=:\n\t"
        "buffer_load_dwordx4 v[40:43], v107, %[ssrd], %[sob] offen offset:0 sc1\n\t"
        "v_lshlrev_b32 v20, 16, v88\n\t"
        "v_and_b32 v24, 0xffff0000, v88\n\t"
        "buffer_load_dwordx4 v[44:47], v107, %[ssrd], %[sob] offen offset:1024 sc1\n\t"
        "v_lshlrev_b32 v21, 16, v89\n\t"
        "v_and_b32 v25, 0xffff0000, v89\n\t"
        "buffer_load_dwordx4 v[48:51], v107, %[ssrd], %[sob] offen offset:2048 sc1\n\t"
        "v_lshlrev_b32 v22, 16, v90\n\t"
        "v_and_b32 v26, 0xffff0000, v90\n\t"
        "buffer_load_dwordx4 v[52:55], v107, %[ssrd], %[sob] offen offset:3072 sc1\n\t"
        "v_lshlrev_b32 v23, 16, v91\n\t"
        "v_and_b32 v27, 0xffff0000, v91\n\t"
        "buffer_load_dwordx4 v[56:59], v107, %[ssrd], %[sob4] offen offset:0 sc1\n\t"
        "v_lshlrev_b32 v28, 16, v92\n\t"
        "v_and_b32 v32, 0xffff0000, v92\n\t"
        "buffer_load_dwordx4 v[60:63], v107, %[ssrd], %[sob4] offen offset:1024 sc1\n\t"
        "v_lshlrev_b32 v29, 16, v93\n\t"
        "v_and_b32 v33, 0xffff0000, v93\n\t"
        "buffer_load_dwordx4 v[64:67], v107, %[ssrd], %[sob4] offen offset:2048 sc1\n\t"
        "v_lshlrev_b32 v30, 16, v94\n\t"
        "v_and_b32 v34, 0xffff0000, v94\n\t"
        "buffer_load_dwordx4 v[6:9], v108, %[ssrd], %[sob] offen offset:0 sc1\n\t"
        "v_lshlrev_b32 v31, 16, v95\n\t"
        "v_and_b32 v35, 0xffff0000, v95\n\t"
        "buffer_load_dwordx4 v[70:73], v108, %[ssrd], %[sob] offen offset:1024 sc1\n\t"
        "v_lshlrev_b32 v36, 16, v96\n\t"
        "v_and_b32 v37, 0xffff0000, v96\n\t"
        "v_lshlrev_b32 v38, 16, v97\n\t"
        "v_and_b32 v39, 0xffff0000, v97\n\t"
        "v_mov_b32 v15, 153984\n\t"
        "v_mov_b32 v16, 1\n\t"
        "v_add_u32 v18, 33536, v107\n\t"
        "v_add_u32 v14, 33536, v106\n\t"
        "ds_read_b128 v[76:79], v18 offset:0\n\t"
        "ds_read_b128 v[80:83], v18 offset:1024\n\t"
        "ds_read_b128 v[84:87], v18 offset:2048\n\t"
        "ds_read_b128 v[88:91], v18 offset:3072\n\t"
        "ds_read_b128 v[92:95], v14 offset:0\n\t"
        "ds_read_b128 v[96:99], v18 offset:4608\n\t"
        "s_waitcnt vmcnt(9)\n\t"
        "s_mov_b64 exec, 1\n\t"
        "ds_add_u32 v15, v16 offset:56\n\t"
        "s_mov_b64 exec, -1\n\t"
        "s_waitcnt vmcnt(8)\n\t"
        "v_max3_u32 v17, v40, v41, v42\n\t"
        "v_max_u32 v17, v17, v43\n\t"
        "v_cmp_eq_u32 vcc, -1, v17\n\t"
        "s_cbranch_vccnz Lx00_%=\n\t"
        "Ld00_%=:\n\t"
        "s_waitcnt lgkmcnt(6)\n\t"
        "v_mfma_f32_16x16x32_bf16 v[20:23], v[76:79], v[40:43], v[20:23]\n\t"
        "ds_read_b128 v[76:79], v18 offset:5632\n\t"
        "s_waitcnt lgkmcnt(6)\n\t"
        "v_mfma_f32_16x16x32_bf16 v[24:27], v[80:83], v[40:43], v[24:27]\n\t"
        "ds_read_b128 v[80:83], v18 offset:6656\n\t"
        "s_waitcnt lgkmcnt(6)\n\t"
        "v_mfma_f32_16x16x32_bf16 v[28:31], v[84:87], v[40:43], v[28:31]\n\t"
        "s_waitcnt vmcnt(7)\n\t"
        "v_max3_u32 v17, v44, v45, v46\n\t"
        "v_max_u32 v17, v17, v47\n\t"
        "v_cmp_eq_u32 vcc, -1, v17\n\t"
        "ds_read_b128 v[84:87], v18 offset:7680\n\t"
        "s_waitcnt lgkmcnt(6)\n\t"
        "v_mfma_f32_16x16x32_bf16 v[32:35], v[88:91], v[40:43], v[32:35]\n\t"
        "ds_read_b128 v[88:91], v14 offset:4608\n\t"
        "s_waitcnt lgkmcnt(6)\n\t"
        "v_mfma_f32_16x16x32_bf16 v[36:39], v[92:95], v[40:43], v[36:39]\n\t"
        "s_cbranch_vccnz Lx10_%=\n\t"
        "Ld10_%=:\n\t"
        "ds_read_b32 v16, v15 offset:56\n\t"
        "ds_read_b128 v[92:95], v18 offset:9216\n\t"
        "ds_read_b128 v[40:43], v18 offset:10240\n\t"
        "s_waitcnt lgkmcnt(8)\n\t"
        "v_mfma_f32_16x16x32_bf16 v[20:23], v[96:99], v[44:47], v[20:23]\n\t"
        "ds_read_b128 v[96:99], v18 offset:11264\n\t"
        "s_waitcnt lgkmcnt(7)\n\t"
        "v_mfma_f32_16x16x32_bf16 v[24:27], v[76:79], v[44:47], v[24:27]\n\t"
        "ds_read_b128 v[76:79], v18 offset:12288\n\t"
        "s_waitcnt lgkmcnt(7)\n\t"
        "v_mfma_f32_16x16x32_bf16 v[28:31], v[80:83], v[44:47], v[28:31]\n\t"
        "s_waitcnt vmcnt(6)\n\t"
        "v_max3_u32 v17, v48, v49, v50\n\t"
        "v_max_u32 v17, v17, v51\n\t"
        "v_cmp_eq_u32 vcc, -1, v17\n\t"
        "ds_read_b128 v[80:83], v14 offset:9216\n\t"
        "s_waitcnt lgkmcnt(7)\n\t"
        "v_mfma_f32_16x16x32_bf16 v[32:35], v[84:87], v[44:47], v[32:35]\n\t"
        "s_waitcnt lgkmcnt(5)\n\t"
        "Lwc00_%=:\n\t"
        "v_readfirstlane_b32 %[sob4x], v16\n\t"
        "s_cmp_lt_u32 %[sob4x], %[swt]\n\t"
        "s_cbranch_scc1 Lws00_%=\n\t"
        "Lwg00_%=:\n\t"
        "ds_read_b128 v[84:87], v18 offset:13824\n\t"
        "v_mfma_f32_16x16x32_bf16 v[36:39], v[88:91], v[44:47], v[36:39]\n\t"
        "s_cbranch_vccnz Lx20_%=\n\t"
        "Ld20_%=:\n\t"
        "ds_read_b128 v[88:91], v18 offset:14848\n\t"
        "ds_read_b128 v[44:47], v18 offset:15872\n\t"
        "s_waitcnt lgkmcnt(7)\n\t"
        "v_mfma_f32_16x16x32_bf16 v[20:23], v[92:95], v[48:51], v[20:23]\n\t"
        "ds_read_b128 v[92:95], v18 offset:16896\n\t"
        "s_waitcnt lgkmcnt(7)\n\t"
        "v_mfma_f32_16x16x32_bf16 v[24:27], v[40:43], v[48:51], v[24:27]\n\t"
        "ds_read_b128 v[40:43], v14 offset:13824\n\t"
        "s_waitcnt lgkmcnt(7)\n\t"
        "v_mfma_f32_16x16x32_bf16 v[28:31], v[96:99], v[48:51], v[28:31]\n\t"
        "s_waitcnt vmcnt(5)\n\t"
        "v_max3_u32 v17, v52, v53, v54\n\t"
        "v_max_u32 v17, v17, v55\n\t"
        "v_cmp_eq_u32 vcc, -1, v17\n\t"
        "ds_read_b128 v[96:99], v18 offset:18432\n\t"
        "s_waitcnt lgkmcnt(7)\n\t"
        "v_mfma_f32_16x16x32_bf16 v[32:35], v[76:79], v[48:51], v[32:35]\n\t"
        "ds_read_b128 v[76:79], v18 offset:19456\n\t"
        "s_waitcnt lgkmcnt(7)\n\t"
        "v_mfma_f32_16x16x32_bf16 v[36:39], v[80:83], v[48:51], v[36:39]\n\t"
        "s_cbranch_vccnz Lx30_%=\n\t"
        "Ld30_%=:\n\t"
        "ds_read_b128 v[80:83], v18 offset:20480\n\t"
        "ds_read_b128 v[48:51], v18 offset:21504\n\t"
        "s_waitcnt lgkmcnt(8)\n\t"
        "v_mfma_f32_16x16x32_bf16 v[20:23], v[84:87], v[52:55], v[20:23]\n\t"
        "ds_read_b128 v[84:87], v14 offset:18432\n\t"
        "s_waitcnt lgkmcnt(8)\n\t"
        "v_mfma_f32_16x16x32_bf16 v[24:27], v[88:91], v[52:55], v[24:27]\n\t"
        "ds_read_b128 v[88:91], v18 offset:23040\n\t"
        "s_waitcnt lgkmcnt(8)\n\t"
        "v_mfma_f32_16x16x32_bf16 v[28:31], v[44:47], v[52:55], v[28:31]\n\t"
        "s_waitcnt vmcnt(4)\n\t"
        "v_max3_u32 v17, v56, v57, v58\n\t"
        "v_max_u32 v17, v17, v59\n\t"
        "v_cmp_eq_u32 vcc, -1, v17\n\t"
        "ds_read_b128 v[44:47], v18 offset:24064\n\t"
        "s_waitcnt lgkmcnt(8)\n\t"
        "v_mfma_f32_16x16x32_bf16 v[32:35], v[92:95], v[52:55], v[32:35]\n\t"
        "ds_read_b128 v[92:95], v18 offset:25088\n\t"
        "s_waitcnt lgkmcnt(8)\n\t"
        "v_mfma_f32_16x16x32_bf16 v[36:39], v[40:43], v[52:55], v[36:39]\n\t"
        "s_cbranch_vccnz Lx40_%=\n\t"
        "Ld40_%=:\n\t"
        "ds_read_b128 v[40:43], v18 offset:26112\n\t"
        "s_waitcnt lgkmcnt(8)\n\t"
        "v_mfma_f32_16x16x32_bf16 v[20:23], v[96:99], v[56:59], v[20:23]\n\t"
        "ds_read_b128 v[52:55], v14 offset:23040\n\t"
        "s_waitcnt lgkmcnt(8)\n\t"
        "v_mfma_f32_16x16x32_bf16 v[24:27], v[76:79], v[56:59], v[24:27]\n\t"
        "ds_read_b128 v[96:99], v18 offset:27648\n\t"
        "s_waitcnt lgkmcnt(8)\n\t"
        "v_mfma_f32_16x16x32_bf16 v[28:31], v[80:83], v[56:59], v[28:31]\n\t"
        "s_waitcnt vmcnt(3)\n\t"
        "v_max3_u32 v17, v60, v61, v62\n\t"
        "v_max_u32 v17, v17, v63\n\t"
        "v_cmp_eq_u32 vcc, -1, v17\n\t"
        "ds_read_b128 v[76:79], v18 offset:28672\n\t"
        "s_waitcnt lgkmcnt(8)\n\t"
        "v_mfma_f32_16x16x32_bf16 v[32:35], v[48:51], v[56:59], v[32:35]\n\t"
        "ds_read_b128 v[80:83], v18 offset:29696\n\t"
        "s_waitcnt lgkmcnt(8)\n\t"
        "v_mfma_f32_16x16x32_bf16 v[36:39], v[84:87], v[56:59], v[36:39]\n\t"
        "s_cbranch_vccnz Lx50_%=\n\t"
        "Ld50_%=:\n\t"
        "ds_read_b128 v[48:51], v18 offset:30720\n\t"
        "s_waitcnt lgkmcnt(8)\n\t"
        "v_mfma_f32_16x16x32_bf16 v[20:23], v[88:91], v[60:63], v[20:23]\n\t"
        "ds_read_b128 v[84:87], v14 offset:27648\n\t"
        "s_waitcnt lgkmcnt(8)\n\t"
        "v_mfma_f32_16x16x32_bf16 v[24:27], v[44:47], v[60:63], v[24:27]\n\t"
        "ds_read_b128 v[56:59], v18 offset:32256\n\t"
        "s_waitcnt lgkmcnt(8)\n\t"
        "v_mfma_f32_16x16x32_bf16 v[28:31], v[92:95], v[60:63], v[28:31]\n\t"
        "s_waitcnt vmcnt(2)\n\t"
        "v_max3_u32 v17, v64, v65, v66\n\t"
        "v_max_u32 v17, v17, v67\n\t"
        "v_cmp_eq_u32 vcc, -1, v17\n\t"
        "ds_read_b128 v[88:91], v18 offset:33280\n\t"
        "s_waitcnt lgkmcnt(8)\n\t"
        "v_mfma_f32_16x16x32_bf16 v[32:35], v[40:43], v[60:63], v[32:35]\n\t"
        "ds_read_b128 v[44:47], v18 offset:34304\n\t"
        "s_waitcnt lgkmcnt(8)\n\t"
        "v_mfma_f32_16x16x32_bf16 v[36:39], v[52:55], v[60:63], v[36:39]\n\t"
        "s_cbranch_vccnz Lx60_%=\n\t"
        "Ld60_%=:\n\t"
        "ds_read_b128 v[92:95], v18 offset:35328\n\t"
        "s_waitcnt lgkmcnt(8)\n\t"
        "v_mfma_f32_16x16x32_bf16 v[20:23], v[96:99], v[64:67], v[20:23]\n\t"
        "ds_read_b128 v[40:43], v14 offset:32256\n\t"
        "s_waitcnt lgkmcnt(8)\n\t"
        "v_mfma_f32_16x16x32_bf16 v[24:27], v[76:79], v[64:67], v[24:27]\n\t"
        "ds_read_b128 v[52:55], v18 offset:36864\n\t"
        "s_waitcnt lgkmcnt(8)\n\t"
        "v_mfma_f32_16x16x32_bf16 v[28:31], v[80:83], v[64:67], v[28:31]\n\t"
        "s_waitcnt vmcnt(1)\n\t"
        "v_max3_u32 v17, v6, v7, v8\n\t"
        "v_max_u32 v17, v17, v9\n\t"
        "v_cmp_eq_u32 vcc, -1, v17\n\t"
        "ds_read_b128 v[60:63], v18 offset:37888\n\t"
        "s_waitcnt lgkmcnt(8)\n\t"
        "v_mfma_f32_16x16x32_bf16 v[32:35], v[48:51], v[64:67], v[32:35]\n\t"
        "ds_read_b128 v[96:99], v18 offset:38912\n\t"
        "s_waitcnt lgkmcnt(8)\n\t"
        "v_mfma_f32_16x16x32_bf16 v[36:39], v[84:87], v[64:67], v[36:39]\n\t"
        "s_cbranch_vccnz Lx70_%=\n\t"
        "Ld70_%=:\n\t"
        "ds_read_b128 v[76:79], v18 offset:39936\n\t"
        "s_waitcnt lgkmcnt(8)\n\t"
        "v_mfma_f32_16x16x32_bf16 v[20:23], v[56:59], v[6:9], v[20:23]\n\t"
        "ds_read_b128 v[80:83], v14 offset:36864\n\t"
        "s_waitcnt lgkmcnt(8)\n\t"
        "v_mfma_f32_16x16x32_bf16 v[24:27], v[88:91], v[6:9], v[24:27]\n\t"
        "s_waitcnt lgkmcnt(7)\n\t"
        "v_mfma_f32_16x16x32_bf16 v[28:31], v[44:47], v[6:9], v[28:31]\n\t"
        "s_waitcnt vmcnt(0)\n\t"
        "v_max3_u32 v17, v70, v71, v72\n\t"
        "v_max_u32 v17, v17, v73\n\t"
        "v_cmp_eq_u32 vcc, -1, v17\n\t"
        "s_waitcnt lgkmcnt(6)\n\t"
        "v_mfma_f32_16x16x32_bf16 v[32:35], v[92:95], v[6:9], v[32:35]\n\t"
        "s_waitcnt lgkmcnt(5)\n\t"
        "v_mfma_f32_16x16x32_bf16 v[36:39], v[40:43], v[6:9], v[36:39]\n\t"
        "s_cbranch_vccnz Lx80_%=\n\t"
        "Ld80_%=:\n\t"
        "s_waitcnt lgkmcnt(4)\n\t"
        "v_mfma_f32_16x16x32_bf16 v[20:23], v[52:55], v[70:73], v[20:23]\n\t"
        "s_waitcnt lgkmcnt(3)\n\t"
        "v_mfma_f32_16x16x32_bf16 v[24:27], v[60:63], v[70:73], v[24:27]\n\t"
        "v_add_u32 v18, %[sscr], v107\n\t"
        "s_waitcnt lgkmcnt(2)\n\t"
        "v_mfma_f32_16x16x32_bf16 v[28:31], v[96:99], v[70:73], v[28:31]\n\t"
        "s_waitcnt lgkmcnt(1)\n\t"
        "v_mfma_f32_16x16x32_bf16 v[32:35], v[76:79], v[70:73], v[32:35]\n\t"
        "s_waitcnt lgkmcnt(0)\n\t"
        "v_mfma_f32_16x16x32_bf16 v[36:39], v[80:83], v[70:73], v[36:39]\n\t"
        "s_lshr_b32 %[sc], %[sstat], 9\n\t"
        "s_and_b32 %[sc], %[sc], 12\n\t"
        "v_add_u32 v12, %[sc], v15\n\t"
        "Lp_%=:\n\t"
        "ds_read_b32 v16, v15 offset:24\n\t"
        "ds_read_b128 v[52:55], v18 offset:0\n\t"
        "ds_read_b128 v[56:59], v18 offset:1024\n\t"
        "ds_read_b128 v[60:63], v18 offset:2048\n\t"
        "ds_read_b128 v[64:67], v18 offset:3072\n\t"
        "ds_read_b128 v[68:71], v18 offset:4096\n\t"
        "v_lshrrev_b32 v19, 8, v107\n\t"
        "v_mov_b32 v17, 163232\n\t"
        "v_lshl_add_u32 v19, v19, 4, v17\n\t"
        "v_add_u32 v17, 192, v17\n\t"
        "s_mov_b32 vcc_lo, 0x22222222\n\t"
        "s_mov_b32 vcc_hi, 0x22222222\n\t"
        "v_cndmask_b32 v19, v17, v19, vcc\n\t"
        "ds_read_b128 v[40:43], v19 offset:0\n\t"
        "ds_read_b128 v[44:47], v19 offset:64\n\t"
        "ds_read_b128 v[48:51], v19 offset:128\n\t"
        "ds_read_b32 v13, v12 offset:112\n\t"
        "v_mov_b32 v98, 0\n\t"
        "v_mov_b32 v99, 0\n\t"
        "v_and_b32 v84, 0xf0, v107\n\t"
        "v_mov_b32 v83, 0\n\t"
        "s_waitcnt lgkmcnt(9)\n\t"
        "v_readfirstlane_b32 %[sob4x], v16\n\t"
        "s_cmp_ge_u32 %[sob4x], %[swt]\n\t"
        "s_cbranch_scc1 Lq_%=\n\t"
        "s_cmp_eq_u32 %[scn], 0\n\t"
        "s_cbranch_scc1 Lq_%=\n\t"
        "s_sub_u32 %[scn], %[scn], 1\n\t"
        "s_waitcnt lgkmcnt(0)\n\t"
        "s_branch Lp_%=\n\t"
        "Lq_%=:\n\t"
        "s_waitcnt lgkmcnt(7)\n\t"
        "v_add_f32 v20, v20, v52\n\t"
        "v_add_f32 v21, v21, v53\n\t"
        "v_add_f32 v22, v22, v54\n\t"
        "v_add_f32 v23, v23, v55\n\t"
        "v_add_f32 v24, v24, v56\n\t"
        "v_add_f32 v25, v25, v57\n\t"
        "v_add_f32 v26, v26, v58\n\t"
        "v_add_f32 v27, v27, v59\n\t"
        "v_cvt_pk_bf16_f32 v88, v20, v24\n\t"
        "v_cvt_pk_bf16_f32 v89, v21, v25\n\t"
        "v_cvt_pk_bf16_f32 v90, v22, v26\n\t"
        "v_cvt_pk_bf16_f32 v91, v23, v27\n\t"
        "s_waitcnt lgkmcnt(5)\n\t"
        "v_add_f32 v28, v28, v60\n\t"
        "v_add_f32 v29, v29, v61\n\t"
        "v_add_f32 v30, v30, v62\n\t"
        "v_add_f32 v31, v31, v63\n\t"
        "v_add_f32 v32, v32, v64\n\t"
        "v_add_f32 v33, v33, v65\n\t"
        "v_add_f32 v34, v34, v66\n\t"
        "v_add_f32 v35, v35, v67\n\t"
        "v_cvt_pk_bf16_f32 v92, v28, v32\n\t"
        "v_cvt_pk_bf16_f32 v93, v29, v33\n\t"
        "v_cvt_pk_bf16_f32 v94, v30, v34\n\t"
        "v_cvt_pk_bf16_f32 v95, v31, v35\n\t"
        "s_waitcnt lgkmcnt(0)\n\t"
        "v_mfma_f32_16x16x32_bf16 v[72:75], v[40:43], v[88:91], 0\n\t"
        "v_mfma_f32_16x16x32_bf16 v[76:79], v[88:91], v[88:91], 0\n\t"
        "s_waitcnt lgkmcnt(4)\n\t"
        "v_add_f32 v36, v36, v68\n\t"
        "v_add_f32 v37, v37, v69\n\t"
        "v_add_f32 v38, v38, v70\n\t"
        "v_add_f32 v39, v39, v71\n\t"
        "v_cvt_pk_bf16_f32 v96, v36, v37\n\t"
        "v_cvt_pk_bf16_f32 v97, v38, v39\n\t"
        "s_mov_b32 exec_lo, 0\n\t"
        "v_mov_b32 v96, 0\n\t"
        "v_mov_b32 v97, 0\n\t"
        "s_mov_b32 exec_lo, -1\n\t"
        "v_mfma_f32_16x16x32_bf16 v[72:75], v[44:47], v[92:95], v[72:75]\n\t"
        "v_mfma_f32_16x16x32_bf16 v[76:79], v[92:95], v[92:95], v[76:79]\n\t"
        "v_mfma_f32_16x16x32_bf16 v[72:75], v[48:51], v[96:99], v[72:75]\n\t"
        "v_mfma_f32_16x16x32_bf16 v[76:79], v[96:99], v[96:99], v[76:79]\n\t"
        "v_readfirstlane_b32 %[sc], v13\n\t"
        "v_and_b32 v17, 0x300, v107\n\t"
        "v_lshrrev_b32 v14, 5, v17\n\t"
        "v_lshrrev_b32 v18, 4, v17\n\t"
        "v_add_u32 v19, v14, v84\n\t"
        "v_min_u32 v88, -2, v88\n\t"
        "v_min_u32 v89, -2, v89\n\t"
        "v_min_u32 v90, -2, v90\n\t"
        "v_min_u32 v91, -2, v91\n\t"
        "v_min_u32 v92, -2, v92\n\t"
        "v_min_u32 v93, -2, v93\n\t"
        "v_min_u32 v94, -2, v94\n\t"
        "v_min_u32 v95, -2, v95\n\t"
        "v_min_u32 v96, -2, v96\n\t"
        "v_min_u32 v97, -2, v97\n\t"
        "Lc_%=:\n\t"
        "s_lshr_b32 %[sob4x], %[sc], 6\n\t"
        "s_cmp_lt_u32 %[sob4x], %[swt]\n\t"
        "s_cbranch_scc1 Lcw_%=\n\t"
        "Lcg_%=:\n\t"
        "s_andn2_b32 %[sob4], 15, %[sc]\n\t"
        "s_lshl_b32 exec_lo, 0x10001, %[sob4]\n\t"
        "s_mov_b32 exec_hi, exec_lo\n\t"
        "buffer_store_dwordx4 v[88:91], v18, %[ssrd], %[scr] offen sc1\n\t"
        "buffer_store_dwordx4 v[92:95], v18, %[ssrd], %[scr] offen offset:64 sc1\n\t"
        "s_mov_b32 exec_hi, 0\n\t"
        "buffer_store_dwordx2 v[96:97], v14, %[ssrd], %[scr] offen offset:128 sc1\n\t"
        "s_mov_b64 exec, -1\n\t"
        "v_min_u32 v80, -2, v72\n\t"
        "v_min_u32 v82, -2, v73\n\t"
        "v_cndmask_b32 v76, v76, v77, s[92:93]\n\t"
        "v_cndmask_b32 v78, v78, v79, s[92:93]\n\t"
        "v_cndmask_b32 v76, v76, v78, s[94:95]\n\t"
        "v_min_u32 v81, -2, v76\n\t"
        "v_mov_b32 v16, 1\n\t"
        "s_mov_b32 exec_lo, 0xf0000f\n\t"
        "s_mov_b32 exec_hi, 0xf0000f00\n\t"
        "buffer_store_dwordx4 v[80:83], v84, %[ssrd], %[sstat] offen sc1\n\t"
        "s_mov_b64 exec, 1\n\t"
        "ds_add_u32 v15, v16 offset:12\n\t"
        "s_mov_b64 exec, -1\n\t"
        "s_cmp_lg_u32 %[swt], 4\n\t"
        "s_cbranch_scc1 Lx_%=\n\t"
        "buffer_store_dwordx4 v[88:91], v107, %[ssrd], %[sx1] offen sc1\n\t"
        "buffer_store_dwordx4 v[92:95], v107, %[ssrd], %[sx1] offen offset:1024 sc1\n\t"
        "s_mov_b32 exec_hi, 0\n\t"
        "buffer_store_dwordx2 v[96:97], v19, %[ssrd], %[sx1] offen offset:2048 sc1\n\t"
        "s_mov_b32 exec_hi, -1\n\t"
        "Lx_%=:\n\t"
        "s_cmp_lg_u32 %[swt], 4\n\t"
        "s_cbranch_scc1 Lend_%=\n\t"
        "v_mov_b32 v17, %[stag]\n\t"
        "s_mov_b64 exec, 1\n\t"
        "s_waitcnt vmcnt(0)\n\t"
        "buffer_store_dword v17, off, %[ssrd], %[sxf] sc1\n\t"
        "s_mov_b64 exec, -1\n\t"
        "Lend_%=:\n\t"
        "Lend2_%=:\n\t"
        : [sob4] "=&s"(ob4), [sob4x] "=&s"(ob4x), [sc] "=&s"(cd), [scn] "+s"(cn)
        : [ssrd] "s"(a.srd), [sst] "s"(a.st), [stt] "s"(a.tt), [sc0] "s"(a.c0), [swx] "s"(a.wx), [sldx] "s"(a.ldx), [sskip] "s"(a.skip), [smsk] "s"(a.msk),
          [sob] "s"(a.ob), [swi] "s"(a.wi), [sld] "s"(a.ld), [sscr] "s"(a.scr), [sstat] "s"(a.stat), [sx1] "s"(a.x1), [scr] "s"(a.cr), [sxf] "s"(a.xf),
          [swt] "s"(wt), [stag] "s"(tag), [sgb] "s"(gb), [sgl] "s"(gl)
        : "memory", "scc", "vcc", "s92", "s93", "s94", "s95", "s96", "s97", "s98", "s99", "v2", "v3", "v4", "v5", "v6", "v7", "v8", "v9", "v10", "v11", "v12", "v13", "v14", "v15", "v16", "v17", "v18", "v19", "v20", "v21", "v22", "v23", "v24", "v25", "v26", "v27", "v28", "v29", "v30", "v31", "v32", "v33", "v34", "v35", "v36", "v37", "v38", "v39", "v40", "v41", "v42", "v43", "v44", "v45", "v46", "v47");
}

DEV void dload(u32x4 srd, u32 a2, u32 so) {
    asm volatile(
        "s_nop 4\n\t"
        "buffer_load_dwordx4 v[112:115], v107, %[ssrd], %[so] offen offset:0" NTD "\n\t"
        "buffer_load_dwordx4 v[116:119], v107, %[ssrd], %[so] offen offset:1152" NTD "\n\t"
        "buffer_load_dwordx4 v[120:123], v107, %[ssrd], %[so] offen offset:2304" NTD "\n\t"
        "buffer_load_ushort v220, %[a2], %[ssrd], %[so] offen offset:1024" NTD "\n\t"
        "buffer_load_ushort v221, %[a2], %[ssrd], %[so] offen offset:2176" NTD "\n\t"
        "buffer_load_ushort v222, %[a2], %[ssrd], %[so] offen offset:3328" NTD "\n\t"
        "s_add_u32 %[so], %[so], 3456\n\t"
        "buffer_load_dwordx4 v[124:127], v107, %[ssrd], %[so] offen offset:0" NTD "\n\t"
        "buffer_load_dwordx4 v[128:131], v107, %[ssrd], %[so] offen offset:1152" NTD "\n\t"
        "buffer_load_dwordx4 v[132:135], v107, %[ssrd], %[so] offen offset:2304" NTD "\n\t"
        "buffer_load_ushort v223, %[a2], %[ssrd], %[so] offen offset:1024" NTD "\n\t"
        "buffer_load_ushort v224, %[a2], %[ssrd], %[so] offen offset:2176" NTD "\n\t"
        "buffer_load_ushort v225, %[a2], %[ssrd], %[so] offen offset:3328" NTD "\n\t"
        "s_add_u32 %[so], %[so], 3456\n\t"
        "buffer_load_dwordx4 v[136:139], v107, %[ssrd], %[so] offen offset:0" NTD "\n\t"
        "buffer_load_dwordx4 v[140:143], v107, %[ssrd], %[so] offen offset:1152" NTD "\n\t"
        "buffer_load_dwordx4 v[144:147], v107, %[ssrd], %[so] offen offset:2304" NTD "\n\t"
        "buffer_load_ushort v226, %[a2], %[ssrd], %[so] offen offset:1024" NTD "\n\t"
        "buffer_load_ushort v227, %[a2], %[ssrd], %[so] offen offset:2176" NTD "\n\t"
        "buffer_load_ushort v228, %[a2], %[ssrd], %[so] offen offset:3328" NTD "\n\t"
        "s_add_u32 %[so], %[so], 3456\n\t"
        "buffer_load_dwordx4 v[148:151], v107, %[ssrd], %[so] offen offset:0" NTD "\n\t"
        "buffer_load_dwordx4 v[152:155], v107, %[ssrd], %[so] offen offset:1152" NTD "\n\t"
        "buffer_load_dwordx4 v[156:159], v107, %[ssrd], %[so] offen offset:2304" NTD "\n\t"
        "buffer_load_ushort v229, %[a2], %[ssrd], %[so] offen offset:1024" NTD "\n\t"
        "buffer_load_ushort v230, %[a2], %[ssrd], %[so] offen offset:2176" NTD "\n\t"
        "buffer_load_ushort v231, %[a2], %[ssrd], %[so] offen offset:3328" NTD "\n\t"
        "s_add_u32 %[so], %[so], 3456\n\t"
        "buffer_load_dwordx4 v[160:163], v107, %[ssrd], %[so] offen offset:0" NTD "\n\t"
        "buffer_load_dwordx4 v[164:167], v107, %[ssrd], %[so] offen offset:1152" NTD "\n\t"
        "buffer_load_dwordx4 v[168:171], v107, %[ssrd], %[so] offen offset:2304" NTD "\n\t"
        "buffer_load_ushort v232, %[a2], %[ssrd], %[so] offen offset:1024" NTD "\n\t"
        "buffer_load_ushort v233, %[a2], %[ssrd], %[so] offen offset:2176" NTD "\n\t"
        "buffer_load_ushort v234, %[a2], %[ssrd], %[so] offen offset:3328" NTD "\n\t"
        "s_add_u32 %[so], %[so], 3456\n\t"
        "buffer_load_dwordx4 v[172:175], v107, %[ssrd], %[so] offen offset:0" NTD "\n\t"
        "buffer_load_dwordx4 v[176:179], v107, %[ssrd], %[so] offen offset:1152" NTD "\n\t"
        "buffer_load_dwordx4 v[180:183], v107, %[ssrd], %[so] offen offset:2304" NTD "\n\t"
        "buffer_load_ushort v235, %[a2], %[ssrd], %[so] offen offset:1024" NTD "\n\t"
        "buffer_load_ushort v236, %[a2], %[ssrd], %[so] offen offset:2176" NTD "\n\t"
        "buffer_load_ushort v237, %[a2], %[ssrd], %[so] offen offset:3328" NTD "\n\t"
        "s_add_u32 %[so], %[so], 3456\n\t"
        "buffer_load_dwordx4 v[184:187], v107, %[ssrd], %[so] offen offset:0" NTD "\n\t"
        "buffer_load_dwordx4 v[188:191], v107, %[ssrd], %[so] offen offset:1152" NTD "\n\t"
        "buffer_load_dwordx4 v[192:195], v107, %[ssrd], %[so] offen offset:2304" NTD "\n\t"
        "buffer_load_ushort v238, %[a2], %[ssrd], %[so] offen offset:1024" NTD "\n\t"
        "buffer_load_ushort v239, %[a2], %[ssrd], %[so] offen offset:2176" NTD "\n\t"
        "buffer_load_ushort v240, %[a2], %[ssrd], %[so] offen offset:3328" NTD "\n\t"
        "s_add_u32 %[so], %[so], 3456\n\t"
        "buffer_load_dwordx4 v[196:199], v107, %[ssrd], %[so] offen offset:0" NTD "\n\t"
        "buffer_load_dwordx4 v[200:203], v107, %[ssrd], %[so] offen offset:1152" NTD "\n\t"
        "buffer_load_dwordx4 v[204:207], v107, %[ssrd], %[so] offen offset:2304" NTD "\n\t"
        "buffer_load_ushort v241, %[a2], %[ssrd], %[so] offen offset:1024" NTD "\n\t"
        "buffer_load_ushort v242, %[a2], %[ssrd], %[so] offen offset:2176" NTD "\n\t"
        "buffer_load_ushort v243, %[a2], %[ssrd], %[so] offen offset:3328" NTD "\n\t"
        "s_add_u32 %[so], %[so], 3456\n\t"
        : [so] "+s"(so) : [ssrd] "s"(srd), [a2] "v"(a2) : "memory", "scc");
}

DEV void xstats(int xa, float (&sx)[2], float (&sq)[2]) {
    int xb; u32 sc;
    asm volatile(
        "v_mov_b32 v24, 0x3f803f80\n\t"
        "v_mov_b32 v25, 0x3f803f80\n\t"
        "v_mov_b32 v26, 0x3f803f80\n\t"
        "v_mov_b32 v27, 0x3f803f80\n\t"
        "v_mov_b32 v48, 0\n\t"
        "v_mov_b32 v49, 0\n\t"
        "v_mov_b32 v50, 0\n\t"
        "v_mov_b32 v51, 0\n\t"
        "v_mov_b32 v52, 0\n\t"
        "v_mov_b32 v53, 0\n\t"
        "v_mov_b32 v54, 0\n\t"
        "v_mov_b32 v55, 0\n\t"
        "v_mov_b32 v56, 0\n\t"
        "v_mov_b32 v57, 0\n\t"
        "v_mov_b32 v58, 0\n\t"
        "v_mov_b32 v59, 0\n\t"
        "v_mov_b32 v60, 0\n\t"
        "v_mov_b32 v61, 0\n\t"
        "v_mov_b32 v62, 0\n\t"
        "v_mov_b32 v63, 0\n\t"
        "v_xor_b32 %[xb], 64, %[xa]\n\t"
        "s_mov_b32 %[sc], 3\n\t"
        "Ll_%=:\n\t"
        "ds_read_b128 v[208:211], %[xa] offset:0\n\t"
        "ds_read_b128 v[212:215], %[xa] offset:6144\n\t"
        "ds_read_b128 v[216:219], %[xb] offset:0\n\t"
        "ds_read_b128 v[220:223], %[xb] offset:6144\n\t"
        "ds_read_b128 v[224:227], %[xa] offset:10240\n\t"
        "ds_read_b128 v[228:231], %[xa] offset:16384\n\t"
        "ds_read_b128 v[232:235], %[xb] offset:10240\n\t"
        "ds_read_b128 v[236:239], %[xb] offset:16384\n\t"
        "ds_read_b128 v[240:243], %[xa] offset:20480\n\t"
        "ds_read_b128 v[244:247], %[xa] offset:26624\n\t"
        "ds_read_b128 v[248:251], %[xb] offset:20480\n\t"
        "ds_read_b128 v[252:255], %[xb] offset:26624\n\t"
        "v_add_u32 %[xa], 30720, %[xa]\n\t"
        "v_add_u32 %[xb], 30720, %[xb]\n\t"
        "s_waitcnt lgkmcnt(11)\n\t"
        "v_mfma_f32_16x16x32_bf16 v[48:51], v[24:27], v[208:211], v[48:51]\n\t"
        "v_mfma_f32_16x16x32_bf16 v[56:59], v[208:211], v[208:211], v[56:59]\n\t"
        "s_waitcnt lgkmcnt(10)\n\t"
        "v_mfma_f32_16x16x32_bf16 v[52:55], v[24:27], v[212:215], v[52:55]\n\t"
        "v_mfma_f32_16x16x32_bf16 v[60:63], v[212:215], v[212:215], v[60:63]\n\t"
        "s_waitcnt lgkmcnt(9)\n\t"
        "v_mfma_f32_16x16x32_bf16 v[48:51], v[24:27], v[216:219], v[48:51]\n\t"
        "v_mfma_f32_16x16x32_bf16 v[56:59], v[216:219], v[216:219], v[56:59]\n\t"
        "s_waitcnt lgkmcnt(8)\n\t"
        "v_mfma_f32_16x16x32_bf16 v[52:55], v[24:27], v[220:223], v[52:55]\n\t"
        "v_mfma_f32_16x16x32_bf16 v[60:63], v[220:223], v[220:223], v[60:63]\n\t"
        "s_waitcnt lgkmcnt(7)\n\t"
        "v_mfma_f32_16x16x32_bf16 v[48:51], v[24:27], v[224:227], v[48:51]\n\t"
        "v_mfma_f32_16x16x32_bf16 v[56:59], v[224:227], v[224:227], v[56:59]\n\t"
        "s_waitcnt lgkmcnt(6)\n\t"
        "v_mfma_f32_16x16x32_bf16 v[52:55], v[24:27], v[228:231], v[52:55]\n\t"
        "v_mfma_f32_16x16x32_bf16 v[60:63], v[228:231], v[228:231], v[60:63]\n\t"
        "s_waitcnt lgkmcnt(5)\n\t"
        "v_mfma_f32_16x16x32_bf16 v[48:51], v[24:27], v[232:235], v[48:51]\n\t"
        "v_mfma_f32_16x16x32_bf16 v[56:59], v[232:235], v[232:235], v[56:59]\n\t"
        "s_waitcnt lgkmcnt(4)\n\t"
        "v_mfma_f32_16x16x32_bf16 v[52:55], v[24:27], v[236:239], v[52:55]\n\t"
        "v_mfma_f32_16x16x32_bf16 v[60:63], v[236:239], v[236:239], v[60:63]\n\t"
        "s_waitcnt lgkmcnt(3)\n\t"
        "v_mfma_f32_16x16x32_bf16 v[48:51], v[24:27], v[240:243], v[48:51]\n\t"
        "v_mfma_f32_16x16x32_bf16 v[56:59], v[240:243], v[240:243], v[56:59]\n\t"
        "s_waitcnt lgkmcnt(2)\n\t"
        "v_mfma_f32_16x16x32_bf16 v[52:55], v[24:27], v[244:247], v[52:55]\n\t"
        "v_mfma_f32_16x16x32_bf16 v[60:63], v[244:247], v[244:247], v[60:63]\n\t"
        "s_waitcnt lgkmcnt(1)\n\t"
        "v_mfma_f32_16x16x32_bf16 v[48:51], v[24:27], v[248:251], v[48:51]\n\t"
        "v_mfma_f32_16x16x32_bf16 v[56:59], v[248:251], v[248:251], v[56:59]\n\t"
        "s_waitcnt lgkmcnt(0)\n\t"
        "v_mfma_f32_16x16x32_bf16 v[52:55], v[24:27], v[252:255], v[52:55]\n\t"
        "v_mfma_f32_16x16x32_bf16 v[60:63], v[252:255], v[252:255], v[60:63]\n\t"
        "s_sub_u32 %[sc], %[sc], 1\n\t"
        "s_cmp_lg_u32 %[sc], 0\n\t"
        "s_cbranch_scc1 Ll_%=\n\t"
        "s_nop 7\n\t"
        "v_mov_b32 %[x0], v48\n\t"
        "v_cndmask_b32 v56, v56, v57, %[spar]\n\t"
        "v_cndmask_b32 v58, v58, v59, %[spar]\n\t"
        "v_cndmask_b32 %[q0], v56, v58, %[sl2]\n\t"
        "v_mov_b32 %[x1], v52\n\t"
        "v_cndmask_b32 v60, v60, v61, %[spar]\n\t"
        "v_cndmask_b32 v62, v62, v63, %[spar]\n\t"
        "v_cndmask_b32 %[q1], v60, v62, %[sl2]\n\t"
        : [xa] "+v"(xa), [xb] "=&v"(xb), [sc] "=&s"(sc), [x0] "=&v"(sx[0]), [q0] "=&v"(sq[0]), [x1] "=&v"(sx[1]), [q1] "=&v"(sq[1])
        : [spar] "s"(0xAAAAAAAAAAAAAAAAull), [sl2] "s"(0xCCCCCCCCCCCCCCCCull)
        : "memory", "scc", "v24", "v25", "v26", "v27");
}

DEV void uv_load(u32x4 sr, u32x4 so, u32 ra, u32 oa, u32 ob) {
    asm volatile(
        "s_nop 4\n\t"
        "buffer_load_dwordx4 v[48:51], %[ra], %[sr], 0 offen offset:0\n\t"
        "buffer_load_dwordx4 v[52:55], %[ra], %[sr], 0 offen offset:256\n\t"
        "buffer_load_dwordx4 v[56:59], %[ra], %[sr], 0 offen offset:512\n\t"
        "buffer_load_dwordx4 v[60:63], %[ra], %[sr], 0 offen offset:768\n\t"
        "buffer_load_dwordx4 v[64:67], %[ra], %[sr], 0 offen offset:1024\n\t"
        "buffer_load_dwordx4 v[68:71], %[oa], %[so], 0 offen offset:0\n\t"
        "buffer_load_dwordx4 v[72:75], %[oa], %[so], 0 offen offset:256\n\t"
        "buffer_load_dwordx4 v[76:79], %[oa], %[so], 0 offen offset:512\n\t"
        "buffer_load_dwordx4 v[80:83], %[oa], %[so], 0 offen offset:768\n\t"
        "buffer_load_dwordx4 v[84:87], %[ob], %[so], 0 offen offset:1024\n\t"
        :: [sr] "s"(sr), [so] "s"(so), [ra] "v"(ra), [oa] "v"(oa), [ob] "v"(ob) : "memory");
}
DEV void uv_dots(float (&o)[4]) {
    asm volatile(
        "v_mov_b32 %[o0], 0\n\t"
        "v_mov_b32 %[o1], 0\n\t"
        "v_mov_b32 %[o2], 0\n\t"
        "v_mov_b32 %[o3], 0\n\t"
        "v_dot2c_f32_bf16 %[o0], %[sone], v48\n\t"
        "v_dot2c_f32_bf16 %[o1], v48, v68\n\t"
        "v_dot2c_f32_bf16 %[o2], %[sone], v68\n\t"
        "v_dot2c_f32_bf16 %[o3], v68, v68\n\t"
        "v_dot2c_f32_bf16 %[o0], %[sone], v49\n\t"
        "v_dot2c_f32_bf16 %[o1], v49, v69\n\t"
        "v_dot2c_f32_bf16 %[o2], %[sone], v69\n\t"
        "v_dot2c_f32_bf16 %[o3], v69, v69\n\t"
        "v_dot2c_f32_bf16 %[o0], %[sone], v50\n\t"
        "v_dot2c_f32_bf16 %[o1], v50, v70\n\t"
        "v_dot2c_f32_bf16 %[o2], %[sone], v70\n\t"
        "v_dot2c_f32_bf16 %[o3], v70, v70\n\t"
        "v_dot2c_f32_bf16 %[o0], %[sone], v51\n\t"
        "v_dot2c_f32_bf16 %[o1], v51, v71\n\t"
        "v_dot2c_f32_bf16 %[o2], %[sone], v71\n\t"
        "v_dot2c_f32_bf16 %[o3], v71, v71\n\t"
        "v_dot2c_f32_bf16 %[o0], %[sone], v52\n\t"
        "v_dot2c_f32_bf16 %[o1], v52, v72\n\t"
        "v_dot2c_f32_bf16 %[o2], %[sone], v72\n\t"
        "v_dot2c_f32_bf16 %[o3], v72, v72\n\t"
        "v_dot2c_f32_bf16 %[o0], %[sone], v53\n\t"
        "v_dot2c_f32_bf16 %[o1], v53, v73\n\t"
        "v_dot2c_f32_bf16 %[o2], %[sone], v73\n\t"
        "v_dot2c_f32_bf16 %[o3], v73, v73\n\t"
        "v_dot2c_f32_bf16 %[o0], %[sone], v54\n\t"
        "v_dot2c_f32_bf16 %[o1], v54, v74\n\t"
        "v_dot2c_f32_bf16 %[o2], %[sone], v74\n\t"
        "v_dot2c_f32_bf16 %[o3], v74, v74\n\t"
        "v_dot2c_f32_bf16 %[o0], %[sone], v55\n\t"
        "v_dot2c_f32_bf16 %[o1], v55, v75\n\t"
        "v_dot2c_f32_bf16 %[o2], %[sone], v75\n\t"
        "v_dot2c_f32_bf16 %[o3], v75, v75\n\t"
        "v_dot2c_f32_bf16 %[o0], %[sone], v56\n\t"
        "v_dot2c_f32_bf16 %[o1], v56, v76\n\t"
        "v_dot2c_f32_bf16 %[o2], %[sone], v76\n\t"
        "v_dot2c_f32_bf16 %[o3], v76, v76\n\t"
        "v_dot2c_f32_bf16 %[o0], %[sone], v57\n\t"
        "v_dot2c_f32_bf16 %[o1], v57, v77\n\t"
        "v_dot2c_f32_bf16 %[o2], %[sone], v77\n\t"
        "v_dot2c_f32_bf16 %[o3], v77, v77\n\t"
        "v_dot2c_f32_bf16 %[o0], %[sone], v58\n\t"
        "v_dot2c_f32_bf16 %[o1], v58, v78\n\t"
        "v_dot2c_f32_bf16 %[o2], %[sone], v78\n\t"
        "v_dot2c_f32_bf16 %[o3], v78, v78\n\t"
        "v_dot2c_f32_bf16 %[o0], %[sone], v59\n\t"
        "v_dot2c_f32_bf16 %[o1], v59, v79\n\t"
        "v_dot2c_f32_bf16 %[o2], %[sone], v79\n\t"
        "v_dot2c_f32_bf16 %[o3], v79, v79\n\t"
        "v_dot2c_f32_bf16 %[o0], %[sone], v60\n\t"
        "v_dot2c_f32_bf16 %[o1], v60, v80\n\t"
        "v_dot2c_f32_bf16 %[o2], %[sone], v80\n\t"
        "v_dot2c_f32_bf16 %[o3], v80, v80\n\t"
        "v_dot2c_f32_bf16 %[o0], %[sone], v61\n\t"
        "v_dot2c_f32_bf16 %[o1], v61, v81\n\t"
        "v_dot2c_f32_bf16 %[o2], %[sone], v81\n\t"
        "v_dot2c_f32_bf16 %[o3], v81, v81\n\t"
        "v_dot2c_f32_bf16 %[o0], %[sone], v62\n\t"
        "v_dot2c_f32_bf16 %[o1], v62, v82\n\t"
        "v_dot2c_f32_bf16 %[o2], %[sone], v82\n\t"
        "v_dot2c_f32_bf16 %[o3], v82, v82\n\t"
        "v_dot2c_f32_bf16 %[o0], %[sone], v63\n\t"
        "v_dot2c_f32_bf16 %[o1], v63, v83\n\t"
        "v_dot2c_f32_bf16 %[o2], %[sone], v83\n\t"
        "v_dot2c_f32_bf16 %[o3], v83, v83\n\t"
        "v_dot2c_f32_bf16 %[o0], %[sone], v64\n\t"
        "v_dot2c_f32_bf16 %[o1], v64, v84\n\t"
        "v_dot2c_f32_bf16 %[o2], %[sone], v84\n\t"
        "v_dot2c_f32_bf16 %[o3], v84, v84\n\t"
        "v_dot2c_f32_bf16 %[o0], %[sone], v65\n\t"
        "v_dot2c_f32_bf16 %[o1], v65, v85\n\t"
        "v_dot2c_f32_bf16 %[o2], %[sone], v85\n\t"
        "v_dot2c_f32_bf16 %[o3], v85, v85\n\t"
        "v_dot2c_f32_bf16 %[o0], %[sone], v66\n\t"
        "v_dot2c_f32_bf16 %[o1], v66, v86\n\t"
        "v_dot2c_f32_bf16 %[o2], %[sone], v86\n\t"
        "v_dot2c_f32_bf16 %[o3], v86, v86\n\t"
        "v_dot2c_f32_bf16 %[o0], %[sone], v67\n\t"
        "v_dot2c_f32_bf16 %[o1], v67, v87\n\t"
        "v_dot2c_f32_bf16 %[o2], %[sone], v87\n\t"
        "v_dot2c_f32_bf16 %[o3], v87, v87\n\t"
        "s_nop 2\n\t"
        : [o0] "=&v"(o[0]), [o1] "=&v"(o[1]), [o2] "=&v"(o[2]), [o3] "=&v"(o[3]) : [sone] "s"(ONES2) : "memory");
}

DEV void ffn_help(u32x4 srd, u32 c0, int g0, int g1, int g2, int pa, u32 fa, int ct, u32 l1, u32& cn) {
    u32 cr;
    asm volatile(
        "s_waitcnt vmcnt(0)\n\t"
        "ds_read_b64_tr_b16 v[40:41], %[g0] offset:0\n\t"
        "ds_read_b64_tr_b16 v[42:43], %[g0] offset:384\n\t"
        "ds_read_b64_tr_b16 v[44:45], %[g1] offset:0\n\t"
        "ds_read_b64_tr_b16 v[46:47], %[g1] offset:384\n\t"
        "ds_read_b64_tr_b16 v[48:49], %[g2] offset:0\n\t"
        "ds_read_b64_tr_b16 v[50:51], %[g2] offset:384\n\t"
        "ds_read_b64_tr_b16 v[52:53], %[g0] offset:1536\n\t"
        "ds_read_b64_tr_b16 v[54:55], %[g0] offset:1920\n\t"
        "ds_read_b64_tr_b16 v[56:57], %[g1] offset:1536\n\t"
        "ds_read_b64_tr_b16 v[58:59], %[g1] offset:1920\n\t"
        "ds_read_b64_tr_b16 v[60:61], %[g2] offset:1536\n\t"
        "ds_read_b64_tr_b16 v[62:63], %[g2] offset:1920\n\t"
        "ds_read_b64_tr_b16 v[64:65], %[g0] offset:3072\n\t"
        "ds_read_b64_tr_b16 v[66:67], %[g0] offset:3456\n\t"
        "ds_read_b64_tr_b16 v[68:69], %[g1] offset:3072\n\t"
        "ds_read_b64_tr_b16 v[70:71], %[g1] offset:3456\n\t"
        "ds_read_b64_tr_b16 v[72:73], %[g2] offset:3072\n\t"
        "ds_read_b64_tr_b16 v[74:75], %[g2] offset:3456\n\t"
        "ds_read_b64_tr_b16 v[76:77], %[g0] offset:4608\n\t"
        "ds_read_b64_tr_b16 v[78:79], %[g0] offset:4992\n\t"
        "ds_read_b64_tr_b16 v[80:81], %[g1] offset:4608\n\t"
        "ds_read_b64_tr_b16 v[82:83], %[g1] offset:4992\n\t"
        "ds_read_b64_tr_b16 v[84:85], %[g2] offset:4608\n\t"
        "ds_read_b64_tr_b16 v[86:87], %[g2] offset:4992\n\t"
        "ds_read_b64_tr_b16 v[88:89], %[g0] offset:6144\n\t"
        "ds_read_b64_tr_b16 v[90:91], %[g0] offset:6528\n\t"
        "ds_read_b64_tr_b16 v[92:93], %[g1] offset:6144\n\t"
        "ds_read_b64_tr_b16 v[94:95], %[g1] offset:6528\n\t"
        "ds_read_b64_tr_b16 v[96:97], %[g2] offset:6144\n\t"
        "ds_read_b64_tr_b16 v[98:99], %[g2] offset:6528\n\t"
        "v_mov_b32 v20, 0x3f803f80\n\t"
        "v_mov_b32 v21, 0x3f803f80\n\t"
        "v_mov_b32 v22, 0x3f803f80\n\t"
        "v_mov_b32 v23, 0x3f803f80\n\t"
        "v_mov_b32 v24, 0x3f803f80\n\t"
        "v_mov_b32 v25, 0x3f803f80\n\t"
        "v_mov_b32 v26, 0x3f803f80\n\t"
        "v_mov_b32 v27, 0x3f803f80\n\t"
        "v_mov_b32 v28, 0x3f803f80\n\t"
        "v_mov_b32 v29, 0x3f803f80\n\t"
        "v_mov_b32 v30, 0x3f803f80\n\t"
        "v_mov_b32 v31, 0x3f803f80\n\t"
        "v_mov_b32 v32, 0x3f803f80\n\t"
        "v_mov_b32 v33, 0x3f803f80\n\t"
        "v_mov_b32 v34, 0x3f803f80\n\t"
        "v_mov_b32 v35, 0x3f803f80\n\t"
        "v_mov_b32 v36, 0x3f803f80\n\t"
        "v_mov_b32 v37, 0x3f803f80\n\t"
        "v_mov_b32 v38, 0x3f803f80\n\t"
        "v_mov_b32 v39, 0x3f803f80\n\t"
        "v_mov_b32 %[g1], %[sl1]\n\t"
        "v_mov_b32 %[g2], %[sfa]\n\t"
        "Lw_%=:\n\t"
        "s_sleep 1\n\t"
        "ds_read_b32 %[g0], %[ct]\n\t"
        "s_sub_u32 %[scn], %[scn], 1\n\t"
        "s_waitcnt lgkmcnt(0)\n\t"
        "v_readfirstlane_b32 %[scr], %[g0]\n\t"
        "s_cmp_lt_i32 %[scn], 1\n\t"
        "s_cbranch_scc1 Lq_%=\n\t"
        "s_cmp_eq_u32 %[scr], 0\n\t"
        "s_cbranch_scc1 Lw_%=\n\t"
        "Lq_%=:\n\t"
        "s_setprio 3\n\t"
        "v_add_u32 %[c0], %[c0], %[g0]\n\t"
        "Lr_%=:\n\t"
        "s_mov_b64 exec, %[sm7]\n\t"
        "buffer_load_dwordx4 v[20:23], %[c0], %[ssrd], 0 offen offset:0 sc1\n\t"
        "buffer_load_dwordx4 v[24:27], %[c0], %[ssrd], 0 offen offset:64 sc1\n\t"
        "buffer_load_dwordx4 v[28:31], %[c0], %[ssrd], 0 offen offset:128 sc1\n\t"
        "buffer_load_dwordx4 v[32:35], %[c0], %[ssrd], 0 offen offset:192 sc1\n\t"
        "buffer_load_dwordx4 v[36:39], %[c0], %[ssrd], 0 offen offset:256 sc1\n\t"
        "s_mov_b64 exec, -1\n\t"
        "v_mov_b32 %[ct], 153984\n\t"
        "s_waitcnt vmcnt(4)\n\t"
        "ds_read_b32 v7, %[ct] offset:12\n\t"
        "v_mfma_f32_16x16x32_bf16 v[8:11], v[20:23], v[40:43], 0\n\t"
        "v_max3_u32 %[g0], v22, v20, v21\n\t"
        "v_mfma_f32_16x16x32_bf16 v[12:15], v[20:23], v[44:47], 0\n\t"
        "v_max3_u32 %[g0], %[g0], v22, v23\n\t"
        "v_mfma_f32_16x16x32_bf16 v[16:19], v[20:23], v[48:51], 0\n\t"
        "s_waitcnt vmcnt(3)\n\t"
        "v_mfma_f32_16x16x32_bf16 v[8:11], v[24:27], v[52:55], v[8:11]\n\t"
        "v_max3_u32 %[g0], %[g0], v24, v25\n\t"
        "v_mfma_f32_16x16x32_bf16 v[12:15], v[24:27], v[56:59], v[12:15]\n\t"
        "v_max3_u32 %[g0], %[g0], v26, v27\n\t"
        "v_mfma_f32_16x16x32_bf16 v[16:19], v[24:27], v[60:63], v[16:19]\n\t"
        "s_waitcnt vmcnt(2)\n\t"
        "v_mfma_f32_16x16x32_bf16 v[8:11], v[28:31], v[64:67], v[8:11]\n\t"
        "v_max3_u32 %[g0], %[g0], v28, v29\n\t"
        "v_mfma_f32_16x16x32_bf16 v[12:15], v[28:31], v[68:71], v[12:15]\n\t"
        "v_max3_u32 %[g0], %[g0], v30, v31\n\t"
        "v_mfma_f32_16x16x32_bf16 v[16:19], v[28:31], v[72:75], v[16:19]\n\t"
        "s_waitcnt vmcnt(1)\n\t"
        "v_mfma_f32_16x16x32_bf16 v[8:11], v[32:35], v[76:79], v[8:11]\n\t"
        "v_max3_u32 %[g0], %[g0], v32, v33\n\t"
        "v_mfma_f32_16x16x32_bf16 v[12:15], v[32:35], v[80:83], v[12:15]\n\t"
        "v_max3_u32 %[g0], %[g0], v34, v35\n\t"
        "v_mfma_f32_16x16x32_bf16 v[16:19], v[32:35], v[84:87], v[16:19]\n\t"
        "s_waitcnt vmcnt(0)\n\t"
        "v_mfma_f32_16x16x32_bf16 v[8:11], v[36:39], v[88:91], v[8:11]\n\t"
        "v_max3_u32 %[g0], %[g0], v36, v37\n\t"
        "v_mfma_f32_16x16x32_bf16 v[12:15], v[36:39], v[92:95], v[12:15]\n\t"
        "v_max3_u32 %[g0], %[g0], v38, v39\n\t"
        "v_mfma_f32_16x16x32_bf16 v[16:19], v[36:39], v[96:99], v[16:19]\n\t"
        "v_cmp_eq_u32 vcc, -1, %[g0]\n\t"
        "s_cbranch_vccz Ld_%=\n\t"
        "s_cmp_lt_i32 %[scn], 1\n\t"
        "s_cbranch_scc1 Ld_%=\n\t"
        "s_sub_u32 %[scn], %[scn], 1\n\t"
        "s_branch Lr_%=\n\t"
        "Ld_%=:\n\t"
        "Lk_%=:\n\t"
        "s_waitcnt lgkmcnt(0)\n\t"
        "v_lshrrev_b32 v7, 2, v7\n\t"
        "v_cmp_gt_u32 vcc, %[sl1], v7\n\t"
        "s_cbranch_vccz Lj_%=\n\t"
        "ds_read_b32 v7, %[ct] offset:12\n\t"
        "s_sub_u32 %[scn], %[scn], 1\n\t"
        "s_cmp_gt_i32 %[scn], 0\n\t"
        "s_cbranch_scc1 Lk_%=\n\t"
        "s_waitcnt lgkmcnt(0)\n\t"
        "Lj_%=:\n\t"
        "s_nop 1\n\t"
        "s_mov_b32 exec_hi, 0\n\t"
        "ds_write_b128 %[pa], v[8:11] offset:0\n\t"
        "ds_write_b128 %[pa], v[12:15] offset:512\n\t"
        "ds_write_b128 %[pa], v[16:19] offset:1024\n\t"
        "ds_write_b32 %[g2], %[g1]\n\t"
        "s_mov_b32 exec_hi, -1\n\t"
        : [scr] "=&s"(cr), [scn] "+s"(cn), [g0] "+v"(g0), [g1] "+v"(g1), [g2] "+v"(g2), [c0] "+v"(c0), [ct] "+v"(ct)
        : [ssrd] "s"(srd), [pa] "v"(pa), [sfa] "s"(fa), [sl1] "s"(l1), [sm7] "s"(0x007F007F007F007Full)
        : "memory", "scc", "vcc", "v7", "v8", "v9", "v10", "v11", "v12", "v13", "v14", "v15", "v16", "v17", "v18", "v19", "v20", "v21", "v22", "v23", "v24", "v25", "v26", "v27", "v28", "v29", "v30", "v31", "v32", "v33", "v34", "v35", "v36", "v37", "v38", "v39", "v40", "v41", "v42", "v43", "v44", "v45", "v46", "v47");
}

DEV void ffn_a(u32x4 srd, u32 ra, u32 sa, float (&o)[3], u32& cn) {
    u32 t;
    asm volatile(
        "s_nop 4\n\t"
        "Lr_%=:\n\t"
        "buffer_load_dwordx4 v[72:75], %[va], %[ssrd], 0 offen sc1\n\t"
        "buffer_load_dwordx4 v[76:79], %[va], %[ssrd], 0 offen offset:1024 sc1\n\t"
        "s_mov_b64 exec, %[sm7]\n\t"
        "buffer_load_dwordx4 v[60:63], %[ra], %[ssrd], 0 offen offset:0 sc1\n\t"
        "buffer_load_dwordx4 v[64:67], %[ra], %[ssrd], 0 offen offset:64 sc1\n\t"
        "buffer_load_dwordx4 v[68:71], %[ra], %[ssrd], 0 offen offset:128 sc1\n\t"
        "s_mov_b64 exec, -1\n\t"
        "s_sub_u32 %[scn], %[scn], 1\n\t"
        "s_waitcnt vmcnt(3)\n\t"
        "v_max3_u32 %[t], v72, v73, v74\n\t"
        "v_max3_u32 %[t], %[t], v75, v76\n\t"
        "v_max3_u32 %[t], %[t], v77, v78\n\t"
        "v_max_u32 %[t], %[t], v79\n\t"
        "v_cmp_eq_u32 vcc, -1, %[t]\n\t"
        "v_add_f32 %[o0], v72, v76\n\t"
        "v_add_f32 %[o1], v73, v77\n\t"
        "v_add_f32 %[o2], v74, v78\n\t"
        "s_cbranch_vccz Le_%=\n\t"
        "s_cmp_gt_i32 %[scn], 0\n\t"
        "s_cbranch_scc1 Lr_%=\n\t"
        "Le_%=:\n\t"
        : [o0] "=&v"(o[0]), [o1] "=&v"(o[1]), [o2] "=&v"(o[2]), [t] "=&v"(t), [scn] "+s"(cn)
        : [ssrd] "s"(srd), [ra] "v"(ra), [va] "v"(sa), [sm7] "s"(0x007F007F007F007Full)
        : "memory", "scc", "vcc");
}
DEV void ffn_ones() {
    asm volatile(
        "v_mov_b32 v60, 0x3f803f80\n\t"
        "v_mov_b32 v61, 0x3f803f80\n\t"
        "v_mov_b32 v62, 0x3f803f80\n\t"
        "v_mov_b32 v63, 0x3f803f80\n\t"
        "v_mov_b32 v64, 0x3f803f80\n\t"
        "v_mov_b32 v65, 0x3f803f80\n\t"
        "v_mov_b32 v66, 0x3f803f80\n\t"
        "v_mov_b32 v67, 0x3f803f80\n\t"
        "v_mov_b32 v68, 0x3f803f80\n\t"
        "v_mov_b32 v69, 0x3f803f80\n\t"
        "v_mov_b32 v70, 0x3f803f80\n\t"
        "v_mov_b32 v71, 0x3f803f80\n\t");
}

DEV void ffn(u32x4 srd, u32 cr, u32 ra, u32 c1, int pa, int ff, int pg, int pu, u32 wo, float m, float r, u32 l1, u32 ya, u32 ye, u32 yb, u32& cn) {
    u32 ap[12];
    asm volatile(
        "v_mov_b32 v45, 153984\n\t"
        "Lr_%=:\n\t"
        "s_waitcnt vmcnt(2)\n\t"
        "ds_read_b32 v46, v45 offset:12\n\t"
        "v_mfma_f32_16x16x32_bf16 v[84:87], v[60:63], v[220:223], 0\n\t"
        "v_max3_u32 v40, v62, v60, v61\n\t"
        "v_mfma_f32_16x16x32_bf16 v[88:91], v[60:63], v[224:227], 0\n\t"
        "v_max3_u32 v40, v40, v62, v63\n\t"
        "v_mfma_f32_16x16x32_bf16 v[92:95], v[60:63], v[228:231], 0\n\t"
        "s_waitcnt vmcnt(1)\n\t"
        "v_mfma_f32_16x16x32_bf16 v[84:87], v[64:67], v[232:235], v[84:87]\n\t"
        "v_max3_u32 v40, v40, v64, v65\n\t"
        "v_mfma_f32_16x16x32_bf16 v[88:91], v[64:67], v[236:239], v[88:91]\n\t"
        "v_max3_u32 v40, v40, v66, v67\n\t"
        "v_mfma_f32_16x16x32_bf16 v[92:95], v[64:67], v[240:243], v[92:95]\n\t"
        "s_waitcnt vmcnt(0)\n\t"
        "v_mfma_f32_16x16x32_bf16 v[84:87], v[68:71], v[244:247], v[84:87]\n\t"
        "v_max3_u32 v40, v40, v68, v69\n\t"
        "v_mfma_f32_16x16x32_bf16 v[88:91], v[68:71], v[248:251], v[88:91]\n\t"
        "v_max3_u32 v40, v40, v70, v71\n\t"
        "v_mfma_f32_16x16x32_bf16 v[92:95], v[68:71], v[252:255], v[92:95]\n\t"
        "v_cmp_eq_u32 vcc, -1, v40\n\t"
        "s_cbranch_vccnz Lx_%=\n\t"
        "Ld_%=:\n\t"
        "buffer_load_dwordx4 v[24:27], v107, %[ssrd], %[scr] offen sc1\n\t"
        "buffer_load_dwordx4 v[28:31], %[c1], %[ssrd], %[scr] offen sc1\n\t"
        "v_add_u32 v42, %[swo], %[pg]\n\t"
        "v_add_u32 v43, %[swo], %[pu]\n\t"
        "Lk_%=:\n\t"
        "s_waitcnt lgkmcnt(0)\n\t"
        "v_lshrrev_b32 v46, 2, v46\n\t"
        "v_cmp_gt_u32 vcc, %[sl1], v46\n\t"
        "s_cbranch_vccz Lj_%=\n\t"
        "ds_read_b32 v46, v45 offset:12\n\t"
        "s_sub_u32 %[scn], %[scn], 1\n\t"
        "s_cmp_gt_i32 %[scn], 0\n\t"
        "s_cbranch_scc1 Lk_%=\n\t"
        "s_waitcnt lgkmcnt(0)\n\t"
        "Lj_%=:\n\t"
        "s_mov_b32 exec_hi, 0\n\t"
        "ds_write_b128 %[pa], v[84:87] offset:0\n\t"
        "ds_write_b128 %[pa], v[88:91] offset:512\n\t"
        "ds_write_b128 %[pa], v[92:95] offset:1024\n\t"
        "s_mov_b32 exec_hi, -1\n\t"
        "Lw_%=:\n\t"
        "ds_read_b128 v[76:79], %[ff]\n\t"
        "ds_read2st64_b32 v[60:61], v42 offset0:0 offset1:6\n\t"
        "ds_read2st64_b32 v[62:63], %[pg] offset0:0 offset1:6\n\t"
        "ds_read2st64_b32 v[64:65], v43 offset0:0 offset1:6\n\t"
        "ds_read2st64_b32 v[66:67], %[pu] offset0:0 offset1:6\n\t"
        "ds_read2st64_b32 v[68:69], v42 offset0:12 offset1:18\n\t"
        "ds_read2st64_b32 v[70:71], %[pg] offset0:12 offset1:18\n\t"
        "ds_read2st64_b32 v[72:73], v43 offset0:12 offset1:18\n\t"
        "ds_read2st64_b32 v[74:75], %[pu] offset0:12 offset1:18\n\t"
        "s_sub_u32 %[scn], %[scn], 1\n\t"
        "s_waitcnt lgkmcnt(8)\n\t"
        "v_min3_u32 v40, v76, v77, v78\n\t"
        "v_cmp_gt_u32 vcc, %[sl1], v40\n\t"
        "s_cmp_lt_i32 %[scn], 1\n\t"
        "s_cbranch_scc1 Lg_%=\n\t"
        "s_cbranch_vccnz Lw_%=\n\t"
        "Lg_%=:\n\t"
        "s_waitcnt lgkmcnt(0)\n\t"
        "v_add_f32 v60, v60, v61\n\t"
        "v_add_f32 v62, v62, v63\n\t"
        "v_add_f32 v64, v64, v65\n\t"
        "v_add_f32 v66, v66, v67\n\t"
        "v_add_f32 v68, v68, v69\n\t"
        "v_add_f32 v70, v70, v71\n\t"
        "v_add_f32 v72, v72, v73\n\t"
        "v_add_f32 v74, v74, v75\n\t"
        "v_add_f32 v60, v60, v68\n\t"
        "v_add_f32 v62, v62, v70\n\t"
        "v_add_f32 v64, v64, v72\n\t"
        "v_add_f32 v66, v66, v74\n\t"
        "v_fma_f32 v42, -%[sm], v62, v60\n\t"
        "v_fma_f32 v43, -%[sm], v66, v64\n\t"
        "v_mul_f32 v42, %[sr], v42\n\t"
        "v_mul_f32 v43, %[sr], v43\n\t"
        "v_mul_f32 v44, 0xbfb8aa3b, v42\n\t"
        "v_exp_f32 v44, v44\n\t"
        "v_mul_f32 v42, v42, v43\n\t"
        "v_add_f32 v44, 1.0, v44\n\t"
        "v_rcp_f32 v44, v44\n\t"
        "v_mov_b32 v98, %[sm]\n\t"
        "v_mov_b32 v99, %[sr]\n\t"
        "v_mul_f32 v42, v42, v44\n\t"
        "v_mov_b32 v60, 0\n\t"
        "v_mov_b32 v61, 0\n\t"
        "v_mov_b32_dpp v43, v42 row_shl:1 row_mask:0xf bank_mask:0xf bound_ctrl:1\n\t"
        "v_cvt_pk_bf16_f32 v42, v42, v43\n\t"
        "v_mov_b32 v62, 0\n\t"
        "v_readlane_b32 %[sa0], v42, 0\n\t"
        "v_mov_b32 v63, 0\n\t"
        "v_readlane_b32 %[sa1], v42, 2\n\t"
        "v_mov_b32 v64, 0\n\t"
        "v_readlane_b32 %[sa2], v42, 4\n\t"
        "v_mov_b32 v65, 0\n\t"
        "v_readlane_b32 %[sa3], v42, 6\n\t"
        "v_mov_b32 v66, 0\n\t"
        "v_readlane_b32 %[sa4], v42, 8\n\t"
        "v_mov_b32 v67, 0\n\t"
        "v_readlane_b32 %[sa5], v42, 10\n\t"
        "v_mov_b32 v68, 0\n\t"
        "v_readlane_b32 %[sa6], v42, 12\n\t"
        "v_readlane_b32 %[sa7], v42, 14\n\t"
        "v_readlane_b32 %[sa8], v42, 16\n\t"
        "v_readlane_b32 %[sa9], v42, 18\n\t"
        "v_readlane_b32 %[sa10], v42, 20\n\t"
        "v_readlane_b32 %[sa11], v42, 22\n\t"
        "v_dot2c_f32_bf16 v60, %[sa0], v112\n\t"
        "v_dot2c_f32_bf16 v61, %[sa0], v116\n\t"
        "v_dot2c_f32_bf16 v62, %[sa0], v113\n\t"
        "v_dot2c_f32_bf16 v63, %[sa0], v117\n\t"
        "v_dot2c_f32_bf16 v60, %[sa1], v120\n\t"
        "v_dot2c_f32_bf16 v61, %[sa1], v124\n\t"
        "v_dot2c_f32_bf16 v62, %[sa1], v121\n\t"
        "v_dot2c_f32_bf16 v63, %[sa1], v125\n\t"
        "v_dot2c_f32_bf16 v60, %[sa2], v128\n\t"
        "v_dot2c_f32_bf16 v61, %[sa2], v132\n\t"
        "v_dot2c_f32_bf16 v62, %[sa2], v129\n\t"
        "v_dot2c_f32_bf16 v63, %[sa2], v133\n\t"
        "v_dot2c_f32_bf16 v60, %[sa3], v136\n\t"
        "v_dot2c_f32_bf16 v61, %[sa3], v140\n\t"
        "v_dot2c_f32_bf16 v62, %[sa3], v137\n\t"
        "v_dot2c_f32_bf16 v63, %[sa3], v141\n\t"
        "v_dot2c_f32_bf16 v60, %[sa4], v144\n\t"
        "v_dot2c_f32_bf16 v61, %[sa4], v148\n\t"
        "v_dot2c_f32_bf16 v62, %[sa4], v145\n\t"
        "v_dot2c_f32_bf16 v63, %[sa4], v149\n\t"
        "v_dot2c_f32_bf16 v60, %[sa5], v152\n\t"
        "v_dot2c_f32_bf16 v61, %[sa5], v156\n\t"
        "v_dot2c_f32_bf16 v62, %[sa5], v153\n\t"
        "v_dot2c_f32_bf16 v63, %[sa5], v157\n\t"
        "v_dot2c_f32_bf16 v60, %[sa6], v160\n\t"
        "v_dot2c_f32_bf16 v61, %[sa6], v164\n\t"
        "v_dot2c_f32_bf16 v62, %[sa6], v161\n\t"
        "v_dot2c_f32_bf16 v63, %[sa6], v165\n\t"
        "v_dot2c_f32_bf16 v60, %[sa7], v168\n\t"
        "v_dot2c_f32_bf16 v61, %[sa7], v172\n\t"
        "v_dot2c_f32_bf16 v62, %[sa7], v169\n\t"
        "v_dot2c_f32_bf16 v63, %[sa7], v173\n\t"
        "v_dot2c_f32_bf16 v60, %[sa8], v176\n\t"
        "v_dot2c_f32_bf16 v61, %[sa8], v180\n\t"
        "v_dot2c_f32_bf16 v62, %[sa8], v177\n\t"
        "v_dot2c_f32_bf16 v63, %[sa8], v181\n\t"
        "v_dot2c_f32_bf16 v60, %[sa9], v184\n\t"
        "v_dot2c_f32_bf16 v61, %[sa9], v188\n\t"
        "v_dot2c_f32_bf16 v62, %[sa9], v185\n\t"
        "v_dot2c_f32_bf16 v63, %[sa9], v189\n\t"
        "v_dot2c_f32_bf16 v60, %[sa10], v192\n\t"
        "v_dot2c_f32_bf16 v61, %[sa10], v196\n\t"
        "v_dot2c_f32_bf16 v62, %[sa10], v193\n\t"
        "v_dot2c_f32_bf16 v63, %[sa10], v197\n\t"
        "v_dot2c_f32_bf16 v60, %[sa11], v200\n\t"
        "v_dot2c_f32_bf16 v61, %[sa11], v204\n\t"
        "v_dot2c_f32_bf16 v62, %[sa11], v201\n\t"
        "v_dot2c_f32_bf16 v63, %[sa11], v205\n\t"
        "v_dot2c_f32_bf16 v64, %[sa0], v114\n\t"
        "v_dot2c_f32_bf16 v65, %[sa0], v118\n\t"
        "v_dot2c_f32_bf16 v66, %[sa0], v115\n\t"
        "v_dot2c_f32_bf16 v67, %[sa0], v119\n\t"
        "v_dot2c_f32_bf16 v68, %[sa0], v208\n\t"
        "v_min_u32 v60, -2, v60\n\t"
        "v_min_u32 v61, -2, v61\n\t"
        "v_min_u32 v62, -2, v62\n\t"
        "v_min_u32 v63, -2, v63\n\t"
        "buffer_store_dwordx4 v[60:63], %[ya], %[ssrd], %[syb] offen sc1\n\t"
        "v_dot2c_f32_bf16 v64, %[sa1], v122\n\t"
        "v_dot2c_f32_bf16 v65, %[sa1], v126\n\t"
        "v_dot2c_f32_bf16 v66, %[sa1], v123\n\t"
        "v_dot2c_f32_bf16 v67, %[sa1], v127\n\t"
        "v_dot2c_f32_bf16 v68, %[sa1], v209\n\t"
        "v_dot2c_f32_bf16 v64, %[sa2], v130\n\t"
        "v_dot2c_f32_bf16 v65, %[sa2], v134\n\t"
        "v_dot2c_f32_bf16 v66, %[sa2], v131\n\t"
        "v_dot2c_f32_bf16 v67, %[sa2], v135\n\t"
        "v_dot2c_f32_bf16 v68, %[sa2], v210\n\t"
        "v_dot2c_f32_bf16 v64, %[sa3], v138\n\t"
        "v_dot2c_f32_bf16 v65, %[sa3], v142\n\t"
        "v_dot2c_f32_bf16 v66, %[sa3], v139\n\t"
        "v_dot2c_f32_bf16 v67, %[sa3], v143\n\t"
        "v_dot2c_f32_bf16 v68, %[sa3], v211\n\t"
        "v_dot2c_f32_bf16 v64, %[sa4], v146\n\t"
        "v_dot2c_f32_bf16 v65, %[sa4], v150\n\t"
        "v_dot2c_f32_bf16 v66, %[sa4], v147\n\t"
        "v_dot2c_f32_bf16 v67, %[sa4], v151\n\t"
        "v_dot2c_f32_bf16 v68, %[sa4], v212\n\t"
        "v_dot2c_f32_bf16 v64, %[sa5], v154\n\t"
        "v_dot2c_f32_bf16 v65, %[sa5], v158\n\t"
        "v_dot2c_f32_bf16 v66, %[sa5], v155\n\t"
        "v_dot2c_f32_bf16 v67, %[sa5], v159\n\t"
        "v_dot2c_f32_bf16 v68, %[sa5], v213\n\t"
        "v_dot2c_f32_bf16 v64, %[sa6], v162\n\t"
        "v_dot2c_f32_bf16 v65, %[sa6], v166\n\t"
        "v_dot2c_f32_bf16 v66, %[sa6], v163\n\t"
        "v_dot2c_f32_bf16 v67, %[sa6], v167\n\t"
        "v_dot2c_f32_bf16 v68, %[sa6], v214\n\t"
        "v_dot2c_f32_bf16 v64, %[sa7], v170\n\t"
        "v_dot2c_f32_bf16 v65, %[sa7], v174\n\t"
        "v_dot2c_f32_bf16 v66, %[sa7], v171\n\t"
        "v_dot2c_f32_bf16 v67, %[sa7], v175\n\t"
        "v_dot2c_f32_bf16 v68, %[sa7], v215\n\t"
        "v_dot2c_f32_bf16 v64, %[sa8], v178\n\t"
        "v_dot2c_f32_bf16 v65, %[sa8], v182\n\t"
        "v_dot2c_f32_bf16 v66, %[sa8], v179\n\t"
        "v_dot2c_f32_bf16 v67, %[sa8], v183\n\t"
        "v_dot2c_f32_bf16 v68, %[sa8], v216\n\t"
        "v_dot2c_f32_bf16 v64, %[sa9], v186\n\t"
        "v_dot2c_f32_bf16 v65, %[sa9], v190\n\t"
        "v_dot2c_f32_bf16 v66, %[sa9], v187\n\t"
        "v_dot2c_f32_bf16 v67, %[sa9], v191\n\t"
        "v_dot2c_f32_bf16 v68, %[sa9], v217\n\t"
        "v_dot2c_f32_bf16 v64, %[sa10], v194\n\t"
        "v_dot2c_f32_bf16 v65, %[sa10], v198\n\t"
        "v_dot2c_f32_bf16 v66, %[sa10], v195\n\t"
        "v_dot2c_f32_bf16 v67, %[sa10], v199\n\t"
        "v_dot2c_f32_bf16 v68, %[sa10], v218\n\t"
        "v_dot2c_f32_bf16 v64, %[sa11], v202\n\t"
        "v_dot2c_f32_bf16 v65, %[sa11], v206\n\t"
        "v_dot2c_f32_bf16 v66, %[sa11], v203\n\t"
        "v_dot2c_f32_bf16 v67, %[sa11], v207\n\t"
        "v_dot2c_f32_bf16 v68, %[sa11], v219\n\t"
        "ds_write_b32 %[ff], %[ff] offset:12\n\t"
        "v_min_u32 v64, -2, v64\n\t"
        "v_min_u32 v65, -2, v65\n\t"
        "v_min_u32 v66, -2, v66\n\t"
        "v_min_u32 v67, -2, v67\n\t"
        "v_min_u32 v68, -2, v68\n\t"
        "buffer_store_dwordx4 v[64:67], %[ya], %[ssrd], %[syc] offen sc1\n\t"
        "buffer_store_dword v68, %[ye], %[ssrd], %[syb] offen sc1\n\t"
        "v_mul_f32 v98, -v98, v99\n\t"
        "s_waitcnt vmcnt(3)\n\t"
        "Lc_%=:\n\t"
        "v_max3_u32 v40, v24, v25, v26\n\t"
        "v_max3_u32 v40, v40, v27, v28\n\t"
        "v_max3_u32 v40, v40, v29, v30\n\t"
        "v_max_u32 v40, v40, v31\n\t"
        "v_cmp_eq_u32 vcc, -1, v40\n\t"
        "s_cbranch_vccnz Ly_%=\n\t"
        "Lz_%=:\n\t"
        "v_lshlrev_b32 v40, 16, v24\n\t"
        "v_and_b32 v41, 0xffff0000, v24\n\t"
        "v_fma_f32 v40, v40, v99, v98\n\t"
        "v_fma_f32 v41, v41, v99, v98\n\t"
        "v_cvt_pk_bf16_f32 v32, v40, v41\n\t"
        "v_lshlrev_b32 v40, 16, v25\n\t"
        "v_and_b32 v41, 0xffff0000, v25\n\t"
        "v_fma_f32 v40, v40, v99, v98\n\t"
        "v_fma_f32 v41, v41, v99, v98\n\t"
        "v_cvt_pk_bf16_f32 v33, v40, v41\n\t"
        "v_lshlrev_b32 v40, 16, v26\n\t"
        "v_and_b32 v41, 0xffff0000, v26\n\t"
        "v_fma_f32 v40, v40, v99, v98\n\t"
        "v_fma_f32 v41, v41, v99, v98\n\t"
        "v_cvt_pk_bf16_f32 v34, v40, v41\n\t"
        "v_lshlrev_b32 v40, 16, v27\n\t"
        "v_and_b32 v41, 0xffff0000, v27\n\t"
        "v_fma_f32 v40, v40, v99, v98\n\t"
        "v_fma_f32 v41, v41, v99, v98\n\t"
        "v_cvt_pk_bf16_f32 v35, v40, v41\n\t"
        "v_lshlrev_b32 v40, 16, v28\n\t"
        "v_and_b32 v41, 0xffff0000, v28\n\t"
        "v_fma_f32 v40, v40, v99, v98\n\t"
        "v_fma_f32 v41, v41, v99, v98\n\t"
        "v_cvt_pk_bf16_f32 v36, v40, v41\n\t"
        "v_lshlrev_b32 v40, 16, v29\n\t"
        "v_and_b32 v41, 0xffff0000, v29\n\t"
        "v_fma_f32 v40, v40, v99, v98\n\t"
        "v_fma_f32 v41, v41, v99, v98\n\t"
        "v_cvt_pk_bf16_f32 v37, v40, v41\n\t"
        "v_lshlrev_b32 v40, 16, v30\n\t"
        "v_and_b32 v41, 0xffff0000, v30\n\t"
        "v_fma_f32 v40, v40, v99, v98\n\t"
        "v_fma_f32 v41, v41, v99, v98\n\t"
        "v_cvt_pk_bf16_f32 v38, v40, v41\n\t"
        "v_lshlrev_b32 v40, 16, v31\n\t"
        "v_and_b32 v41, 0xffff0000, v31\n\t"
        "v_fma_f32 v40, v40, v99, v98\n\t"
        "v_fma_f32 v41, v41, v99, v98\n\t"
        "v_cvt_pk_bf16_f32 v39, v40, v41\n\t"
        "v_add_u32 v40, 155168, v107\n\t"
        "ds_write_b128 v40, v[32:35]\n\t"
        "ds_write_b128 v40, v[24:27] offset:1152\n\t"
        "s_mov_b64 exec, 0xff\n\t"
        "ds_write_b128 v40, v[36:39] offset:1024\n\t"
        "ds_write_b128 v40, v[28:31] offset:2176\n\t"
        "s_mov_b64 exec, -1\n\t"
        "s_waitcnt lgkmcnt(0)\n\t"
        "s_branch Le_%=\n\t"
        "Lx_%=:\n\t"
        "s_cmp_lt_i32 %[scn], 1\n\t"
        "s_cbranch_scc1 Ld_%=\n\t"
        "s_sub_u32 %[scn], %[scn], 1\n\t"
        "s_mov_b64 exec, %[sm7]\n\t"
        "buffer_load_dwordx4 v[60:63], %[ra], %[ssrd], 0 offen offset:0 sc1\n\t"
        "buffer_load_dwordx4 v[64:67], %[ra], %[ssrd], 0 offen offset:64 sc1\n\t"
        "buffer_load_dwordx4 v[68:71], %[ra], %[ssrd], 0 offen offset:128 sc1\n\t"
        "s_mov_b64 exec, -1\n\t"
        "s_branch Lr_%=\n\t"
        "Ly_%=:\n\t"
        "s_cmp_lt_i32 %[scn], 1\n\t"
        "s_cbranch_scc1 Lz_%=\n\t"
        "s_sub_u32 %[scn], %[scn], 1\n\t"
        "buffer_load_dwordx4 v[24:27], v107, %[ssrd], %[scr] offen sc1\n\t"
        "buffer_load_dwordx4 v[28:31], %[c1], %[ssrd], %[scr] offen sc1\n\t"
        "s_waitcnt vmcnt(0)\n\t"
        "s_branch Lc_%=\n\t"
        "Le_%=:\n\t"
        : [sa0] "=&s"(ap[0]), [sa1] "=&s"(ap[1]), [sa2] "=&s"(ap[2]), [sa3] "=&s"(ap[3]), [sa4] "=&s"(ap[4]), [sa5] "=&s"(ap[5]), [sa6] "=&s"(ap[6]), [sa7] "=&s"(ap[7]), [sa8] "=&s"(ap[8]), [sa9] "=&s"(ap[9]), [sa10] "=&s"(ap[10]), [sa11] "=&s"(ap[11]), [scn] "+s"(cn)
        : [ssrd] "s"(srd), [scr] "s"(cr), [ra] "v"(ra), [c1] "v"(c1), [pa] "v"(pa), [ff] "v"(ff), [pg] "v"(pg), [pu] "v"(pu), [ya] "v"(ya), [ye] "v"(ye),
          [syb] "s"(yb), [syc] "s"(yb + 262144), [swo] "s"(wo), [sm] "s"(m), [sr] "s"(r), [sl1] "s"(l1), [sm7] "s"(0x007F007F007F007Full)
        : "memory", "scc", "vcc", "v24", "v25", "v26", "v27", "v28", "v29", "v30", "v31", "v32", "v33", "v34", "v35", "v36", "v37", "v38", "v39", "v40", "v41", "v42", "v43", "v44", "v45", "v46", "v47");
}

DEV void poll_stats(u32x4 srd, u32 va, u32 so, float (&o)[3], u32& cn) {
    u32 t;
    asm volatile(
        "s_nop 4\n\t"
        "s_sleep 12\n\t"
        "buffer_load_dwordx4 v[48:51], %[va], %[ssrd], %[so] offen offset:0 sc1\n\t"
        "buffer_load_dwordx4 v[52:55], %[va], %[ssrd], %[so] offen offset:256 sc1\n\t"
        "buffer_load_dwordx4 v[56:59], %[va], %[ssrd], %[so] offen offset:512 sc1\n\t"
        "buffer_load_dwordx4 v[60:63], %[va], %[ssrd], %[so] offen offset:768 sc1\n\t"
        "buffer_load_dwordx4 v[64:67], %[va], %[ssrd], %[so] offen offset:1024 sc1\n\t"
        "buffer_load_dwordx4 v[68:71], %[va], %[ssrd], %[so] offen offset:1280 sc1\n\t"
        "buffer_load_dwordx4 v[72:75], %[va], %[ssrd], %[so] offen offset:1536 sc1\n\t"
        "buffer_load_dwordx4 v[76:79], %[va], %[ssrd], %[so] offen offset:1792 sc1\n\t"
        "Lp_%=:\n\t"
        "s_waitcnt vmcnt(0)\n\t"
        "v_max_u32 %[t], v48, v49\n\t"
        "v_max3_u32 %[t], %[t], v50, v52\n\t"
        "v_max3_u32 %[t], %[t], v53, v54\n\t"
        "v_max3_u32 %[t], %[t], v56, v57\n\t"
        "v_max3_u32 %[t], %[t], v58, v60\n\t"
        "v_max3_u32 %[t], %[t], v61, v62\n\t"
        "v_max3_u32 %[t], %[t], v64, v65\n\t"
        "v_max3_u32 %[t], %[t], v66, v68\n\t"
        "v_max3_u32 %[t], %[t], v69, v70\n\t"
        "v_max3_u32 %[t], %[t], v72, v73\n\t"
        "v_max3_u32 %[t], %[t], v74, v76\n\t"
        "v_max3_u32 %[t], %[t], v77, v78\n\t"
        "v_cmp_eq_u32 vcc, -1, %[t]\n\t"
        "s_cbranch_vccz Lf0_%=\n\t"
        "buffer_load_dwordx4 v[48:51], %[va], %[ssrd], %[so] offen offset:0 sc1\n\t"
        "buffer_load_dwordx4 v[52:55], %[va], %[ssrd], %[so] offen offset:256 sc1\n\t"
        "buffer_load_dwordx4 v[56:59], %[va], %[ssrd], %[so] offen offset:512 sc1\n\t"
        "buffer_load_dwordx4 v[60:63], %[va], %[ssrd], %[so] offen offset:768 sc1\n\t"
        "buffer_load_dwordx4 v[64:67], %[va], %[ssrd], %[so] offen offset:1024 sc1\n\t"
        "buffer_load_dwordx4 v[68:71], %[va], %[ssrd], %[so] offen offset:1280 sc1\n\t"
        "buffer_load_dwordx4 v[72:75], %[va], %[ssrd], %[so] offen offset:1536 sc1\n\t"
        "buffer_load_dwordx4 v[76:79], %[va], %[ssrd], %[so] offen offset:1792 sc1\n\t"
        "s_sub_u32 %[scn], %[scn], 1\n\t"
        "s_cmp_lg_u32 %[scn], 0\n\t"
        "s_cbranch_scc1 Lp_%=\n\t"
        "s_waitcnt vmcnt(0)\n\t"
        "Lf0_%=:\n\t"
        "v_add_f32 v48, v48, v64\n\t"
        "v_add_f32 v49, v49, v65\n\t"
        "v_add_f32 v50, v50, v66\n\t"
        "v_add_f32 v52, v52, v68\n\t"
        "v_add_f32 v53, v53, v69\n\t"
        "v_add_f32 v54, v54, v70\n\t"
        "v_add_f32 v56, v56, v72\n\t"
        "v_add_f32 v57, v57, v73\n\t"
        "v_add_f32 v58, v58, v74\n\t"
        "v_add_f32 v60, v60, v76\n\t"
        "v_add_f32 v61, v61, v77\n\t"
        "v_add_f32 v62, v62, v78\n\t"
        "v_add_f32 v48, v48, v52\n\t"
        "v_add_f32 v49, v49, v53\n\t"
        "v_add_f32 v50, v50, v54\n\t"
        "v_add_f32 v56, v56, v60\n\t"
        "v_add_f32 v57, v57, v61\n\t"
        "v_add_f32 v58, v58, v62\n\t"
        "v_add_f32 %[o0], v48, v56\n\t"
        "v_add_f32 %[o1], v49, v57\n\t"
        "v_add_f32 %[o2], v50, v58\n\t"
        "Le_%=:\n\t"
        : [o0] "=&v"(o[0]), [o1] "=&v"(o[1]), [o2] "=&v"(o[2]), [t] "=&v"(t), [scn] "+s"(cn)
        : [ssrd] "s"(srd), [va] "v"(va), [so] "s"(so)
        : "memory", "scc", "vcc");
}
DEV void poll_keye(u32x4 srd, u32 so, u32 tag, u32 layer, int g0, int g1, int g2, u32 (&o)[2], u32& cn) {
    u32 t;
    asm volatile(
        "s_nop 4\n\t"
        "s_sleep 2\n\t"
        "buffer_load_dwordx4 v[84:87], v107, %[ssrd], %[so] offen sc1\n\t"
        "s_cmp_lg_u32 %[slay], 0\n\t"
        "s_cbranch_scc1 Lq_%=\n\t"
        "v_perm_b32 %[t], v116, v112, %[slo]\n\t"
        "v_perm_b32 v116, v116, v112, %[shi]\n\t"
        "v_mov_b32 v112, %[t]\n\t"
        "v_perm_b32 %[t], v117, v113, %[slo]\n\t"
        "v_perm_b32 v117, v117, v113, %[shi]\n\t"
        "v_mov_b32 v113, %[t]\n\t"
        "v_perm_b32 %[t], v118, v114, %[slo]\n\t"
        "v_perm_b32 v118, v118, v114, %[shi]\n\t"
        "v_mov_b32 v114, %[t]\n\t"
        "v_perm_b32 %[t], v119, v115, %[slo]\n\t"
        "v_perm_b32 v119, v119, v115, %[shi]\n\t"
        "v_mov_b32 v115, %[t]\n\t"
        "v_perm_b32 v208, v221, v220, %[slo]\n\t"
        "v_perm_b32 %[t], v124, v120, %[slo]\n\t"
        "v_perm_b32 v124, v124, v120, %[shi]\n\t"
        "v_mov_b32 v120, %[t]\n\t"
        "v_perm_b32 %[t], v125, v121, %[slo]\n\t"
        "v_perm_b32 v125, v125, v121, %[shi]\n\t"
        "v_mov_b32 v121, %[t]\n\t"
        "v_perm_b32 %[t], v126, v122, %[slo]\n\t"
        "v_perm_b32 v126, v126, v122, %[shi]\n\t"
        "v_mov_b32 v122, %[t]\n\t"
        "v_perm_b32 %[t], v127, v123, %[slo]\n\t"
        "v_perm_b32 v127, v127, v123, %[shi]\n\t"
        "v_mov_b32 v123, %[t]\n\t"
        "v_perm_b32 v209, v223, v222, %[slo]\n\t"
        "v_perm_b32 %[t], v132, v128, %[slo]\n\t"
        "v_perm_b32 v132, v132, v128, %[shi]\n\t"
        "v_mov_b32 v128, %[t]\n\t"
        "v_perm_b32 %[t], v133, v129, %[slo]\n\t"
        "v_perm_b32 v133, v133, v129, %[shi]\n\t"
        "v_mov_b32 v129, %[t]\n\t"
        "v_perm_b32 %[t], v134, v130, %[slo]\n\t"
        "v_perm_b32 v134, v134, v130, %[shi]\n\t"
        "v_mov_b32 v130, %[t]\n\t"
        "v_perm_b32 %[t], v135, v131, %[slo]\n\t"
        "v_perm_b32 v135, v135, v131, %[shi]\n\t"
        "v_mov_b32 v131, %[t]\n\t"
        "v_perm_b32 v210, v225, v224, %[slo]\n\t"
        "v_perm_b32 %[t], v140, v136, %[slo]\n\t"
        "v_perm_b32 v140, v140, v136, %[shi]\n\t"
        "v_mov_b32 v136, %[t]\n\t"
        "v_perm_b32 %[t], v141, v137, %[slo]\n\t"
        "v_perm_b32 v141, v141, v137, %[shi]\n\t"
        "v_mov_b32 v137, %[t]\n\t"
        "v_perm_b32 %[t], v142, v138, %[slo]\n\t"
        "v_perm_b32 v142, v142, v138, %[shi]\n\t"
        "v_mov_b32 v138, %[t]\n\t"
        "v_perm_b32 %[t], v143, v139, %[slo]\n\t"
        "v_perm_b32 v143, v143, v139, %[shi]\n\t"
        "v_mov_b32 v139, %[t]\n\t"
        "v_perm_b32 v211, v227, v226, %[slo]\n\t"
        "v_perm_b32 %[t], v148, v144, %[slo]\n\t"
        "v_perm_b32 v148, v148, v144, %[shi]\n\t"
        "v_mov_b32 v144, %[t]\n\t"
        "v_perm_b32 %[t], v149, v145, %[slo]\n\t"
        "v_perm_b32 v149, v149, v145, %[shi]\n\t"
        "v_mov_b32 v145, %[t]\n\t"
        "v_perm_b32 %[t], v150, v146, %[slo]\n\t"
        "v_perm_b32 v150, v150, v146, %[shi]\n\t"
        "v_mov_b32 v146, %[t]\n\t"
        "v_perm_b32 %[t], v151, v147, %[slo]\n\t"
        "v_perm_b32 v151, v151, v147, %[shi]\n\t"
        "v_mov_b32 v147, %[t]\n\t"
        "v_perm_b32 v212, v229, v228, %[slo]\n\t"
        "v_perm_b32 %[t], v156, v152, %[slo]\n\t"
        "v_perm_b32 v156, v156, v152, %[shi]\n\t"
        "v_mov_b32 v152, %[t]\n\t"
        "v_perm_b32 %[t], v157, v153, %[slo]\n\t"
        "v_perm_b32 v157, v157, v153, %[shi]\n\t"
        "v_mov_b32 v153, %[t]\n\t"
        "v_perm_b32 %[t], v158, v154, %[slo]\n\t"
        "v_perm_b32 v158, v158, v154, %[shi]\n\t"
        "v_mov_b32 v154, %[t]\n\t"
        "v_perm_b32 %[t], v159, v155, %[slo]\n\t"
        "v_perm_b32 v159, v159, v155, %[shi]\n\t"
        "v_mov_b32 v155, %[t]\n\t"
        "v_perm_b32 v213, v231, v230, %[slo]\n\t"
        "v_perm_b32 %[t], v164, v160, %[slo]\n\t"
        "v_perm_b32 v164, v164, v160, %[shi]\n\t"
        "v_mov_b32 v160, %[t]\n\t"
        "v_perm_b32 %[t], v165, v161, %[slo]\n\t"
        "v_perm_b32 v165, v165, v161, %[shi]\n\t"
        "v_mov_b32 v161, %[t]\n\t"
        "v_perm_b32 %[t], v166, v162, %[slo]\n\t"
        "v_perm_b32 v166, v166, v162, %[shi]\n\t"
        "v_mov_b32 v162, %[t]\n\t"
        "v_perm_b32 %[t], v167, v163, %[slo]\n\t"
        "v_perm_b32 v167, v167, v163, %[shi]\n\t"
        "v_mov_b32 v163, %[t]\n\t"
        "v_perm_b32 v214, v233, v232, %[slo]\n\t"
        "v_perm_b32 %[t], v172, v168, %[slo]\n\t"
        "v_perm_b32 v172, v172, v168, %[shi]\n\t"
        "v_mov_b32 v168, %[t]\n\t"
        "v_perm_b32 %[t], v173, v169, %[slo]\n\t"
        "v_perm_b32 v173, v173, v169, %[shi]\n\t"
        "v_mov_b32 v169, %[t]\n\t"
        "v_perm_b32 %[t], v174, v170, %[slo]\n\t"
        "v_perm_b32 v174, v174, v170, %[shi]\n\t"
        "v_mov_b32 v170, %[t]\n\t"
        "v_perm_b32 %[t], v175, v171, %[slo]\n\t"
        "v_perm_b32 v175, v175, v171, %[shi]\n\t"
        "v_mov_b32 v171, %[t]\n\t"
        "v_perm_b32 v215, v235, v234, %[slo]\n\t"
        "v_perm_b32 %[t], v180, v176, %[slo]\n\t"
        "v_perm_b32 v180, v180, v176, %[shi]\n\t"
        "v_mov_b32 v176, %[t]\n\t"
        "v_perm_b32 %[t], v181, v177, %[slo]\n\t"
        "v_perm_b32 v181, v181, v177, %[shi]\n\t"
        "v_mov_b32 v177, %[t]\n\t"
        "v_perm_b32 %[t], v182, v178, %[slo]\n\t"
        "v_perm_b32 v182, v182, v178, %[shi]\n\t"
        "v_mov_b32 v178, %[t]\n\t"
        "v_perm_b32 %[t], v183, v179, %[slo]\n\t"
        "v_perm_b32 v183, v183, v179, %[shi]\n\t"
        "v_mov_b32 v179, %[t]\n\t"
        "v_perm_b32 v216, v237, v236, %[slo]\n\t"
        "v_perm_b32 %[t], v188, v184, %[slo]\n\t"
        "v_perm_b32 v188, v188, v184, %[shi]\n\t"
        "v_mov_b32 v184, %[t]\n\t"
        "v_perm_b32 %[t], v189, v185, %[slo]\n\t"
        "v_perm_b32 v189, v189, v185, %[shi]\n\t"
        "v_mov_b32 v185, %[t]\n\t"
        "v_perm_b32 %[t], v190, v186, %[slo]\n\t"
        "v_perm_b32 v190, v190, v186, %[shi]\n\t"
        "v_mov_b32 v186, %[t]\n\t"
        "v_perm_b32 %[t], v191, v187, %[slo]\n\t"
        "v_perm_b32 v191, v191, v187, %[shi]\n\t"
        "v_mov_b32 v187, %[t]\n\t"
        "v_perm_b32 v217, v239, v238, %[slo]\n\t"
        "v_perm_b32 %[t], v196, v192, %[slo]\n\t"
        "v_perm_b32 v196, v196, v192, %[shi]\n\t"
        "v_mov_b32 v192, %[t]\n\t"
        "v_perm_b32 %[t], v197, v193, %[slo]\n\t"
        "v_perm_b32 v197, v197, v193, %[shi]\n\t"
        "v_mov_b32 v193, %[t]\n\t"
        "v_perm_b32 %[t], v198, v194, %[slo]\n\t"
        "v_perm_b32 v198, v198, v194, %[shi]\n\t"
        "v_mov_b32 v194, %[t]\n\t"
        "v_perm_b32 %[t], v199, v195, %[slo]\n\t"
        "v_perm_b32 v199, v199, v195, %[shi]\n\t"
        "v_mov_b32 v195, %[t]\n\t"
        "v_perm_b32 v218, v241, v240, %[slo]\n\t"
        "v_perm_b32 %[t], v204, v200, %[slo]\n\t"
        "v_perm_b32 v204, v204, v200, %[shi]\n\t"
        "v_mov_b32 v200, %[t]\n\t"
        "v_perm_b32 %[t], v205, v201, %[slo]\n\t"
        "v_perm_b32 v205, v205, v201, %[shi]\n\t"
        "v_mov_b32 v201, %[t]\n\t"
        "v_perm_b32 %[t], v206, v202, %[slo]\n\t"
        "v_perm_b32 v206, v206, v202, %[shi]\n\t"
        "v_mov_b32 v202, %[t]\n\t"
        "v_perm_b32 %[t], v207, v203, %[slo]\n\t"
        "v_perm_b32 v207, v207, v203, %[shi]\n\t"
        "v_mov_b32 v203, %[t]\n\t"
        "v_perm_b32 v219, v243, v242, %[slo]\n\t"
        "v_mov_b32 %[o1], 153984\n\t"
        "Lw_%=:\n\t"
        "ds_read_b32 %[t], %[o1] offset:%[owc]\n\t"
        "s_sub_u32 %[scn], %[scn], 1\n\t"
        "s_waitcnt lgkmcnt(0)\n\t"
        "v_cmp_gt_u32 vcc, 4, %[t]\n\t"
        "s_cmp_lt_u32 %[scn], 2\n\t"
        "s_cbranch_scc1 Lu_%=\n\t"
        "s_cbranch_vccnz Lw_%=\n\t"
        "Lu_%=:\n\t"
        "ds_read_b64_tr_b16 v[220:221], %[g0] offset:0\n\t"
        "ds_read_b64_tr_b16 v[222:223], %[g0] offset:384\n\t"
        "ds_read_b64_tr_b16 v[224:225], %[g1] offset:0\n\t"
        "ds_read_b64_tr_b16 v[226:227], %[g1] offset:384\n\t"
        "ds_read_b64_tr_b16 v[228:229], %[g2] offset:0\n\t"
        "ds_read_b64_tr_b16 v[230:231], %[g2] offset:384\n\t"
        "ds_read_b64_tr_b16 v[232:233], %[g0] offset:1536\n\t"
        "ds_read_b64_tr_b16 v[234:235], %[g0] offset:1920\n\t"
        "ds_read_b64_tr_b16 v[236:237], %[g1] offset:1536\n\t"
        "ds_read_b64_tr_b16 v[238:239], %[g1] offset:1920\n\t"
        "ds_read_b64_tr_b16 v[240:241], %[g2] offset:1536\n\t"
        "ds_read_b64_tr_b16 v[242:243], %[g2] offset:1920\n\t"
        "ds_read_b64_tr_b16 v[244:245], %[g0] offset:3072\n\t"
        "ds_read_b64_tr_b16 v[246:247], %[g0] offset:3456\n\t"
        "ds_read_b64_tr_b16 v[248:249], %[g1] offset:3072\n\t"
        "ds_read_b64_tr_b16 v[250:251], %[g1] offset:3456\n\t"
        "ds_read_b64_tr_b16 v[252:253], %[g2] offset:3072\n\t"
        "ds_read_b64_tr_b16 v[254:255], %[g2] offset:3456\n\t"
        "Lq_%=:\n\t"
        "v_mov_b32 v60, 0x3f803f80\n\t"
        "v_mov_b32 v61, 0x3f803f80\n\t"
        "v_mov_b32 v62, 0x3f803f80\n\t"
        "v_mov_b32 v63, 0x3f803f80\n\t"
        "v_mov_b32 v64, 0x3f803f80\n\t"
        "v_mov_b32 v65, 0x3f803f80\n\t"
        "v_mov_b32 v66, 0x3f803f80\n\t"
        "v_mov_b32 v67, 0x3f803f80\n\t"
        "v_mov_b32 v68, 0x3f803f80\n\t"
        "v_mov_b32 v69, 0x3f803f80\n\t"
        "v_mov_b32 v70, 0x3f803f80\n\t"
        "v_mov_b32 v71, 0x3f803f80\n\t"
        "s_waitcnt lgkmcnt(0)\n\t"
        "s_setprio 3\n\t"
        "Lp_%=:\n\t"
        "s_waitcnt vmcnt(0)\n\t"
        "v_xor_b32 %[t], %[stag], v85\n\t"
        "v_xor_b32 %[o0], %[stag], v87\n\t"
        "v_or_b32 %[t], %[t], %[o0]\n\t"
        "v_cmp_ne_u32 vcc, 0, %[t]\n\t"
        "s_cbranch_vccz Lf0_%=\n\t"
        "buffer_load_dwordx4 v[84:87], v107, %[ssrd], %[so] offen sc1\n\t"
        "s_sub_u32 %[scn], %[scn], 1\n\t"
        "s_cmp_lg_u32 %[scn], 0\n\t"
        "s_cbranch_scc1 Lp_%=\n\t"
        "s_waitcnt vmcnt(0)\n\t"
        "Lf0_%=:\n\t"
        "v_mov_b32 %[o0], v84\n\t"
        "v_mov_b32 %[o1], v86\n\t"
        "Le_%=:\n\t"
        : [o0] "=&v"(o[0]), [o1] "=&v"(o[1]), [t] "=&v"(t), [scn] "+s"(cn)
        : [ssrd] "s"(srd), [so] "s"(so), [stag] "s"(tag), [g0] "v"(g0), [g1] "v"(g1), [g2] "v"(g2), [slay] "s"(layer), [slo] "s"(0x05040100u), [shi] "s"(0x07060302u), [owc] "n"(BC_WC)
        : "memory", "scc", "vcc");
}
DEV void poll_flag4(u32x4 srd, u32 so, u32 tag, u32& cn) {
    u32 t, d0;
    asm volatile(
        "s_nop 4\n\t"
        "buffer_load_dwordx4 v[84:87], v107, %[ssrd], %[so] offen sc1\n\t"
        "Lp_%=:\n\t"
        "s_waitcnt vmcnt(0)\n\t"
        "v_xor_b32 %[t], %[stag], v84\n\t"
        "v_xor_b32 v85, %[stag], v85\n\t"
        "v_xor_b32 v86, %[stag], v86\n\t"
        "v_xor_b32 v87, %[stag], v87\n\t"
        "v_or3_b32 %[t], %[t], v85, v86\n\t"
        "v_or_b32 %[t], %[t], v87\n\t"
        "v_cmp_ne_u32 vcc, 0, %[t]\n\t"
        "s_cbranch_vccz Lf0_%=\n\t"
        "buffer_load_dwordx4 v[84:87], v107, %[ssrd], %[so] offen sc1\n\t"
        "s_sub_u32 %[scn], %[scn], 1\n\t"
        "s_cmp_lg_u32 %[scn], 0\n\t"
        "s_cbranch_scc1 Lp_%=\n\t"
        "s_waitcnt vmcnt(0)\n\t"
        "Lf0_%=:\n\t"
        "s_nop 0\n\t"
        "Le_%=:\n\t"
        : [d0] "=&s"(d0), [t] "=&v"(t), [scn] "+s"(cn)
        : [ssrd] "s"(srd), [so] "s"(so), [stag] "s"(tag)
        : "memory", "scc", "vcc");
}
DEV void poll_flags(u32x4 srd, u32 fo, u32 tag, u32& cn) {
    u32 t, d0;
    asm volatile(
        "s_nop 4\n\t"
        "s_sleep 5\n\t"
        "buffer_load_dword v48, %[fo], %[ssrd], 0 offen sc1\n\t"
        "Lp_%=:\n\t"
        "s_waitcnt vmcnt(0)\n\t"
        "v_cmp_ne_u32 vcc, %[stag], v48\n\t"
        "s_cbranch_vccz Lf0_%=\n\t"
        "buffer_load_dword v48, %[fo], %[ssrd], 0 offen sc1\n\t"
        "s_sub_u32 %[scn], %[scn], 1\n\t"
        "s_cmp_lg_u32 %[scn], 0\n\t"
        "s_cbranch_scc1 Lp_%=\n\t"
        "s_waitcnt vmcnt(0)\n\t"
        "Lf0_%=:\n\t"
        "s_nop 0\n\t"
        "Le_%=:\n\t"
        : [d0] "=&s"(d0), [t] "=&v"(t), [scn] "+s"(cn)
        : [ssrd] "s"(srd), [fo] "v"(fo), [stag] "s"(tag)
        : "memory", "scc", "vcc");
}
DEV void poll_flagx(u32x4 srd, u32 fo, u32 tag, u32& cn) {
    u32 t, d0;
    asm volatile(
        "s_nop 4\n\t"
        "s_sleep 16\n\t"
        "buffer_load_dword v48, %[fo], %[ssrd], 0 offen sc1\n\t"
        "Lp_%=:\n\t"
        "s_waitcnt vmcnt(0)\n\t"
        "v_cmp_ne_u32 vcc, %[stag], v48\n\t"
        "s_cbranch_vccz Lf0_%=\n\t"
        "buffer_load_dword v48, %[fo], %[ssrd], 0 offen sc1\n\t"
        "s_sub_u32 %[scn], %[scn], 1\n\t"
        "s_cmp_lg_u32 %[scn], 0\n\t"
        "s_cbranch_scc1 Lp_%=\n\t"
        "s_waitcnt vmcnt(0)\n\t"
        "Lf0_%=:\n\t"
        "s_nop 0\n\t"
        "Le_%=:\n\t"
        : [d0] "=&s"(d0), [t] "=&v"(t), [scn] "+s"(cn)
        : [ssrd] "s"(srd), [fo] "v"(fo), [stag] "s"(tag)
        : "memory", "scc", "vcc");
}
DEV void poll_flagh(u32x4 srd, u32 fo, u32 tag, u32& cn) {
    u32 t, d0;
    asm volatile(
        "s_nop 4\n\t"
        "buffer_load_dword v48, %[fo], %[ssrd], 0 offen sc1\n\t"
        "Lp_%=:\n\t"
        "s_waitcnt vmcnt(0)\n\t"
        "v_cmp_ne_u32 vcc, %[stag], v48\n\t"
        "s_cbranch_vccz Lf0_%=\n\t"
        "buffer_load_dword v48, %[fo], %[ssrd], 0 offen sc1\n\t"
        "s_sub_u32 %[scn], %[scn], 1\n\t"
        "s_cmp_lg_u32 %[scn], 0\n\t"
        "s_cbranch_scc1 Lp_%=\n\t"
        "s_waitcnt vmcnt(0)\n\t"
        "Lf0_%=:\n\t"
        "s_nop 0\n\t"
        "Le_%=:\n\t"
        : [d0] "=&s"(d0), [t] "=&v"(t), [scn] "+s"(cn)
        : [ssrd] "s"(srd), [fo] "v"(fo), [stag] "s"(tag)
        : "memory", "scc", "vcc");
}
DEV void poll_ky(u32x4 srd, u32 vk, u32 tag, u32 va, u32 so, u32& k, float (&o)[4], u32& cn) {
    u32 t; u64 m;
    asm volatile(
        "s_nop 4\n\t"
        "s_sleep 8\n\t"
        "buffer_load_dwordx4 v[64:67], %[vk], %[ssrd], 0 offen sc1\n\t"
        "buffer_load_dwordx4 v[48:51], %[va], %[ssrd], %[so0] offen sc1\n\t"
        "buffer_load_dwordx4 v[52:55], %[va], %[ssrd], %[so1] offen sc1\n\t"
        "buffer_load_dwordx4 v[56:59], %[va], %[ssrd], %[so2] offen sc1\n\t"
        "buffer_load_dwordx4 v[60:63], %[va], %[ssrd], %[so3] offen sc1\n\t"
        "Lp_%=:\n\t"
        "s_waitcnt vmcnt(0)\n\t"
        "v_cmp_ne_u32 vcc, %[stag], v65\n\t"
        "v_cmp_ne_u32 %[sm], %[stag], v67\n\t"
        "s_or_b64 %[sm], %[sm], vcc\n\t"
        "v_max3_u32 %[t], v48, v49, v50\n\t"
        "v_max3_u32 %[t], %[t], v51, v52\n\t"
        "v_max3_u32 %[t], %[t], v53, v54\n\t"
        "v_max3_u32 %[t], %[t], v55, v56\n\t"
        "v_max3_u32 %[t], %[t], v57, v58\n\t"
        "v_max3_u32 %[t], %[t], v59, v60\n\t"
        "v_max3_u32 %[t], %[t], v61, v62\n\t"
        "v_max_u32 %[t], %[t], v63\n\t"
        "v_cmp_eq_u32 vcc, -1, %[t]\n\t"
        "s_or_b64 vcc, %[sm], vcc\n\t"
        "s_cbranch_vccz Lf0_%=\n\t"
        "buffer_load_dwordx4 v[64:67], %[vk], %[ssrd], 0 offen sc1\n\t"
        "buffer_load_dwordx4 v[48:51], %[va], %[ssrd], %[so0] offen sc1\n\t"
        "buffer_load_dwordx4 v[52:55], %[va], %[ssrd], %[so1] offen sc1\n\t"
        "buffer_load_dwordx4 v[56:59], %[va], %[ssrd], %[so2] offen sc1\n\t"
        "buffer_load_dwordx4 v[60:63], %[va], %[ssrd], %[so3] offen sc1\n\t"
        "s_sub_u32 %[scn], %[scn], 1\n\t"
        "s_cmp_lg_u32 %[scn], 0\n\t"
        "s_cbranch_scc1 Lp_%=\n\t"
        "s_waitcnt vmcnt(0)\n\t"
        "Lf0_%=:\n\t"
        "v_max_u32 %[k], v64, v66\n\t"
        "v_add_f32 v48, v48, v52\n\t"
        "v_add_f32 v49, v49, v53\n\t"
        "v_add_f32 v50, v50, v54\n\t"
        "v_add_f32 v51, v51, v55\n\t"
        "v_add_f32 v56, v56, v60\n\t"
        "v_add_f32 v57, v57, v61\n\t"
        "v_add_f32 v58, v58, v62\n\t"
        "v_add_f32 v59, v59, v63\n\t"
        "v_add_f32 %[o0], v48, v56\n\t"
        "v_add_f32 %[o1], v49, v57\n\t"
        "v_add_f32 %[o2], v50, v58\n\t"
        "v_add_f32 %[o3], v51, v59\n\t"
        "Le_%=:\n\t"
        : [k] "=&v"(k), [sm] "=&s"(m), [o0] "=&v"(o[0]), [o1] "=&v"(o[1]), [o2] "=&v"(o[2]), [o3] "=&v"(o[3]), [t] "=&v"(t), [scn] "+s"(cn)
        : [ssrd] "s"(srd), [vk] "v"(vk), [stag] "s"(tag), [va] "v"(va), [so0] "s"(so + 0), [so1] "s"(so + 4096), [so2] "s"(so + 8192), [so3] "s"(so + 12288)
        : "memory", "scc", "vcc");
}
DEV void poll_kr(u32x4 srd, u32 v1, u32 v2, int h0, int h1, u32 (&o)[6], u32& cn) {
    u32 t;
    asm volatile(
        "s_nop 4\n\t"
        "s_sleep 12\n\t"
        "buffer_load_dwordx2 v[48:49], %[v1], %[ssrd], 0 offen sc1\n\t"
        "buffer_load_dwordx2 v[50:51], %[v1], %[ssrd], 0 offen offset:2048 sc1\n\t"
        "buffer_load_dwordx2 v[52:53], %[v2], %[ssrd], 0 offen sc1\n\t"
        "Lp_%=:\n\t"
        "s_waitcnt vmcnt(0)\n\t"
        "v_max3_u32 %[t], v48, v49, v50\n\t"
        "v_max3_u32 %[t], %[t], v51, v52\n\t"
        "v_max_u32 %[t], %[t], v53\n\t"
        "v_cmp_eq_u32 vcc, -1, %[t]\n\t"
        "s_cbranch_vccz Lf0_%=\n\t"
        "buffer_load_dwordx2 v[48:49], %[v1], %[ssrd], 0 offen sc1\n\t"
        "buffer_load_dwordx2 v[50:51], %[v1], %[ssrd], 0 offen offset:2048 sc1\n\t"
        "buffer_load_dwordx2 v[52:53], %[v2], %[ssrd], 0 offen sc1\n\t"
        "s_sub_u32 %[scn], %[scn], 1\n\t"
        "s_cmp_lg_u32 %[scn], 0\n\t"
        "s_cbranch_scc1 Lp_%=\n\t"
        "s_waitcnt vmcnt(0)\n\t"
        "Lf0_%=:\n\t"
        "ds_write_b128 %[h0], v[48:51]\n\t"
        "ds_write_b64 %[h1], v[52:53]\n\t"
        "v_mov_b32 %[o0], v48\n\t"
        "v_mov_b32 %[o1], v49\n\t"
        "v_mov_b32 %[o2], v50\n\t"
        "v_mov_b32 %[o3], v51\n\t"
        "v_mov_b32 %[o4], v52\n\t"
        "v_mov_b32 %[o5], v53\n\t"
        "Le_%=:\n\t"
        : [o0] "=&v"(o[0]), [o1] "=&v"(o[1]), [o2] "=&v"(o[2]), [o3] "=&v"(o[3]), [o4] "=&v"(o[4]), [o5] "=&v"(o[5]), [t] "=&v"(t), [scn] "+s"(cn)
        : [ssrd] "s"(srd), [v1] "v"(v1), [v2] "v"(v2), [h0] "v"(h0), [h1] "v"(h1)
        : "memory", "scc", "vcc");
}

DEV int ffn_slice(int b, int& nu) {
    const int j = b >> 3, t = j - 20, t3 = (t * 43) >> 7, r = t - 3 * t3;
    nu = (j >= 20 && r < 2) ? 24 : 16;
    return (b & 7) * 576 + (j < 20 ? j * 16 : 320 + t3 * 64 + r * 24);
}
DEV void gu_dma(const u16* wg, const u16* wu, int lane, int b, int i0, int i1) {
    int nu;
    const int f0 = ffn_slice(b, nu);
    u32 gv[3];
#pragma unroll
    for (int m = 0; m < 3; ++m) {
        const int q = 64 * m + lane, sg = (q * 171) >> 9, part = q - 3 * sg;
        const int kin = 32 * (sg >> 5) + 16 * ((sg >> 4) & 1) + 8 * ((sg >> 2) & 1) + 4 * ((sg >> 3) & 1) + (sg & 3);
        gv[m] = (u32)kin * 9216u + f0 * 2u + ((part == 2 && nu == 16) ? 16u : part * 16u);
    }
    sts128(L_HROW + lane * 16, (u32x4){gv[0], gv[1] - 1024, gv[2] - 2048, gv[0]});
#pragma nounroll
    for (int i = i0; i < i1; ++i) {
        int gi = i + ((b >> 3) & 3) * 4; if (gi >= 18) gi -= 18;
        const u32x4 sd = mk_srd(gi < 9 ? wg : wu, 5308416);
        asm volatile("" : "+s"(gi));
        int g9 = gi - 9 * ((gi * 57) >> 9);
        asm volatile("" : "+s"(g9));
        const u32 so = g9 * (64 * 9216), ld = L_G + gi * 3072;
#pragma unroll
        for (int m = 0; m < 3; ++m) dma16g(sd, gv[m], so, ld + m * 1024);
    }
}

struct RowS { float m0, var, m, rs; u32 key; };
DEV RowS row_key(const float (&t)[3], float wrsum, u32 idx) {
    RowS q;
    q.m0 = t[0] * (1.f / 576.f);
    q.var = fmaxf(__builtin_fmaf(t[1], 1.f / 576.f, -(q.m0 * q.m0)), 0.f);
    q.m = ln_m(q.m0); q.rs = ln_rs(q.var);
    const u32 sb = pk2(0.f, q.rs * __builtin_fmaf(-q.m, wrsum, t[2]));
    q.key = ((sb ^ ((u32)((int)sb >> 31) | 0x80000000u)) & 0xffff0000u) | idx | 0x800u;
    return q;
}
DEV u32 xt_update(u32 (&XT)[10], int lane, int w, u32 target) {
    xt_get(XT);
    LWAIT0(L_BC + BC_TF, target, 24);
    const u32x4 q = lds128(L_MX + (16 * w + (lane & 15)) * 16);
    const float qm = __uint_as_float(q.x), qr = __uint_as_float(q.y);
#pragma unroll
    for (int i = 0; i < 10; ++i) {
        const float x = lo16(XT[i]), y = hi16(XT[i]);
        const u32 h = pk2((x - qm) * qr, (y - qm) * qr);
        XT[i] = pk2(x + lo16(h), y + hi16(h));
    }
    return q.z;
}
DEV u32 wmax(u32 k) {
    k = rmax(k);
    return umax2(umax2(__builtin_amdgcn_readlane(k, 0), __builtin_amdgcn_readlane(k, 16)), umax2(__builtin_amdgcn_readlane(k, 32), __builtin_amdgcn_readlane(k, 48)));
}

DEV void gu_pf(const u16* wg, const u16* wu, int lane, int b, int i) {
    const int j = b >> 3;
    const u32x4 sg = mk_srd(j < 16 ? wg : wu, 5308416);
    const int q = i * 54 + lane, r = (q * 57) >> 9;
    const u32 o = lane < 54 ? (u32)((j & 15) * 36 + r) * 9216u + (b & 7) * 1152u + (q - 9 * r) * 128u : (u32)OOB;
    asm volatile("s_nop 4\n\tbuffer_load_dword v98, %0, %1, 0 offen" :: "v"(o), "s"(sg) : "memory");
}

DEV u32 w_voff(int wp, int r16, int g, int cb) {
    int ch = 16 * wp + r16; if (ch > 107) ch = 107;
    const int sec = (ch * 57) >> 11, c = ch - 36 * sec;
    return (u32)(8 * g) * 3456u + (sec == 2 ? 0u : (u32)(sec + 1) * 1152u) + cb * 144u + c * 4u;
}

__global__ void NOPK __attribute__((amdgpu_num_vgpr(24))) __launch_bounds__(512, 2)
mega(const u16* __restrict__ x0, const u16* __restrict__ wqkv, const u16* __restrict__ wo, const u16* __restrict__ wr,
     const u16* __restrict__ wg, const u16* __restrict__ wu, const u16* __restrict__ wd, u16* __restrict__ out, char* __restrict__ ws) {
    const int w_ = __builtin_amdgcn_readfirstlane(threadIdx.x >> 6);
    const int b_ = blockIdx.x;
    {
        u64 pc; u32 off;
        asm volatile("s_getpc_b64 %0\n"
                     "Lcode0:\n\t"
                     "v_lshlrev_b32 %1, 7, %2\n\t"
                     "v_and_b32 v99, 3, %2\n\t"
                     "v_cmp_gt_u32 vcc, Lcode1-Lcode0, %1\n\t"
                     "s_mov_b64 exec, vcc\n\t"
                     "v_cmp_eq_u32 vcc, %3, v99\n\t"
                     "s_and_b64 exec, exec, vcc\n\t"
                     "s_cbranch_execz Lcode2\n\t"
                     "global_load_dword v99, %1, %0\n"
                     "Lcode2:\n\t"
                     "s_mov_b64 exec, -1"
                     : "=&s"(pc), "=&v"(off) : "v"(threadIdx.x), "s"((b_ >> 3) & 3) : "vcc", "scc", "memory");
    }
    const __amdgpu_buffer_rsrc_t rs = __builtin_amdgcn_make_buffer_rsrc((void*)ws, 0, WS_BYTES, 0x00020000);
    asm volatile("v_mov_b32 v109, 0");
    u32 tag_;
    {
        IDS
        int u = rg * 4 + (w & 3);
        const bool uok = u < 126;
        if (u > 125) u = 125;
        const int su = (u * 147) >> 10, wp = u - 7 * su;
        u32 wv = w_voff(wp, r16, g, cb), so = (u32)su * 110592u;
        sts32(L_BC + lane * 4, 0u);
        asm volatile("" : "+v"(wv), "+s"(so));
        asm volatile("" :: "s"(x0), "s"(wqkv), "s"(wo), "s"(ws));
        wld8p(mk_srd(wqkv, 1990656), wv, so, w);
        bar();
        { const char* ep_ = ws; asm volatile("" : "+s"(ep_)); tag_ = *(const __attribute__((address_space(4))) u32*)(ep_ + OFF_EPOCH) + 1u; }
        wunit_st(mk_srd(ws, WS_BYTES), (uok && !(wp == 6 && r16 >= 12)) ? OFF_WI + ((cb * 7 + wp) * 18 + su) * 2048 + (wp == 6 ? g * 12 + r16 : lane) * 16 : OOB, w);
        x_dma(mk_srd(x0 - 9216, 2359296 + 18432), (u32)((rg * 64 + (lane >> 3)) * 1152) + (u32)(((lane ^ (lane >> 4)) & 7) << 4), rg == 0 ? OOB : 0,
              ((XCI >> (4 * w)) & 15) + cb, (XCN >> (4 * w)) & 15);
        if (w < 4) {
            const int fa = lane < WREP ? OFF_FLAG + ((lane * 8 + cb) * 32 + rg) * FST : OOB;
            if (w > 0) { VMWAIT(10); } else { VMWAIT(0); }
            BST32(tag_, lds_inc_rtn(L_BC + BC_FC) == 3u ? fa : OOB, 0);
            __builtin_amdgcn_s_setprio(0);
        }
#define PFI(w) ((w) >= 4 ? (w) - 4 : (w) + 2)
#define PFON(w) ((w) > 0 && (w) < 7 && PFI(w) < PFA)
        if (PFON(w)) gu_pf(wg, wu, lane, b, PFI(w));
        if (w == 6) {
#pragma unroll
            for (int i = 0; i < 3; ++i) {
                const int q = lane + 64 * i;
                if (q < 144) sts128((q >> 4) * XCH + (q & 15) * 16, (u32x4){ONES2, ONES2, ONES2, ONES2});
            }
        }
        lc_set(lane_consts(w_, b_));
    }
    MidS a;
    {
        IDS
        const int rt4 = w & 3, kh = w >> 2, hp = 2 * cb + kh;
        const u32 obase = OFF_OT + (rg * 4 + rt4) * OTS;
        const u32 wl = OFF_WOI + cb * 82944;
        const u32 lo = w < 4 ? 16384 + w * 6144 : (w < 7 ? 64512 + (w - 4) * 6144 : 57344), eo = kh * 41472 + rt4 * 4096;
        a.ob = obase + kh * 9216; a.st = obase + hp * 1024; a.tt = obase + 16384 + hp * 256;
        a.c0 = (rg == 0 && rt4 == 0) ? 0xff61b1e6u : 0u;
        a.xf = kh ? OFF_KEYE + cb * 4096 + (rg * 4 + rt4) * 8 : OFF_XFLAG + ((rg * 8 + cb) * 4 + w) * XFS;
        a.wi = wl + lo; a.ld = lo >= 64512 ? lo - 64512 : L_WOE + lo; a.wx = wl + eo; a.ldx = L_WOE + eo;
        a.msk = (u32)(kh - 1); a.scr = rt4 == 2 ? L_SCR3 : L_SCR + (rt4 == 3 ? 2 : rt4) * 4608; a.cr = OFF_CROW + (rg * 4 + rt4) * 1280 + cb * 144;
        a.stat = OFF_STATS + (rg * 4 + rt4) * 2048 + cb * 256;
        a.x1 = kh ? OFF_KEYE - 32768 + (b & 31) * 1024 : OFF_X1G + (rg * 4 + rt4) * 18432 + cb * 2304;
        asm volatile("" : "+s"(a.ob), "+s"(a.st), "+s"(a.tt), "+s"(a.c0), "+s"(a.wi), "+s"(a.ld), "+s"(a.wx), "+s"(a.ldx), "+s"(a.msk),
                          "+s"(a.scr), "+s"(a.stat), "+s"(a.x1), "+s"(a.cr), "+s"(a.xf));
    }
    asm volatile("" : "+s"(tag_));
#define VA_MASK (CSL(w) < 6 ? ~0ull : 0x0000FFFFFFFFFFFFull)
#define V0_MASK (CSL(w) < 4 ? ~0ull : (w == 4 ? 0xFFFFFFFFull : 0ull))
    a.skip = 0;
#pragma nounroll
    for (int layer = 0; layer < 2; ++layer) {
    u32 tag = tag_; asm volatile("" : "+s"(tag));
    u64 gub = 0; u32 gul = 0;
    if (layer == 0) {
    {
        IDS
        int gi = GN1 + __builtin_popcount(GWM & ((1u << w) - 1u)) + ((b >> 3) & 3) * 4; if (gi >= 18) gi -= 18;
        gub = ((u64)(gi < 9 ? wg : wu) + (gi < 9 ? gi : gi - 9) * (64 * 9216)) & 0xffffffffffffull;
        gul = (GWM >> w & 1) ? L_G + gi * 3072 : 0;
        asm volatile("" : "+s"(gub), "+s"(gul));
    }
    if (w_ < 7) {
        IDS
        const u32x4 sws = mk_srd(ws, WS_BYTES);
        const u32 fo = w > 0 ? OOB : OFF_GFLAG + (lane < 18 ? lane * 8 + cb : rg * 8 + (lane & 7)) * 16;
        int xt[3];
        {
            const int row = 16 + 16 * (w & 3) + r16;
#pragma unroll
            for (int p = 0; p < 3; ++p) {
                const int pc = 9 * cb + 4 * p + (p < 2 ? g : 0);
                xt[p] = (pc >> 3) * XCH + row * 128 + (((pc ^ (row >> 1)) & 7) << 4) + (p < 2 ? 0 : (g & 1) * 8);
            }
            asm volatile("" : "+v"(xt[0]), "+v"(xt[1]), "+v"(xt[2]));
        }
        if (w >= 1) {
            if (PFON(w)) { VMWAIT(1); } else { VMWAIT(0); }
            lds_inc(L_BC + BC_XC, lane);
        }
        if (w >= 4) {
            LWAIT0(L_BC + BC_XC, 6, 22);
            float xs[2], xq[2];
            xstats((w - 4) * 2048 + r16 * 128 + (((g ^ (r16 >> 1)) & 7) << 4), xs, xq);
#pragma unroll
            for (int t = 0; t < 2; ++t) {
                const int row = 16 * (w - 4 + 3 * t) + r16;
                float m = xs[t] * (1.f / 576.f);
                const float xv = fmaxf(xq[t] * (1.f / 576.f) - m * m, 0.f);
                float rsd = ln_rs(xv);
                m = ln_m(m);
                if (g == (r16 >> 2) && row < 80 && row >= 16) sts32(L_VAR + (row - 16) * 4, __float_as_uint(xv));
                if (row < 2) { m = 0.f; rsd = 0.f; }
                if (g == (r16 >> 2) && row < 80) sts64(L_MR + row * 8, (u32x2){__float_as_uint(m), __float_as_uint(rsd)});
            }
            lds_inc(L_BC + BC_MC, lane);
        }
        if (w == 0) {
            u32 cn = SPIN_MAX;
            poll_flags(sws, OFF_FLAG + ((rg & (WREP - 1)) * 256 + cb * 32 + (lane & 31)) * FST, tag, cn);
            sts32v(L_BC + BC_GC, 1u);
            if (cn == 0) TMO(3);
        } else {
            LWAIT0(L_BC + BC_GC, 1, 23);
        }
        LWAIT0(L_BC + BC_XC, 6, 22);
        {
            const u32 ib = OFF_WI + (cb * 7 + CSL(w)) * 36864;
            u32 cn = SPIN_MAX;
            gemm0(sws, r16 * 128 + (((g ^ (r16 >> 1)) & 7) << 4), (CSL(w) == 6 ? g * 12 + (r16 < 12 ? r16 : 11) : lane) * 16, ib, xt, fo, w > 0 ? 0u : tag, L_CS + w * 128, VA_MASK, V0_MASK, cn);
            if ((int)cn < 1) TMO(25);
        }
    } else {
        IDS
        __builtin_amdgcn_s_setprio(3);
        {
            u32 U[3][8];
            const int ks = rg < 18 ? rg : 0;
            const u32 rb = rg < 18 ? (u32)(ks < 16 ? 36 * ks + 4 * g : 36 * (8 * (ks - 16) + 2 * g) + 16) * 1152u + cb * 144u : (u32)OOB;
#pragma unroll
            for (int p = 0; p < 3; ++p) old8(U[p], mk_srd(wo, 663552), rb + p * 64u + (p < 2 ? r16 * 4u : ((r16 & 7) >> 1) * 4u), (ks < 16 ? 20 : 36) * 1152);
            u32 sw_ = SENT; asm volatile("" : "+v"(sw_));
            const u32x4 sv = {sw_, sw_, sw_, sw_};
            const int ob = OFF_OT + rg * 4 * OTS;
            LWAIT(L_BC + BC_FC, 4, 43);
#pragma unroll
            for (int i = 0; i < 8; ++i) BST128(sv, lane * 16 + (i & 1) * 1024, ob + (i >> 1) * OTS + cb * 2048);
            BST128(sv, g * OTS + r16 * 16, ob + 16384 + cb * 512); BST128(sv, g * OTS + r16 * 16 + 256, ob + 16384 + cb * 512);
            BST128(sv, g * 2048 + r16 * 16, OFF_STATS + rg * 4 * 2048 + cb * 256);
            const int wb = rg < 18 ? OFF_WOI + cb * 82944 + ks * 4608 : OOB;
#pragma unroll
            for (int p = 0; p < 3; ++p) {
                vmwait8n(U[p], 11);
                u32x4 f0, f1;
                wfrag(U[p], f0, f1);
                if (p < 2) { BST128(f0, wb + p * 2048 + lane * 16, 0); BST128(f1, wb + p * 2048 + 1024 + lane * 16, 0); }
                else BST128((r16 & 1) ? f1 : f0, r16 < 8 ? wb + 4096 + (g * 8 + r16) * 16 : OOB, 0);
            }
        }
#define UV_LOAD() do { const u32 oa = rg < 18 ? (u32)(72 * cb + 4 * rg + g) * 1152u + r16 * 16u : (u32)OOB; \
            uv_load(mk_srd(wr, 1152), mk_srd(wo, 663552), r16 * 16u, oa, r16 < 8 ? oa : (u32)OOB); } while (0)
#define UV_DOTS() do { \
            float d[4]; \
            uv_dots(d); \
            const float wsum = rsum(d[0]), wmean = wsum * (1.f / 576.f); \
            const int q = rg >= 9 ? rg - 9 : rg; \
            const u32 u = __float_as_uint((rsum(d[1]) - wmean * rsum(d[2])) * (q == 4 ? 0.25f : 1.f)); \
            const u32 c = __float_as_uint(gsum(rsum(d[3])) * (36.f / 32.f / 2304.f)) & 0xffff0000u; \
            const u32 r0 = pk2(__uint_as_float(__builtin_amdgcn_readlane(u, 0)), __uint_as_float(__builtin_amdgcn_readlane(u, 16))); \
            const u32 r1 = pk2(__uint_as_float(__builtin_amdgcn_readlane(u, 32)), __uint_as_float(__builtin_amdgcn_readlane(u, 48))); \
            BST128(q < 4 ? ((u32x4){tag, c, r0, r1}) : ((u32x4){r0, r1, c, tag}), (rg < 18 && lane == 0) ? OFF_UV + (2 * cb + (rg >= 9)) * 256 + q * 16 : OOB, 0); \
            if (lane < 36) sts32(L_WR + 192 + lane * 4, ONES2); \
            if (lane == 0) { sts32(L_BC + BC_WRS, __float_as_uint(wsum)); sts32(L_WR + 336, __float_as_uint(wmean)); } \
        } while (0)
        UV_LOAD();
        u32 wq;
        bld32(wq, mk_srd(wr, 1152), lane < 32 ? cb * 144u + lane * 4u : ((lane < 40 && !(lane & 2)) ? cb * 144u + 128u + ((lane >> 2) & 1) * 8u + (lane & 1) * 4u : (u32)OOB), 0u, 0);
        { int nu; const int f0 = ffn_slice(b, nu); dload(mk_srd(wd, (f0 + nu) * 1152), lane * 2u, f0 * 1152u); }
        asm volatile("s_waitcnt vmcnt(48)" : "+v"(wq) :: "memory");
        if (lane == 0) BST32(tag, OFF_GFLAG + (rg * 8 + cb) * 16, 0);
        if (lane < 48) sts32(L_WR + lane * 4, wq);
        UV_DOTS();
        asm volatile("s_nop 4\n\tbuffer_load_dword v98, %0, %1, 0 offen sc1" :: "v"(lane < 4 ? (lane < 2 ? OFF_KEY : OFF_KEYE - 256) + b * 256 + lane * 128 : (lane == 4 ? OFF_XFLAG + b * 128 : (lane == 5 ? OFF_HX + (rg * 8 + cb) * 10240 : OOB))), "s"(mk_srd(ws, WS_BYTES)) : "memory");
        LWAIT(L_BC + BC_GC, 1, 31);
        __builtin_amdgcn_s_sleep(16);
        __builtin_amdgcn_s_setprio(3);
#pragma unroll
        for (int i = PFA; i < 6; ++i) gu_pf(wg, wu, lane, b, i);
        VMWAIT(0);
        __builtin_amdgcn_s_setprio(3);
#define UV_DMA() asm volatile("s_mov_b32 m0, %0\n\ts_mov_b32 exec_hi, 0\n\ts_nop 2\n\tbuffer_load_dwordx4 %1, %2, %3 offen sc1 lds\n\ts_mov_b32 exec_hi, -1" \
                              :: "s"(L_UV), "v"(lane * 16), "s"(mk_srd(ws, WS_BYTES)), "s"(OFF_UV + cb * 512) : "memory")
        UV_DMA();
        gu_dma(wg, wu, lane, b, 0, GN1);
        vmwaitn(3 * GN1);
        {
            int it = 0;
            const int ta = L_UV + (g & 1) * 256 + (r16 < 4 ? r16 * 16 : (r16 < 9 ? r16 : 8) * 16 + 12);
#pragma nounroll
            while (__builtin_expect(__builtin_amdgcn_ballot_w64(lds32v(ta) != tag) != 0ull, 0)) {
                if (++it > SPIN_MAX) { TMO(19); break; }
                UV_DMA();
                VMWAIT(0);
            }
        }
        {
            u32 sw_ = SENT; asm volatile("" : "+v"(sw_));
            const u32x4 sv = {sw_, sw_, sw_, sw_};
            const int l16 = lane * 16;
#pragma unroll
            for (int l = 0; l < 2; ++l) {
                BST128(sv, (r16 < 9 ? g * 1280 + cb * 144 + r16 * 16 : OOB), OFF_CROW + (l * 128 + rg * 4) * 1280);
            }
            if (b < 144) {
#pragma unroll
                for (int i = 0; i < 4; ++i) BST128(sv, l16 + i * 1024, OFF_YP + b * 4096);
            }
            if (b >= 246) BST128(sv, l16, OFF_X2R + (b - 246) * 1024);
            const int ob = OFF_OT + TILES_B + rg * 4 * OTS;
#pragma unroll
            for (int i = 0; i < 8; ++i) BST128(sv, l16 + (i & 1) * 1024, ob + (i >> 1) * OTS + cb * 2048);
            BST128(sv, g * OTS + r16 * 16, ob + 16384 + cb * 512); BST128(sv, g * OTS + r16 * 16 + 256, ob + 16384 + cb * 512);
            BST128(sv, g * 2048 + r16 * 16, OFF_STATS + STATS_B + rg * 4 * 2048 + cb * 256);
        }
        __builtin_amdgcn_s_setprio(0);
    }
    }
#define XFA(r) (OFF_XFLAG + ((r) * 32 + (lane & 31)) * XFS)
#define FFN_ADDR(h) \
            const int q4 = r16 & 3; \
            const int sl = (16 * (g >> 1) + 4 * (g & 1) + (r16 >> 2)) * 48 + (h) * 7680; \
            int g0 = L_G + sl + q4 * 8, g1 = L_U + sl + q4 * 8, g2 = (q4 < 2 ? L_G + q4 * 8 : L_U + (q4 - 2) * 8) + sl + 32; \
            u32 c0 = TBIAS + (h) * 320 + g * 16; \
            int pa = L_FP + (h) * 1536 + lane * 16, ct = L_BC + BC_CT + layer * 64 + (r16 < 7 ? r16 : 7) * 8;
        {
            IDS
            u32 cn = LSPIN_MAX / 4;
            a.srd = mk_srd(ws, WS_BYTES);
            mid(a, tag, gub, gul, 4 * layer + 4, cn);
            a.ob += TILES_B; a.st += TILES_B; a.tt += TILES_B; a.stat += STATS_B; a.cr += 128 * 1280; a.xf += 32768;
            asm volatile("" : "+s"(a.ob), "+s"(a.st), "+s"(a.tt), "+s"(a.stat), "+s"(a.cr), "+s"(a.xf));
            if (cn == 0) TMO(4);
        }
        if (w_ >= 4) {
            IDS
            __builtin_amdgcn_s_setprio(0);
            if (w < 7) {
                if (layer == 0) LWAIT(L_BC + BC_WC, 4, 27);
                FFN_ADDR(w - 4)
                u32 cn = LSPIN_MAX;
                ffn_help(mk_srd(ws, WS_BYTES), c0, g0, g1, g2, pa, L_BC + BC_HF + (w - 4) * 4, ct, layer + 1, cn);
                if ((int)cn < 1) TMO(38);
                __builtin_amdgcn_s_setprio(0);
            }
        } else {
            IDS
            if (w == 0) {
                float wrsum = __uint_as_float(lds32(L_BC + BC_WRS));
                int ka = r16 < 4 ? OFF_KEY + (layer * 32 + cb * 4 + r16) * 1024 + (rg * 4 + g) * 8 : OOB;
                u32 idx = 2047 - rg * 64 - lane, tv = tag;
                asm volatile("" : "+v"(wrsum), "+v"(ka), "+v"(idx), "+v"(tv));
                float t[3];
                u32 cn = SPIN_MAX;
                poll_stats(mk_srd(ws, WS_BYTES), g * 2048 + r16 * 16, OFF_STATS + layer * STATS_B + rg * 8192, t, cn);
                const RowS q = row_key(t, wrsum, idx);
                const u32 tb = rmax(q.key);
                BST64(((u32x2){tb, tv}), ka, 0);
                CFENCE();
                sts128(L_MX + lane * 16, (u32x4){__float_as_uint(q.m), __float_as_uint(q.rs), tb, tb});
                CFENCE();
                sts32v(L_BC + BC_TF, layer + 1);
                sts64(L_MR + (16 + lane) * 8, ln_next(q.m0, q.var, q.m, q.rs));
                lds_inc(L_BC + BC_MC, lane);
                sts32(L_VAR + lane * 4, __float_as_uint((1.f + q.rs) * (1.f + q.rs) * q.var));
                if (cn == 0) TMO(5);
            }
            const u32x4 sws = mk_srd(ws, WS_BYTES);
            if (layer == 0 && w == 3) {
                u32 cn = SPIN_MAX;
                poll_flagx(sws, XFA(rg), tag, cn);
                gather(sws, OFF_X1G + rg * 73728);
                if (cn == 0) TMO(6);
                LWAIT(L_BC + BC_HF + 12, 1, 28);
            } else {
                {
                    u32 XT[10];
                    const u32 best = xt_update(XT, lane, w, layer + 1);
                    if (layer == 1) {
                        const int grow = (rg * 4 + w) * 16 + r16;
                        const bool isb = (best & 0x7ffu) == (u32)(2047 - grow);
                        const __amdgpu_buffer_rsrc_t ro = __builtin_amdgcn_make_buffer_rsrc((void*)out, 0, 2359296, 0x00020000);
                        const int ob = grow * 1152 + cb * 144;
                        const u32x4 v0 = {XT[0], XT[1], XT[2], XT[3]}, v1 = {XT[4], XT[5], XT[6], XT[7]};
                        const u32x2 v2 = {XT[8], XT[9]};
                        if (!isb) {
                            __builtin_amdgcn_raw_buffer_store_b128(v0, ro, ob + g * 16, 0, 0);
                            __builtin_amdgcn_raw_buffer_store_b128(v1, ro, ob + 64 + g * 16, 0, 0);
                            if (g < 2) __builtin_amdgcn_raw_buffer_store_b64(v2, ro, ob + 128 + g * 8, 0, 0);
                        }
                        LWAIT(L_BC + BC_WIN, 1, 17);
                        const bool held = isb && ((best ^ lds32v(L_BC + BC_WIN)) & 0x7ffu) == 0u;
                        if (isb && !held) {
                            __builtin_amdgcn_raw_buffer_store_b128(v0, ro, ob + g * 16, 0, 16);
                            __builtin_amdgcn_raw_buffer_store_b128(v1, ro, ob + 64 + g * 16, 0, 16);
                            if (g < 2) __builtin_amdgcn_raw_buffer_store_b64(v2, ro, ob + 128 + g * 8, 0, 16);
                        }
                        LWAIT(L_BC + BC_FIN, 1, 13);
                        if (__builtin_expect(__builtin_amdgcn_readfirstlane(lds32v(L_BC + BC_FIN)) == 1u, 0)) {
                            u32 X[10];
                            xt_get(X);
                            BST128(((u32x4){X[0], X[1], X[2], X[3]}), a.x1 + lane * 16, 0);
                            BST128(((u32x4){X[4], X[5], X[6], X[7]}), a.x1 + 1024 + lane * 16, 0);
                            BST64(((u32x2){X[8], X[9]}), g < 2 ? a.x1 + 2048 + r16 * 16 + g * 8 : OOB, 0);
                            VMWAIT(0);
                            u32 tg = tag; asm volatile("" : "+s"(tg));
                            BST32(tg, lane == 0 ? OFF_XF2 + ((rg * 8 + cb) * 4 + w) * XFS : OOB, 0);
                            if (held) {
                                __builtin_amdgcn_raw_buffer_store_b128(v0, ro, ob + g * 16, 0, 0);
                                __builtin_amdgcn_raw_buffer_store_b128(v1, ro, ob + 64 + g * 16, 0, 0);
                                if (g < 2) __builtin_amdgcn_raw_buffer_store_b64(v2, ro, ob + 128 + g * 8, 0, 0);
                            }
                        }
                        break;
                    }
                    xt_set(XT);
                }
            }
        }
        if (w_ < 7) {
            if (layer == 0) {
                IDS
                int xr = L_XR + cb * 144 + g * 16, xq = L_XR + cb * 144 + 128 + (g & 1) * 8, fb = L_BC;
                asm volatile("" : "+v"(xr), "+v"(xq), "+v"(fb));
                const u32 hx = OFF_HX + ((rg * 8 + cb) * 5 + CSL(w)) * 2048;
                LWAIT0(L_BC + BC_G0, 1, 16);
                u32 cn = LSPIN_MAX, go;
                gemm1a(mk_srd(ws, WS_BYTES), hx, rg > 0 ? hx - 81920 : hx, tag, fb, L_CS + w * 128, VA_MASK, V0_MASK, go, cn);
                a.skip = 3;
                if (w < 4) {
                    a.skip = 1;
                    if (w < 3) {
                        const u32x4 sws = mk_srd(ws, WS_BYTES);
                        const u32 wl = OFF_WOI + cb * 82944 + (5 + 2 * w) * 4096, dl = L_WOE + (5 + 2 * w) * 4096;
                        LWAIT0(L_BC + BC_C1, 7, 48);
                        dma16x4_sc1(sws, lane * 16u, wl, dl);
                        if (w < 2) dma16x4_sc1(sws, lane * 16u, wl + 4096, dl + 4096);
                    }
                    if (w == 3) { u32 XT[10]; xt_update(XT, lane, w, 1); xt_set(XT); }
                }
                if (go != 1u) gemm1b(fb, xr, xq, w + 1, VA_MASK, V0_MASK, cn);
                if ((int)cn < 1) TMO(16);
            }
        } else {
            u32 best = 0, yb = OFF_YP + layer * YP_B, xb = OFF_X2R;
#pragma nounroll
            for (;;) {
                IDS
                FFN_ADDR(3)
                u32 c1 = 1024 + (lane & 7) * 16;
                u32 ya = (lane >> 2) * 16384u + (lane & 3) * 16u, ye = (32 + g) * 16384u + r16 * 4u;
                int pg = L_FP + 268 + (g == 0 ? r16 * 16 : 1024 + (r16 & 7) * 16), pu = L_FP + 268 + (g == 0 ? 512 + r16 * 16 : 1152 + (r16 & 7) * 16), ff = L_BC + BC_HF;
                u32 sg = TBIAS + g * 256, e0 = OFF_CROW - TBIAS + (layer * 128 + 2 * lane) * 1280, e1 = OFF_STATS - TBIAS + layer * STATS_B + lane * 4096 + 240;
                float wrsum = __uint_as_float(lds32(L_BC + BC_WRS));
                asm volatile("" : "+v"(g0), "+v"(g1), "+v"(g2), "+v"(c0), "+v"(c1), "+v"(ya), "+v"(ye), "+v"(pa), "+v"(pg), "+v"(pu), "+v"(ff), "+v"(ct), "+v"(sg), "+v"(e0), "+v"(e1), "+v"(wrsum));
                const u32x4 sws = mk_srd(ws, WS_BYTES);
                if (best == 0u) {
                    u32 ke[2];
                    u32 cn = SPIN_MAX;
                    poll_keye(sws, OFF_KEYE + (layer * 32 + (b & 31)) * 1024, tag, layer, g0, g1, g2, ke, cn);
                    if (cn == 0) TMO(7);
                    const u32 top = wmax(umax2(ke[0], ke[1]));
                    u32 thr = umax2(top, KDEL) - KDEL;
                    u64 m0 = __builtin_amdgcn_ballot_w64(ke[0] >= thr && ke[0] != top), m1 = __builtin_amdgcn_ballot_w64(ke[1] >= thr && ke[1] != top);
#pragma nounroll
                    while (__builtin_expect(__builtin_popcountll(m0) + __builtin_popcountll(m1) > 6, 0)) {
                        thr += (top - thr + 7u) >> 3;
                        m0 &= __builtin_amdgcn_ballot_w64(ke[0] >= thr); m1 &= __builtin_amdgcn_ballot_w64(ke[1] >= thr);
                    }
                    const bool s0 = __builtin_amdgcn_inverse_ballot_w64(m0), s1 = __builtin_amdgcn_inverse_ballot_w64(m1);
                    const u32 p0 = __builtin_amdgcn_mbcnt_hi((u32)(m0 >> 32), __builtin_amdgcn_mbcnt_lo((u32)m0, 1u));
                    const u32 p1 = __builtin_amdgcn_mbcnt_hi((u32)(m1 >> 32), __builtin_amdgcn_mbcnt_lo((u32)m1, 1u + __builtin_popcountll(m0)));
                    const int cb_ = L_BC + BC_CT + layer * 64;
                    LWAIT0(L_BC + BC_PK, 4 * layer + 4, 33);
                    if (s0) sts64(cb_ + p0 * 8, (u32x2){e0, e1 - (ke[0] & 15u) * 16});
                    if (s1) sts64(cb_ + p1 * 8, (u32x2){e0 + 1280, e1 + 2048 - (ke[1] & 15u) * 16});
                    CFENCE();
                    const u32 tr = ~top & 0x7ffu;
                    sts64(cb_, (u32x2){OFF_CROW - TBIAS + (layer * 128 + (tr >> 4)) * 1280, OFF_STATS - TBIAS + layer * STATS_B + (tr >> 4) * 2048 + (tr & 15u) * 16});
                    CFENCE();
                }
                const u32x2 ce = lds64(ct);
                u32 cn = LSPIN_MAX, win;
                {
                    float t[3];
                    c0 += ce.x;
                    u32 idx = ~(((ce.y + TBIAS - OFF_STATS) >> 7 & 0x7f0u) | (ce.y >> 4 & 15u)) & 0x7ffu;
                    asm volatile("" : "+v"(idx));
                    ffn_a(sws, c0, sg + ce.y, t, cn);
                    gsum3(t);
                    const RowS q = row_key(t, wrsum, idx);
                    const u32 key = ce.x != 0u ? q.key : 0u;
                    win = rmax(key);
                    const int sl_ = __builtin_ctzll(__builtin_amdgcn_ballot_w64(key == win));
                    sts32v((layer == 1 && best == 0u) ? L_BC + BC_WIN : L_BC + BC_NIL, win);
                    win = __builtin_amdgcn_readfirstlane(win);
                    ffn(sws, __builtin_amdgcn_readlane(ce.x, sl_) + TBIAS, c0, c1, pa, ff, pg, pu, (sl_ >> 2) * 256 + (sl_ & 3) * 4 - 268,
                        __uint_as_float(__builtin_amdgcn_readlane(__float_as_uint(q.m), sl_)), __uint_as_float(__builtin_amdgcn_readlane(__float_as_uint(q.rs), sl_)), layer + 1,
                        ya, ye, yb + b * 64, cn);
                }
                if ((int)cn < 1) TMO(14);
                if (layer == 0) UV_DMA();
                if (best != 0u) {
                    VMWAIT(0);
                    u32 tg = tag; asm volatile("" : "+s"(tg));
                    BST32(tg, lane == 0 ? OFF_YF2 + (layer * 256 + b) * 4 : OOB, 0);
                    cn = SPIN_MAX;
                    if (b < 144) poll_flag4(sws, OFF_YF2 + layer * 1024, tag, cn);
                    if (cn == 0) TMO(9);
                }
                u32 k; int bad;
                const int prow = (int)(~win & 0x7ffu) - (rg * 64 - 16);
                const bool affected = prow >= (rg == 0 ? 16 : 2) && prow < 80;
                {
                    const int q = (b & 7) * 18 + (b >> 3), f0 = q < 128 ? 8 * (q & 63) + 4 * (q >> 6) : 4 * q;
                    const int cidx = f0 + 2 * (g & 1) + (g >> 1);
                    float xv = __uint_as_float((u32)*(const u16*)(smem + L_XROW + cidx * 2) << 16);
                    float hv = __uint_as_float((u32)*(const u16*)(smem + L_HROW + cidx * 2) << 16);
                    const u32 sbits = win >> 16;
                    const u32 sraw = (sbits & 0x8000u) ? (sbits & 0x7fffu) : (~sbits & 0xffffu);
                    const float sc = __uint_as_float(sraw << 16);
                    float sg_ = rbf(__builtin_amdgcn_rcpf(1.f + __builtin_amdgcn_exp2f(-sc * 1.4426950408889634f)));
                    const bool one = lane == 0 && b < 144;
                    int xo = (one && layer == 0) ? xb + 32 * q : OOB, oo = (one && layer == 1) ? (int)(~win & 0x7ffu) * 1152 + 2 * f0 : OOB;
                    int fa_ = layer == 1 ? (best == 0u ? L_BC + BC_FIN : L_BC + BC_NIL) : (affected ? L_BC + BC_NIL : L_BC + BC_GO);
                    asm volatile("" : "+v"(xv), "+v"(hv), "+v"(sg_), "+v"(xo), "+v"(oo), "+v"(fa_));
                    cn = SPIN_MAX;
                    float yf[4];
                    poll_ky(sws, OFF_KEY + (layer * 32 + (b & 31)) * 1024 + lane * 16, tag, b < 144 ? lane * 64u : (u32)OOB, yb + (q >> 2) * 16384 + (q & 3) * 16, k, yf, cn);
                    bad = (__builtin_amdgcn_ballot_w64(k > win) == 0ull || best != 0u) ? 0 : OOB;
                    sts32v(fa_, (bad ? 0u : 1u) + layer);
                    CFENCE();
                    const __amdgpu_buffer_rsrc_t ro = __builtin_amdgcn_make_buffer_rsrc((void*)out, 0, 2359296, 0x00020000);
                    const u32 ys[4] = {__float_as_uint(yf[0]), __float_as_uint(yf[1]), __float_as_uint(yf[2]), __float_as_uint(yf[3])};
                    auto p0 = __builtin_amdgcn_permlane32_swap(ys[0], ys[1], false, false);
                    auto p1 = __builtin_amdgcn_permlane32_swap(ys[2], ys[3], false, false);
                    const u32 yd = __float_as_uint(__uint_as_float(p0[0]) + __uint_as_float(p0[1])), yc = __float_as_uint(__uint_as_float(p1[0]) + __uint_as_float(p1[1]));
                    auto p2 = __builtin_amdgcn_permlane16_swap(yd, yc, false, false);
                    const float y = rsum(__uint_as_float(p2[0]) + __uint_as_float(p2[1]));
                    const u32 r1 = pk2(xv + rbf(hv + rbf(rbf(y) * sg_)), 0.f);
                    auto p3 = __builtin_amdgcn_permlane32_swap(r1, r1, false, false);
                    const u32 res = p3[0] | (p3[1] << 16);
                    auto p4 = __builtin_amdgcn_permlane16_swap(res, res, false, false);
                    BST64(((u32x2){san(p4[0]), san(p4[1])}), xo, 0);
                    __builtin_amdgcn_raw_buffer_store_b64(((u32x2){p4[0], p4[1]}), ro, oo | bad, 0, 0);
                    CFENCE();
                    if (cn == 0) TMO(10);
                }
                if (layer == 0 && bad == 0 && affected) {
                    u32 o[6], gw = 1u | (u32)prow << 8;
                    asm volatile("" : "+v"(gw));
                    cn = SPIN_MAX;
                    poll_kr(sws, xb + lane * 32, xb + 4096 + r16 * 32, L_XR + lane * 16, L_XR + 1024 + r16 * 8, o, cn);
                    sts32v(L_BC + BC_GO, gw);
                    CFENCE();
                    if (cn == 0) TMO(2);
                    {
                        if (lane >= 16) { o[4] = 0u; o[5] = 0u; }
                        float psx = 0.f, psq = 0.f;
#pragma unroll
                        for (int i = 0; i < 6; ++i) { psx = dot2(o[i], ONES2, psx); psq = dot2(o[i], o[i], psq); }
                        psx = gsum(rsum(psx)); psq = gsum(rsum(psq));
                        CFENCE();
                        const float pm0 = psx * (1.f / 576.f);
                        const float pv = fmaxf(psq * (1.f / 576.f) - pm0 * pm0, 0.f);
                        const float prs = ln_rs(pv), pm = ln_m(pm0);
                        sts32v(L_BC + BC_PM, __float_as_uint(pm)); sts32v(L_BC + BC_PM + 4, __float_as_uint(prs)); sts32v(L_BC + BC_PST, 255u);
                        if (prow >= 16) sts32v(L_VAR + (prow - 16) * 4, __float_as_uint(pv));
                    }
                }
                asm volatile("" : "+s"(bad));
                if (__builtin_expect(bad == 0, 1)) { best = win; break; }
                best = wmax(k);
                {
                    const u32 tr = ~best & 0x7ffu;
                    cn = SPIN_MAX;
                    poll_flagh(sws, XFA(tr >> 6) + layer * (OFF_XF2 - OFF_XFLAG), tag, cn);
                    if (cn == 0) TMO(11);
                    const int xs = OFF_X1G + (tr >> 4) * 18432 + (tr & 15u) * 16, cr = OFF_CROW2 + b * 1280;
                    const u32x4 r0 = __builtin_amdgcn_raw_buffer_load_b128(rs, xs + lane * 256, 0, 16), r1 = __builtin_amdgcn_raw_buffer_load_b128(rs, xs + (64 + (lane & 7)) * 256, 0, 16);
                    BST128(r0, cr + lane * 16, 0); BST128(r1, cr + 1024 + (lane & 7) * 16, 0);
                    VMWAIT(0);
                    const u32x2 e = {cr - TBIAS, OFF_STATS - TBIAS + layer * STATS_B + (tr >> 4) * 2048 + (tr & 15u) * 16};
                    if (lane < 8) sts64(ct, lane == 0 ? e : (u32x2){0u, 0u});
                    yb = OFF_YP2; xb = OFF_X2R + 5120;
                }
#pragma nounroll
                for (int h = 0; h < 3; ++h) {
                    IDS
                    FFN_ADDR(h)
                    cn = LSPIN_MAX;
                    ffn_help(mk_srd(ws, WS_BYTES), c0, g0, g1, g2, pa, L_BC + BC_HF + h * 4, ct, layer + 1, cn);
                    if ((int)cn < 1) TMO(12);
                }
                ffn_ones();
            }
            IDS
            a.skip = 1;
            if (layer == 0) BST32(tag, (b == 255 && lane == 0) ? OFF_EPOCH : OOB, 0);
            if (layer == 0 && b < 144) {
                u32 sw_ = SENT; asm volatile("" : "+v"(sw_));
                const u32x4 sv = {sw_, sw_, sw_, sw_};
#pragma unroll
                for (int i = 0; i < 4; ++i) BST128(sv, lane * 16 + i * 1024, OFF_YP + YP_B + b * 4096);
            }
            __builtin_amdgcn_s_setprio(0);
            if (layer == 0) {
                const u32x4 sws = mk_srd(ws, WS_BYTES);
                const u32 wl = OFF_WOI + cb * 82944;
                LWAIT(L_BC + BC_C2, 7, 48);
#pragma unroll
                for (int i = 0; i < 5; ++i) dma16x4_sc1(sws, lane * 16u, wl + i * 4096, L_WOE + i * 4096);
            }
        }
    }
    { u32 tmo; asm volatile("s_nop 1\n\tv_readlane_b32 %0, v109, 47" : "=s"(tmo)); if (tmo != 0u) BST32(tmo, OFF_TMO, 0); }
    asm volatile("Lcode1:");
}

extern "C" void kernel_launch(void* const* d_in, const int* in_sizes, int n_in, void* d_out, int out_size, void* d_ws, size_t ws_size,
                              hipStream_t stream) {
    (void)in_sizes; (void)n_in; (void)out_size; (void)ws_size;
    hipFuncSetAttribute((const void*)mega, hipFuncAttributeMaxDynamicSharedMemorySize, LDS_BYTES);
    mega<<<256, 512, LDS_BYTES, stream>>>((const u16*)d_in[0], (const u16*)d_in[1], (const u16*)d_in[2], (const u16*)d_in[4],
                                          (const u16*)d_in[5], (const u16*)d_in[6], (const u16*)d_in[7], (u16*)d_out, (char*)d_ws);
}
```

```cpp
#include <hip/hip_runtime.h>
#include <stdint.h>

#define WREP 8
#define FST 32
#define XFS 32
#define NTD " nt"
#define GWM 0xAA
#define GN1 (18 - __builtin_popcount(GWM))
#define XCI 0x04208760u
#define XCN 0x02221110u
#define KDEL 0x1c0000u
#define PFA 3

#if defined(__HIP_DEVICE_COMPILE__)
#define NOPK __attribute__((target("no-packed-fp32-ops")))
#else
#define NOPK
#endif
#define DEV __device__ __forceinline__ NOPK
typedef unsigned short u16;
typedef unsigned int u32;
typedef unsigned long long u64;
#define __uint_as_float(x) __builtin_bit_cast(float, (u32)(x))
#define __float_as_uint(x) __builtin_bit_cast(u32, (float)(x))
#define fmaxf(a, b) __builtin_fmaxf((a), (b))
typedef __attribute__((ext_vector_type(2))) float f32x2;
typedef __attribute__((ext_vector_type(2))) __bf16 bf16x2;
typedef __attribute__((ext_vector_type(2))) u32 u32x2;
typedef __attribute__((ext_vector_type(4))) u32 u32x4;

extern __shared__ __attribute__((aligned(16))) char smem[];

#define SPIN_MAX 20000
#define LSPIN_MAX 400000

#define WS_BYTES 0x1400000
#define OFF_EPOCH 0
#define OFF_TMO 256
#define OFF_FLAG 0xF0000
#define OFF_GFLAG 4096
#define OFF_UV 8192
#define OFF_KEY 16384
#define OFF_KEYE 0x220000
#define OFF_YF2 0x230000
#define OFF_XFLAG 0x18000
#define OFF_XF2 0x238000
#define OFF_X2R 81920
#define OFF_STATS 0x20000
#define STATS_B 0x40000
#define OFF_CROW 0xA0000
#define OFF_CROW2 0xDB0000
#define OFF_YP 0x100000
#define OFF_YP2 0xE00000
#define YP_B 589824
#define OFF_OT 0x240000
#define OTS 20480
#define TILES_B 2621440
#define OFF_X1G 0x740000
#define OFF_HX 0x1000000
#define OFF_WI 0x980000
#define OFF_WOI 0xD00000
#define OOB 0x7FFFFF00
#define TBIAS 0x7FFF0000u

#define LDS_BYTES 163840
#define XCH 10240
#define L_KV 0
#define KVS 304
#define L_Q 24320
#define L_WOE 33536
#define L_SCR 18432
#define L_G 98048
#define L_U 125696
#define L_MISC 153344
#define L_MR (L_MISC)
#define L_BC (L_MISC + 640)
#define BC_TF 0
#define BC_WRS 4
#define BC_C2 8
#define BC_PK 12
#define BC_GC 16
#define BC_C1 20
#define BC_PC 24
#define BC_GO 28
#define BC_PST 32
#define BC_FIN 36
#define BC_WIN 40
#define BC_NIL 72
#define BC_XC 44
#define BC_ZERO 48
#define BC_WC 56
#define BC_G0 64
#define BC_FC 76
#define BC_HF 80
#define BC_MC 96
#define BC_PM 104
#define BC_CD 112
#define BC_CT 128
#define L_WR (L_MISC + 9888)
#define L_VAR (L_WR + 340)
#define L_CS (L_MISC + 928)
#define L_HROW (L_MISC + 1824)
#define L_XROW (L_MISC + 2976)
#define L_XR (L_MISC + 4128)
#define L_SCR3 (L_MISC + 5280)
#define L_MX (L_MISC + 8192)
#define L_UV (L_MISC + 9216)
#define L_FP L_HROW

#define SENT 0xFFFFFFFFu

DEV void bar() {
    asm volatile("s_waitcnt lgkmcnt(0)" ::: "memory");
    __builtin_amdgcn_s_barrier();
    asm volatile("" ::: "memory");
}
#define VMWAIT(n) asm volatile("s_waitcnt vmcnt(" #n ")" ::: "memory")
DEV void vmwaitn(const int n) { asm volatile("s_waitcnt vmcnt(%0)" :: "n"(n) : "memory"); }
DEV void vmwait8n(u32 (&d)[8], const int n) {
    asm volatile("s_waitcnt vmcnt(%8)" : "+v"(d[0]), "+v"(d[1]), "+v"(d[2]), "+v"(d[3]), "+v"(d[4]), "+v"(d[5]), "+v"(d[6]), "+v"(d[7]) : "n"(n) : "memory");
}
DEV u32x4 mk_srd(const void* p, u32 bytes) {
    u64 a = (u64)p;
    asm volatile("" : "+s"(a));
    return (u32x4){(u32)a, (u32)(a >> 32) & 0xffffu, bytes, 0x00020000u};
}
DEV void dma16g(u32x4 srd, u32 voff, u32 soff, u32 lds_dst) {
    asm volatile("s_mov_b32 m0, %0\n\ts_nop 3\n\tbuffer_load_dwordx4 %1, %2, %3 offen lds" :: "s"(lds_dst), "v"(voff), "s"(srd), "s"(soff) : "memory");
}
DEV void dma16x4_sc1(u32x4 srd, u32 voff, u32 soff, u32 lds_dst) {
    asm volatile("s_mov_b32 m0, %0\n\ts_nop 3\n\tbuffer_load_dwordx4 %1, %2, %3 offen sc1 lds\n\tbuffer_load_dwordx4 %1, %2, %3 offen offset:1024 sc1 lds\n\t"
                 "buffer_load_dwordx4 %1, %2, %3 offen offset:2048 sc1 lds\n\tbuffer_load_dwordx4 %1, %2, %3 offen offset:3072 sc1 lds"
                 :: "s"(lds_dst), "v"(voff), "s"(srd), "s"(soff) : "memory");
}
DEV void bld32(u32& d, u32x4 srd, u32 voff, u32 soff, const int imm) {
    asm volatile("s_nop 4\n\tbuffer_load_dword %0, %1, %2, %3 offen offset:%4" : "=v"(d) : "v"(voff), "s"(srd), "s"(soff), "n"(imm));
}
DEV void old8(u32 (&d)[8], u32x4 srd, u32 voff, u32 s2) {
    asm volatile("s_nop 4\n\tbuffer_load_dword %0, %8, %9, 0 offen nt\n\tbuffer_load_dword %1, %8, %9, 0 offen offset:1152 nt\n\t"
                 "buffer_load_dword %2, %8, %9, 0 offen offset:2304 nt\n\tbuffer_load_dword %3, %8, %9, 0 offen offset:3456 nt\n\t"
                 "buffer_load_dword %4, %8, %9, %10 offen nt\n\tbuffer_load_dword %5, %8, %9, %10 offen offset:1152 nt\n\t"
                 "buffer_load_dword %6, %8, %9, %10 offen offset:2304 nt\n\tbuffer_load_dword %7, %8, %9, %10 offen offset:3456 nt"
                 : "=&v"(d[0]), "=&v"(d[1]), "=&v"(d[2]), "=&v"(d[3]), "=&v"(d[4]), "=&v"(d[5]), "=&v"(d[6]), "=&v"(d[7])
                 : "v"(voff), "s"(srd), "s"(s2));
}
DEV void wld8p(u32x4 srd, u32 voff, u32 soff, int w) {
    u32 t;
    asm volatile("s_cmp_gt_u32 %4, 3\n\ts_cbranch_scc1 Lw8_%=\n\ts_setprio 3\n\ts_nop 1\n\t"
                 "buffer_load_dword v48, %1, %2, %3 offen nt\n\tbuffer_load_dword v49, %1, %2, %3 offen offset:3456 nt\n\ts_add_u32 %0, %3, 6912\n\t"
                 "buffer_load_dword v50, %1, %2, %0 offen nt\n\tbuffer_load_dword v51, %1, %2, %0 offen offset:3456 nt\n\ts_add_u32 %0, %3, 13824\n\t"
                 "buffer_load_dword v52, %1, %2, %0 offen nt\n\tbuffer_load_dword v53, %1, %2, %0 offen offset:3456 nt\n\ts_add_u32 %0, %3, 20736\n\t"
                 "buffer_load_dword v54, %1, %2, %0 offen nt\n\tbuffer_load_dword v55, %1, %2, %0 offen offset:3456 nt\n"
                 "Lw8_%=:"
                 : "=&s"(t) : "v"(voff), "s"(srd), "s"(soff), "s"(w) : "scc", "memory");
}
DEV void x_dma(u32x4 srd, u32 xv, u32 orm, u32 c, u32 n) {
    u32 z1, z2, z3, y0, y1, so, t;
    asm volatile("s_cmp_eq_u32 %8, 0\n\ts_cbranch_scc1 Lx1_%=\n\t"
                 "v_xor_b32 %2, 64, %9\n\tv_or_b32 %3, %11, %9\n\tv_add_u32 %0, 0x2000, %2\n\tv_add_u32 %1, 0x4000, %9\n\tv_or_b32 %4, %11, %0\n\tv_add_u32 %2, 0x6000, %2\n"
                 "Lx0_%=:\n\t"
                 "s_cmp_gt_u32 %7, 8\n\ts_cselect_b32 %6, 9, 0\n\ts_sub_u32 %7, %7, %6\n\ts_mul_i32 m0, %7, 10240\n\ts_lshl_b32 %5, %7, 7\n\t"
                 "s_mov_b32 exec_lo, 0xffff0000\n\tbuffer_load_dwordx4 %3, %10, %5 offen lds\n\ts_mov_b32 exec_lo, -1\n\t"
                 "buffer_load_dwordx4 %4, %10, %5 offen offset:1024 lds\n\tbuffer_load_dwordx4 %1, %10, %5 offen offset:2048 lds\n\tbuffer_load_dwordx4 %2, %10, %5 offen offset:3072 lds\n\t"
                 "s_add_u32 m0, m0, 4096\n\ts_add_u32 %5, %5, 36864\n\t"
                 "buffer_load_dwordx4 %9, %10, %5 offen lds\n\tbuffer_load_dwordx4 %0, %10, %5 offen offset:1024 lds\n\t"
                 "buffer_load_dwordx4 %1, %10, %5 offen offset:2048 lds\n\tbuffer_load_dwordx4 %2, %10, %5 offen offset:3072 lds\n\t"
                 "s_add_u32 m0, m0, 4096\n\ts_add_u32 %5, %5, 36864\n\t"
                 "buffer_load_dwordx4 %9, %10, %5 offen lds\n\tbuffer_load_dwordx4 %0, %10, %5 offen offset:1024 lds\n\t"
                 "s_add_u32 %7, %7, 1\n\ts_sub_u32 %8, %8, 1\n\ts_cmp_lg_u32 %8, 0\n\ts_cbranch_scc1 Lx0_%=\n"
                 "Lx1_%=:"
                 : "=&v"(z1), "=&v"(z2), "=&v"(z3), "=&v"(y0), "=&v"(y1), "=&s"(so), "=&s"(t), "+s"(c), "+s"(n) : "v"(xv), "s"(srd), "s"(orm) : "scc", "memory");
}
DEV void wunit_st(u32x4 srd, u32 ib, int w) {
    asm volatile("s_cmp_gt_u32 %2, 3\n\ts_cbranch_scc1 Lu_%=\n\ts_waitcnt vmcnt(0)\n\t"
                 "v_perm_b32 v56, v49, v48, %3\n\tv_perm_b32 v57, v51, v50, %3\n\tv_perm_b32 v58, v53, v52, %3\n\tv_perm_b32 v59, v55, v54, %3\n\t"
                 "buffer_store_dwordx4 v[56:59], %0, %1, 0 offen sc1\n\t"
                 "v_perm_b32 v60, v49, v48, %4\n\tv_perm_b32 v61, v51, v50, %4\n\tv_perm_b32 v62, v53, v52, %4\n\tv_perm_b32 v63, v55, v54, %4\n\t"
                 "buffer_store_dwordx4 v[60:63], %0, %1, 0 offen offset:1024 sc1\n"
                 "Lu_%=:" :: "v"(ib), "s"(srd), "s"(w), "s"(0x05040100u), "s"(0x07060302u) : "scc", "memory");
}
DEV u32 plo(u32 hi, u32 lo) { return __builtin_amdgcn_perm(hi, lo, 0x05040100u); }
DEV u32 phi(u32 hi, u32 lo) { return __builtin_amdgcn_perm(hi, lo, 0x07060302u); }
DEV void wfrag(const u32 (&d)[8], u32x4& f0, u32x4& f1) {
    f0 = (u32x4){plo(d[1], d[0]), plo(d[3], d[2]), plo(d[5], d[4]), plo(d[7], d[6])};
    f1 = (u32x4){phi(d[1], d[0]), phi(d[3], d[2]), phi(d[5], d[4]), phi(d[7], d[6])};
}
DEV u32 pk2(float a, float b) { f32x2 v = {a, b}; return __builtin_bit_cast(u32, __builtin_convertvector(v, bf16x2)); }
DEV float rbf(float a) { return (float)(__bf16)a; }
DEV float ln_rs(float var) { return __builtin_amdgcn_rcpf(rbf(__builtin_amdgcn_sqrtf(rbf(rbf(var) + 1e-5f)))); }
DEV float ln_m(float m) { return rbf(m); }
DEV u32x2 ln_next(float m, float var, float mb, float rs) {
    const float c = 1.f + rs;
    const float mb2 = ln_m(c * m - mb * rs);
    return (u32x2){__float_as_uint((mb * rs + mb2) * __builtin_amdgcn_rcpf(c)), __float_as_uint(c * ln_rs(c * c * var))};
}
DEV float lo16(u32 v) { return __uint_as_float(v << 16); }
DEV float hi16(u32 v) { return __uint_as_float(v & 0xffff0000u); }
DEV u32 san(u32 v) { return v < 0xFFFFFFFEu ? v : 0xFFFFFFFEu; }
DEV u32 umax2(u32 a, u32 b) { return a > b ? a : b; }
DEV float dot2(u32 a, u32 b, float c) {
    return __builtin_amdgcn_fdot2_f32_bf16(__builtin_bit_cast(bf16x2, a), __builtin_bit_cast(bf16x2, b), c, false);
}
DEV void gsum3(float (&t)[3]) {
    u32 a[3], c[3];
#pragma unroll
    for (int i = 0; i < 3; ++i) { a[i] = __float_as_uint(t[i]); c[i] = a[i]; }
    asm volatile("s_nop 1\n\tv_permlane16_swap_b32 %0, %3\n\tv_permlane16_swap_b32 %1, %4\n\tv_permlane16_swap_b32 %2, %5\n\t"
                 "v_add_f32 %0, %0, %3\n\tv_add_f32 %1, %1, %4\n\tv_add_f32 %2, %2, %5\n\t"
                 "v_mov_b32 %3, %0\n\tv_mov_b32 %4, %1\n\tv_mov_b32 %5, %2\n\t"
                 "v_permlane32_swap_b32 %0, %3\n\tv_permlane32_swap_b32 %1, %4\n\tv_permlane32_swap_b32 %2, %5\n\t"
                 "v_add_f32 %0, %0, %3\n\tv_add_f32 %1, %1, %4\n\tv_add_f32 %2, %2, %5"
                 : "+v"(a[0]), "+v"(a[1]), "+v"(a[2]), "+v"(c[0]), "+v"(c[1]), "+v"(c[2]));
#pragma unroll
    for (int i = 0; i < 3; ++i) t[i] = __uint_as_float(a[i]);
}
DEV float gsum(float v) {
    u32 u = __float_as_uint(v);
    auto a = __builtin_amdgcn_permlane16_swap(u, u, false, false);
    v = __uint_as_float(a[0]) + __uint_as_float(a[1]);
    u = __float_as_uint(v);
    auto c = __builtin_amdgcn_permlane32_swap(u, u, false, false);
    return __uint_as_float(c[0]) + __uint_as_float(c[1]);
}
#define DPP(v, ctrl) ((u32)__builtin_amdgcn_update_dpp(0, (int)(v), (ctrl), 0xf, 0xf, false))
DEV u32 rmax(u32 v) {
    v = umax2(v, DPP(v, 0x128)); v = umax2(v, DPP(v, 0x124)); v = umax2(v, DPP(v, 0x122)); v = umax2(v, DPP(v, 0x121));
    return v;
}
DEV float rsum(float v) {
    v += __uint_as_float(DPP(__float_as_uint(v), 0x128)); v += __uint_as_float(DPP(__float_as_uint(v), 0x124));
    v += __uint_as_float(DPP(__float_as_uint(v), 0x122)); v += __uint_as_float(DPP(__float_as_uint(v), 0x121));
    return v;
}
DEV u32 lds32(int off) { return *(const u32*)(smem + off); }
typedef __attribute__((address_space(3))) volatile u32* lds_vu32;
DEV u32 lds32v(int off) { return *(lds_vu32)(smem + off); }
DEV u32x2 lds64(int off) { return *(const u32x2*)(smem + off); }
DEV u32x4 lds128(int off) { return *(const u32x4*)(smem + off); }
DEV void sts32(int off, u32 v) { *(u32*)(smem + off) = v; }
DEV void sts32v(int off, u32 v) { *(lds_vu32)(smem + off) = v; }
DEV void sts64(int off, u32x2 v) { *(u32x2*)(smem + off) = v; }
DEV void sts128(int off, u32x4 v) { *(u32x4*)(smem + off) = v; }
DEV void lds_inc(int off, int lane) {
    (void)lane;
    asm volatile("s_mov_b64 exec, 1\n\tds_add_u32 %0, %1\n\ts_mov_b64 exec, -1" :: "v"(off), "v"(1u) : "memory");
}
DEV u32 lds_inc_rtn(int off) {
    u32 old;
    asm volatile("s_mov_b64 exec, 1\n\tds_add_rtn_u32 %0, %1, %2\n\ts_mov_b64 exec, -1\n\ts_waitcnt lgkmcnt(0)" : "=&v"(old) : "v"(off), "v"(1u) : "memory");
    return __builtin_amdgcn_readfirstlane(old);
}
#define BST128(v, off, soff) __builtin_amdgcn_raw_buffer_store_b128((v), rs, (off) + (soff), 0, 16)
#define BST64(v, off, soff) __builtin_amdgcn_raw_buffer_store_b64((v), rs, (off), (soff), 16)
#define BST32(v, off, soff) __builtin_amdgcn_raw_buffer_store_b32((v), rs, (off), (soff), 16)
#define CFENCE() asm volatile("" ::: "memory")
#define TMO(code) asm volatile("v_writelane_b32 v109, %0, 47" :: "n"(code))
#define LWAITS(off, target, code, slp) do { u32 t_, c_, f_; \
    asm volatile("s_mov_b32 %1, %5\nLl_%=:\n\tds_read_b32 %0, %3\n\ts_waitcnt lgkmcnt(0)\n\tv_readfirstlane_b32 %2, %0\n\ts_cmp_ge_u32 %2, %4\n\ts_cbranch_scc1 Ld_%=\n\t" \
                 slp "s_sub_u32 %1, %1, 1\n\ts_cmp_lg_u32 %1, 0\n\ts_cbranch_scc1 Ll_%=\n\tv_writelane_b32 v109, %6, 47\nLd_%=:" \
                 : "=&v"(t_), "=&s"(c_), "=&s"(f_) : "v"(off), "s"((u32)(target)), "n"(LSPIN_MAX), "n"(code) : "scc", "memory"); } while (0)
#define LWAIT(off, target, code) LWAITS(off, target, code, "s_sleep 1\n\t")
#define LWAIT0(off, target, code) LWAITS(off, target, code, "")

#define ONES2 0x3F803F80u
#define RG_OF(b) (((b) & 7) * 4 + (((b) >> 3) & 3))
#define CB_OF(b) ((b) >> 5)
#define IDS \
    int lane; asm volatile("v_mbcnt_lo_u32_b32 %0, -1, 0\n\tv_mbcnt_hi_u32_b32 %0, -1, %0" : "=v"(lane)); \
    int w = w_; asm volatile("" : "+s"(w)); \
    int b = b_; asm volatile("" : "+s"(b)); \
    const int rg = RG_OF(b), cb = CB_OF(b); \
    const int r16 = lane & 15, g = lane >> 4; \
    (void)rg; (void)cb; (void)r16; (void)g; (void)w;

#define CSL(w) ((w) == 3 ? 6 : ((w) == 6 ? 3 : (w)))
struct LaneC { int ka, kt, qa, qt, va, ta, l0; u32 av, tv0, ot; int sd, sstr; };
DEV LaneC lane_consts(int w_, int b_) {
    IDS
    LaneC c;
    const int rt4 = w & 3, pp = w >> 2;
    const int kvb = L_KV + 16 * rt4 * KVS + 72 * pp;
    const int qb = L_Q + (16 * rt4 + r16) * 144 + 72 * pp;
    const int trr = kvb + (4 * g + (r16 >> 2)) * KVS + 144;
    c.kt = kvb + r16 * KVS + 32; c.ka = kvb + r16 * KVS + g * 8;
    c.qa = qb + g * 8; c.qt = g == 2 ? qb + 32 : L_BC + BC_ZERO;
    c.va = trr + (r16 & 3) * 8; c.ta = trr + 32;
    c.l0 = 4096 + (8 * g + (r16 & 7)) * 16;
    c.av = lane * 16;
    c.tv0 = pp ? 7168 + g * 512 + r16 * 16 : 7168 + lane * 16;
    c.ot = 0;
    const int P = 4 * CSL(w) + g;
    c.sstr = P >= 18 ? 144 : KVS;
    c.sd = (P >= 18 ? (L_Q - 16 * 144 + (P - 18) * 16) : (L_KV + P * 16)) + r16 * c.sstr;
    c.sstr *= 16;
    return c;
}

struct MidS { u32x4 srd; u32 ob, st, tt, c0, wi, ld, wx, ldx, msk, scr, stat, x1, cr, skip, xf; };
DEV void lc_set(const LaneC& c) {
    asm volatile("v_mov_b32 v100, %0\n\t" "v_mov_b32 v101, %1\n\t" "v_mov_b32 v102, %2\n\t" "v_mov_b32 v103, %3\n\t" "v_mov_b32 v104, %4\n\t" "v_mov_b32 v105, %5\n\t" "v_mov_b32 v106, %6\n\t" "v_mov_b32 v107, %7\n\t" "v_mov_b32 v108, %8\n\t" "v_mov_b32 v110, %10\n\t" "v_mov_b32 v111, %11\n\t"
        :: "v"(c.ka), "v"(c.kt), "v"(c.qa), "v"(c.qt), "v"(c.va), "v"(c.ta), "v"(c.l0), "v"(c.av), "v"(c.tv0), "v"(c.ot), "v"(c.sd), "v"(c.sstr));
}
DEV void xt_get(u32 (&x)[10]) {
    asm volatile("v_mov_b32 %0, v88\n\t" "v_mov_b32 %1, v89\n\t" "v_mov_b32 %2, v90\n\t" "v_mov_b32 %3, v91\n\t" "v_mov_b32 %4, v92\n\t" "v_mov_b32 %5, v93\n\t" "v_mov_b32 %6, v94\n\t" "v_mov_b32 %7, v95\n\t" "v_mov_b32 %8, v96\n\t" "v_mov_b32 %9, v97\n\t"
        : "=v"(x[0]), "=v"(x[1]), "=v"(x[2]), "=v"(x[3]), "=v"(x[4]), "=v"(x[5]), "=v"(x[6]), "=v"(x[7]), "=v"(x[8]), "=v"(x[9]));
}
DEV void xt_set(const u32 (&x)[10]) {
    asm volatile("v_mov_b32 v88, %0\n\t" "v_mov_b32 v89, %1\n\t" "v_mov_b32 v90, %2\n\t" "v_mov_b32 v91, %3\n\t" "v_mov_b32 v92, %4\n\t" "v_mov_b32 v93, %5\n\t" "v_mov_b32 v94, %6\n\t" "v_mov_b32 v95, %7\n\t" "v_mov_b32 v96, %8\n\t" "v_mov_b32 v97, %9\n\t"
        :: "v"(x[0]), "v"(x[1]), "v"(x[2]), "v"(x[3]), "v"(x[4]), "v"(x[5]), "v"(x[6]), "v"(x[7]), "v"(x[8]), "v"(x[9]));
}

DEV void gemm0(u32x4 srd, int xa, u32 iv, u32 si, int (&xt)[3], u32 fo, u32 fexp, u32 cs, u64 va, u64 v0, u32& cn) {
    int xb; u32 fl, sf;
    asm volatile(
        "s_nop 4\n\t"
        "s_setprio 2\n\t"
        "buffer_load_dwordx4 v[112:115], %[iv], %[ssrd], %[si] offen offset:0 sc1\n\t"
        "buffer_load_dwordx4 v[116:119], %[iv], %[ssrd], %[si] offen offset:1024 sc1\n\t"
        "buffer_load_dwordx4 v[120:123], %[iv], %[ssrd], %[si] offen offset:2048 sc1\n\t"
        "buffer_load_dwordx4 v[124:127], %[iv], %[ssrd], %[si] offen offset:3072 sc1\n\t"
        "s_add_u32 %[si], %[si], 4096\n\t"
        "buffer_load_dwordx4 v[128:131], %[iv], %[ssrd], %[si] offen offset:0 sc1\n\t"
        "buffer_load_dwordx4 v[132:135], %[iv], %[ssrd], %[si] offen offset:1024 sc1\n\t"
        "buffer_load_dwordx4 v[136:139], %[iv], %[ssrd], %[si] offen offset:2048 sc1\n\t"
        "buffer_load_dwordx4 v[140:143], %[iv], %[ssrd], %[si] offen offset:3072 sc1\n\t"
        "s_add_u32 %[si], %[si], 4096\n\t"
        "buffer_load_dwordx4 v[144:147], %[iv], %[ssrd], %[si] offen offset:0 sc1\n\t"
        "buffer_load_dwordx4 v[148:151], %[iv], %[ssrd], %[si] offen offset:1024 sc1\n\t"
        "ds_read_b128 v[88:91], %[x0]\n\t"
        "ds_read_b128 v[92:95], %[x1]\n\t"
        "ds_read_b64 v[96:97], %[x2]\n\t"
        "v_xor_b32 %[xb], 64, %[xa]\n\t"
        "ds_read_b128 v[24:27], %[xa] offset:0\n\t"
        "ds_read_b128 v[28:31], %[xa] offset:2048\n\t"
        "ds_read_b128 v[32:35], %[xa] offset:4096\n\t"
        "ds_read_b128 v[36:39], %[xa] offset:6144\n\t"
        "ds_read_b128 v[40:43], %[xa] offset:8192\n\t"
        "ds_read_b128 v[44:47], %[xb] offset:0\n\t"
        "s_waitcnt vmcnt(8) lgkmcnt(5)\n\t"
        "v_mfma_f32_16x16x32_bf16 v[48:51], v[112:115], v[24:27], 0\n\t"
        "v_mfma_f32_16x16x32_bf16 v[68:71], v[116:119], v[24:27], 0\n\t"
        "ds_read_b128 v[24:27], %[xb] offset:2048\n\t"
        "s_waitcnt lgkmcnt(5)\n\t"
        "v_mfma_f32_16x16x32_bf16 v[52:55], v[112:115], v[28:31], 0\n\t"
        "v_mfma_f32_16x16x32_bf16 v[72:75], v[116:119], v[28:31], 0\n\t"
        "buffer_load_dwordx4 v[152:155], %[iv], %[ssrd], %[si] offen offset:2048 sc1\n\t"
        "buffer_load_dwordx4 v[156:159], %[iv], %[ssrd], %[si] offen offset:3072 sc1\n\t"
        "ds_read_b128 v[28:31], %[xb] offset:4096\n\t"
        "s_waitcnt lgkmcnt(5)\n\t"
        "v_mfma_f32_16x16x32_bf16 v[56:59], v[112:115], v[32:35], 0\n\t"
        "v_mfma_f32_16x16x32_bf16 v[76:79], v[116:119], v[32:35], 0\n\t"
        "ds_read_b128 v[32:35], %[xb] offset:6144\n\t"
        "s_waitcnt lgkmcnt(5)\n\t"
        "v_mfma_f32_16x16x32_bf16 v[60:63], v[112:115], v[36:39], 0\n\t"
        "v_mfma_f32_16x16x32_bf16 v[80:83], v[116:119], v[36:39], 0\n\t"
        "ds_read_b128 v[36:39], %[xb] offset:8192\n\t"
        "s_waitcnt lgkmcnt(5)\n\t"
        "v_mfma_f32_16x16x32_bf16 v[64:67], v[112:115], v[40:43], 0\n\t"
        "v_mfma_f32_16x16x32_bf16 v[84:87], v[116:119], v[40:43], 0\n\t"
        "ds_read_b128 v[40:43], %[xa] offset:10240\n\t"
        "s_waitcnt vmcnt(8) lgkmcnt(5)\n\t"
        "v_mfma_f32_16x16x32_bf16 v[48:51], v[120:123], v[44:47], v[48:51]\n\t"
        "v_mfma_f32_16x16x32_bf16 v[68:71], v[124:127], v[44:47], v[68:71]\n\t"
        "ds_read_b128 v[44:47], %[xa] offset:12288\n\t"
        "s_waitcnt lgkmcnt(5)\n\t"
        "v_mfma_f32_16x16x32_bf16 v[52:55], v[120:123], v[24:27], v[52:55]\n\t"
        "v_mfma_f32_16x16x32_bf16 v[72:75], v[124:127], v[24:27], v[72:75]\n\t"
        "s_add_u32 %[si], %[si], 4096\n\t"
        "buffer_load_dwordx4 v[160:163], %[iv], %[ssrd], %[si] offen offset:0 sc1\n\t"
        "buffer_load_dwordx4 v[164:167], %[iv], %[ssrd], %[si] offen offset:1024 sc1\n\t"
        "ds_read_b128 v[24:27], %[xa] offset:14336\n\t"
        "s_waitcnt lgkmcnt(5)\n\t"
        "v_mfma_f32_16x16x32_bf16 v[56:59], v[120:123], v[28:31], v[56:59]\n\t"
        "v_mfma_f32_16x16x32_bf16 v[76:79], v[124:127], v[28:31], v[76:79]\n\t"
        "ds_read_b128 v[28:31], %[xa] offset:16384\n\t"
        "s_waitcnt lgkmcnt(5)\n\t"
        "v_mfma_f32_16x16x32_bf16 v[60:63], v[120:123], v[32:35], v[60:63]\n\t"
        "v_mfma_f32_16x16x32_bf16 v[80:83], v[124:127], v[32:35], v[80:83]\n\t"
        "ds_read_b128 v[32:35], %[xa] offset:18432\n\t"
        "s_waitcnt lgkmcnt(5)\n\t"
        "v_mfma_f32_16x16x32_bf16 v[64:67], v[120:123], v[36:39], v[64:67]\n\t"
        "v_mfma_f32_16x16x32_bf16 v[84:87], v[124:127], v[36:39], v[84:87]\n\t"
        "ds_read_b128 v[36:39], %[xb] offset:10240\n\t"
        "s_waitcnt vmcnt(8) lgkmcnt(5)\n\t"
        "v_mfma_f32_16x16x32_bf16 v[48:51], v[128:131], v[40:43], v[48:51]\n\t"
        "v_mfma_f32_16x16x32_bf16 v[68:71], v[132:135], v[40:43], v[68:71]\n\t"
        "ds_read_b128 v[40:43], %[xb] offset:12288\n\t"
        "s_waitcnt lgkmcnt(5)\n\t"
        "v_mfma_f32_16x16x32_bf16 v[52:55], v[128:131], v[44:47], v[52:55]\n\t"
        "v_mfma_f32_16x16x32_bf16 v[72:75], v[132:135], v[44:47], v[72:75]\n\t"
        "buffer_load_dwordx4 v[168:171], %[iv], %[ssrd], %[si] offen offset:2048 sc1\n\t"
        "buffer_load_dwordx4 v[172:175], %[iv], %[ssrd], %[si] offen offset:3072 sc1\n\t"
        "ds_read_b128 v[44:47], %[xb] offset:14336\n\t"
        "s_waitcnt lgkmcnt(5)\n\t"
        "v_mfma_f32_16x16x32_bf16 v[56:59], v[128:131], v[24:27], v[56:59]\n\t"
        "v_mfma_f32_16x16x32_bf16 v[76:79], v[132:135], v[24:27], v[76:79]\n\t"
        "ds_read_b128 v[24:27], %[xb] offset:16384\n\t"
        "s_waitcnt lgkmcnt(5)\n\t"
        "v_mfma_f32_16x16x32_bf16 v[60:63], v[128:131], v[28:31], v[60:63]\n\t"
        "v_mfma_f32_16x16x32_bf16 v[80:83], v[132:135], v[28:31], v[80:83]\n\t"
        "ds_read_b128 v[28:31], %[xb] offset:18432\n\t"
        "s_waitcnt lgkmcnt(5)\n\t"
        "v_mfma_f32_16x16x32_bf16 v[64:67], v[128:131], v[32:35], v[64:67]\n\t"
        "v_mfma_f32_16x16x32_bf16 v[84:87], v[132:135], v[32:35], v[84:87]\n\t"
        "ds_read_b128 v[32:35], %[xa] offset:20480\n\t"
        "s_waitcnt vmcnt(8) lgkmcnt(5)\n\t"
        "v_mfma_f32_16x16x32_bf16 v[48:51], v[136:139], v[36:39], v[48:51]\n\t"
        "v_mfma_f32_16x16x32_bf16 v[68:71], v[140:143], v[36:39], v[68:71]\n\t"
        "ds_read_b128 v[36:39], %[xa] offset:22528\n\t"
        "s_waitcnt lgkmcnt(5)\n\t"
        "v_mfma_f32_16x16x32_bf16 v[52:55], v[136:139], v[40:43], v[52:55]\n\t"
        "v_mfma_f32_16x16x32_bf16 v[72:75], v[140:143], v[40:43], v[72:75]\n\t"
        "s_add_u32 %[si], %[si], 4096\n\t"
        "buffer_load_dwordx4 v[176:179], %[iv], %[ssrd], %[si] offen offset:0 sc1\n\t"
        "buffer_load_dwordx4 v[180:183], %[iv], %[ssrd], %[si] offen offset:1024 sc1\n\t"
        "ds_read_b128 v[40:43], %[xa] offset:24576\n\t"
        "s_waitcnt lgkmcnt(5)\n\t"
        "v_mfma_f32_16x16x32_bf16 v[56:59], v[136:139], v[44:47], v[56:59]\n\t"
        "v_mfma_f32_16x16x32_bf16 v[76:79], v[140:143], v[44:47], v[76:79]\n\t"
        "ds_read_b128 v[44:47], %[xa] offset:26624\n\t"
        "s_waitcnt lgkmcnt(5)\n\t"
        "v_mfma_f32_16x16x32_bf16 v[60:63], v[136:139], v[24:27], v[60:63]\n\t"
        "v_mfma_f32_16x16x32_bf16 v[80:83], v[140:143], v[24:27], v[80:83]\n\t"
        "ds_read_b128 v[24:27], %[xa] offset:28672\n\t"
        "s_waitcnt lgkmcnt(5)\n\t"
        "v_mfma_f32_16x16x32_bf16 v[64:67], v[136:139], v[28:31], v[64:67]\n\t"
        "v_mfma_f32_16x16x32_bf16 v[84:87], v[140:143], v[28:31], v[84:87]\n\t"
        "ds_read_b128 v[28:31], %[xb] offset:20480\n\t"
        "s_waitcnt vmcnt(8) lgkmcnt(5)\n\t"
        "v_mfma_f32_16x16x32_bf16 v[48:51], v[144:147], v[32:35], v[48:51]\n\t"
        "v_mfma_f32_16x16x32_bf16 v[68:71], v[148:151], v[32:35], v[68:71]\n\t"
        "ds_read_b128 v[32:35], %[xb] offset:22528\n\t"
        "s_waitcnt lgkmcnt(5)\n\t"
        "v_mfma_f32_16x16x32_bf16 v[52:55], v[144:147], v[36:39], v[52:55]\n\t"
        "v_mfma_f32_16x16x32_bf16 v[72:75], v[148:151], v[36:39], v[72:75]\n\t"
        "buffer_load_dwordx4 v[184:187], %[iv], %[ssrd], %[si] offen offset:2048 sc1\n\t"
        "buffer_load_dwordx4 v[188:191], %[iv], %[ssrd], %[si] offen offset:3072 sc1\n\t"
        "ds_read_b128 v[36:39], %[xb] offset:24576\n\t"
        "s_waitcnt lgkmcnt(5)\n\t"
        "v_mfma_f32_16x16x32_bf16 v[56:59], v[144:147], v[40:43], v[56:59]\n\t"
        "v_mfma_f32_16x16x32_bf16 v[76:79], v[148:151], v[40:43], v[76:79]\n\t"
        "ds_read_b128 v[40:43], %[xb] offset:26624\n\t"
        "s_waitcnt lgkmcnt(5)\n\t"
        "v_mfma_f32_16x16x32_bf16 v[60:63], v[144:147], v[44:47], v[60:63]\n\t"
        "v_mfma_f32_16x16x32_bf16 v[80:83], v[148:151], v[44:47], v[80:83]\n\t"
        "ds_read_b128 v[44:47], %[xb] offset:28672\n\t"
        "s_waitcnt lgkmcnt(5)\n\t"
        "v_mfma_f32_16x16x32_bf16 v[64:67], v[144:147], v[24:27], v[64:67]\n\t"
        "v_mfma_f32_16x16x32_bf16 v[84:87], v[148:151], v[24:27], v[84:87]\n\t"
        "ds_read_b128 v[24:27], %[xa] offset:30720\n\t"
        "s_waitcnt vmcnt(8) lgkmcnt(5)\n\t"
        "v_mfma_f32_16x16x32_bf16 v[48:51], v[152:155], v[28:31], v[48:51]\n\t"
        "v_mfma_f32_16x16x32_bf16 v[68:71], v[156:159], v[28:31], v[68:71]\n\t"
        "ds_read_b128 v[28:31], %[xa] offset:32768\n\t"
        "s_waitcnt lgkmcnt(5)\n\t"
        "v_mfma_f32_16x16x32_bf16 v[52:55], v[152:155], v[32:35], v[52:55]\n\t"
        "v_mfma_f32_16x16x32_bf16 v[72:75], v[156:159], v[32:35], v[72:75]\n\t"
        "s_add_u32 %[si], %[si], 4096\n\t"
        "buffer_load_dwordx4 v[192:195], %[iv], %[ssrd], %[si] offen offset:0 sc1\n\t"
        "buffer_load_dwordx4 v[196:199], %[iv], %[ssrd], %[si] offen offset:1024 sc1\n\t"
        "ds_read_b128 v[32:35], %[xa] offset:34816\n\t"
        "s_waitcnt lgkmcnt(5)\n\t"
        "v_mfma_f32_16x16x32_bf16 v[56:59], v[152:155], v[36:39], v[56:59]\n\t"
        "v_mfma_f32_16x16x32_bf16 v[76:79], v[156:159], v[36:39], v[76:79]\n\t"
        "ds_read_b128 v[36:39], %[xa] offset:36864\n\t"
        "s_waitcnt lgkmcnt(5)\n\t"
        "v_mfma_f32_16x16x32_bf16 v[60:63], v[152:155], v[40:43], v[60:63]\n\t"
        "v_mfma_f32_16x16x32_bf16 v[80:83], v[156:159], v[40:43], v[80:83]\n\t"
        "ds_read_b128 v[40:43], %[xa] offset:38912\n\t"
        "s_waitcnt lgkmcnt(5)\n\t"
        "v_mfma_f32_16x16x32_bf16 v[64:67], v[152:155], v[44:47], v[64:67]\n\t"
        "v_mfma_f32_16x16x32_bf16 v[84:87], v[156:159], v[44:47], v[84:87]\n\t"
        "ds_read_b128 v[44:47], %[xb] offset:30720\n\t"
        "s_setprio 1\n\t"
        "s_waitcnt vmcnt(8) lgkmcnt(5)\n\t"
        "v_mfma_f32_16x16x32_bf16 v[48:51], v[160:163], v[24:27], v[48:51]\n\t"
        "v_mfma_f32_16x16x32_bf16 v[68:71], v[164:167], v[24:27], v[68:71]\n\t"
        "ds_read_b128 v[24:27], %[xb] offset:32768\n\t"
        "s_waitcnt lgkmcnt(5)\n\t"
        "v_mfma_f32_16x16x32_bf16 v[52:55], v[160:163], v[28:31], v[52:55]\n\t"
        "v_mfma_f32_16x16x32_bf16 v[72:75], v[164:167], v[28:31], v[72:75]\n\t"
        "buffer_load_dwordx4 v[200:203], %[iv], %[ssrd], %[si] offen offset:2048 sc1\n\t"
        "buffer_load_dwordx4 v[204:207], %[iv], %[ssrd], %[si] offen offset:3072 sc1\n\t"
        "ds_read_b128 v[28:31], %[xb] offset:34816\n\t"
        "s_waitcnt lgkmcnt(5)\n\t"
        "v_mfma_f32_16x16x32_bf16 v[56:59], v[160:163], v[32:35], v[56:59]\n\t"
        "v_mfma_f32_16x16x32_bf16 v[76:79], v[164:167], v[32:35], v[76:79]\n\t"
        "ds_read_b128 v[32:35], %[xb] offset:36864\n\t"
        "s_waitcnt lgkmcnt(5)\n\t"
        "v_mfma_f32_16x16x32_bf16 v[60:63], v[160:163], v[36:39], v[60:63]\n\t"
        "v_mfma_f32_16x16x32_bf16 v[80:83], v[164:167], v[36:39], v[80:83]\n\t"
        "ds_read_b128 v[36:39], %[xb] offset:38912\n\t"
        "s_waitcnt lgkmcnt(5)\n\t"
        "v_mfma_f32_16x16x32_bf16 v[64:67], v[160:163], v[40:43], v[64:67]\n\t"
        "v_mfma_f32_16x16x32_bf16 v[84:87], v[164:167], v[40:43], v[84:87]\n\t"
        "ds_read_b128 v[40:43], %[xa] offset:40960\n\t"
        "v_mov_b32 %[x2], 153984\n\t"
        "v_mov_b32 %[x0], 1\n\t"
        "s_mov_b64 exec, 1\n\t"
        "ds_add_u32 %[x2], %[x0] offset:%[omc]+4\n\t"
        "s_mov_b64 exec, -1\n\t"
        "s_waitcnt vmcnt(8) lgkmcnt(6)\n\t"
        "v_mfma_f32_16x16x32_bf16 v[48:51], v[168:171], v[44:47], v[48:51]\n\t"
        "v_mfma_f32_16x16x32_bf16 v[68:71], v[172:175], v[44:47], v[68:71]\n\t"
        "ds_read_b128 v[44:47], %[xa] offset:43008\n\t"
        "s_waitcnt lgkmcnt(6)\n\t"
        "v_mfma_f32_16x16x32_bf16 v[52:55], v[168:171], v[24:27], v[52:55]\n\t"
        "v_mfma_f32_16x16x32_bf16 v[72:75], v[172:175], v[24:27], v[72:75]\n\t"
        "s_add_u32 %[si], %[si], 4096\n\t"
        "buffer_load_dwordx4 v[208:211], %[iv], %[ssrd], %[si] offen offset:0 sc1\n\t"
        "buffer_load_dwordx4 v[212:215], %[iv], %[ssrd], %[si] offen offset:1024 sc1\n\t"
        "ds_read_b128 v[24:27], %[xa] offset:45056\n\t"
        "s_waitcnt lgkmcnt(6)\n\t"
        "v_mfma_f32_16x16x32_bf16 v[56:59], v[168:171], v[28:31], v[56:59]\n\t"
        "v_mfma_f32_16x16x32_bf16 v[76:79], v[172:175], v[28:31], v[76:79]\n\t"
        "ds_read_b128 v[28:31], %[xa] offset:47104\n\t"
        "s_waitcnt lgkmcnt(6)\n\t"
        "v_mfma_f32_16x16x32_bf16 v[60:63], v[168:171], v[32:35], v[60:63]\n\t"
        "v_mfma_f32_16x16x32_bf16 v[80:83], v[172:175], v[32:35], v[80:83]\n\t"
        "ds_read_b128 v[32:35], %[xa] offset:49152\n\t"
        "s_waitcnt lgkmcnt(6)\n\t"
        "v_mfma_f32_16x16x32_bf16 v[64:67], v[168:171], v[36:39], v[64:67]\n\t"
        "v_mfma_f32_16x16x32_bf16 v[84:87], v[172:175], v[36:39], v[84:87]\n\t"
        "ds_read_b128 v[36:39], %[xb] offset:40960\n\t"
        "s_waitcnt vmcnt(8) lgkmcnt(6)\n\t"
        "v_mfma_f32_16x16x32_bf16 v[48:51], v[176:179], v[40:43], v[48:51]\n\t"
        "v_mfma_f32_16x16x32_bf16 v[68:71], v[180:183], v[40:43], v[68:71]\n\t"
        "ds_read_b128 v[40:43], %[xb] offset:43008\n\t"
        "s_waitcnt lgkmcnt(5)\n\t"
        "v_mfma_f32_16x16x32_bf16 v[52:55], v[176:179], v[44:47], v[52:55]\n\t"
        "v_mfma_f32_16x16x32_bf16 v[72:75], v[180:183], v[44:47], v[72:75]\n\t"
        "buffer_load_dwordx4 v[216:219], %[iv], %[ssrd], %[si] offen offset:2048 sc1\n\t"
        "buffer_load_dwordx4 v[220:223], %[iv], %[ssrd], %[si] offen offset:3072 sc1\n\t"
        "ds_read_b128 v[44:47], %[xb] offset:45056\n\t"
        "s_waitcnt lgkmcnt(5)\n\t"
        "v_mfma_f32_16x16x32_bf16 v[56:59], v[176:179], v[24:27], v[56:59]\n\t"
        "v_mfma_f32_16x16x32_bf16 v[76:79], v[180:183], v[24:27], v[76:79]\n\t"
        "ds_read_b128 v[24:27], %[xb] offset:47104\n\t"
        "s_waitcnt lgkmcnt(5)\n\t"
        "v_mfma_f32_16x16x32_bf16 v[60:63], v[176:179], v[28:31], v[60:63]\n\t"
        "v_mfma_f32_16x16x32_bf16 v[80:83], v[180:183], v[28:31], v[80:83]\n\t"
        "ds_read_b128 v[28:31], %[xb] offset:49152\n\t"
        "s_waitcnt lgkmcnt(5)\n\t"
        "v_mfma_f32_16x16x32_bf16 v[64:67], v[176:179], v[32:35], v[64:67]\n\t"
        "v_mfma_f32_16x16x32_bf16 v[84:87], v[180:183], v[32:35], v[84:87]\n\t"
        "v_add_u32 %[xa], 51200, %[xa]\n\t"
        "ds_read_b128 v[32:35], %[xa] offset:0\n\t"
        "s_waitcnt vmcnt(8) lgkmcnt(5)\n\t"
        "v_mfma_f32_16x16x32_bf16 v[48:51], v[184:187], v[36:39], v[48:51]\n\t"
        "v_mfma_f32_16x16x32_bf16 v[68:71], v[188:191], v[36:39], v[68:71]\n\t"
        "ds_read_b128 v[36:39], %[xa] offset:2048\n\t"
        "s_waitcnt lgkmcnt(5)\n\t"
        "v_mfma_f32_16x16x32_bf16 v[52:55], v[184:187], v[40:43], v[52:55]\n\t"
        "v_mfma_f32_16x16x32_bf16 v[72:75], v[188:191], v[40:43], v[72:75]\n\t"
        "s_add_u32 %[si], %[si], 4096\n\t"
        "buffer_load_dwordx4 v[224:227], %[iv], %[ssrd], %[si] offen offset:0 sc1\n\t"
        "buffer_load_dwordx4 v[228:231], %[iv], %[ssrd], %[si] offen offset:1024 sc1\n\t"
        "ds_read_b128 v[40:43], %[xa] offset:4096\n\t"
        "s_waitcnt lgkmcnt(5)\n\t"
        "v_mfma_f32_16x16x32_bf16 v[56:59], v[184:187], v[44:47], v[56:59]\n\t"
        "v_mfma_f32_16x16x32_bf16 v[76:79], v[188:191], v[44:47], v[76:79]\n\t"
        "ds_read_b128 v[44:47], %[xa] offset:6144\n\t"
        "s_waitcnt lgkmcnt(5)\n\t"
        "v_mfma_f32_16x16x32_bf16 v[60:63], v[184:187], v[24:27], v[60:63]\n\t"
        "v_mfma_f32_16x16x32_bf16 v[80:83], v[188:191], v[24:27], v[80:83]\n\t"
        "ds_read_b128 v[24:27], %[xa] offset:8192\n\t"
        "s_waitcnt lgkmcnt(5)\n\t"
        "v_mfma_f32_16x16x32_bf16 v[64:67], v[184:187], v[28:31], v[64:67]\n\t"
        "v_mfma_f32_16x16x32_bf16 v[84:87], v[188:191], v[28:31], v[84:87]\n\t"
        "v_add_u32 %[xb], 51200, %[xb]\n\t"
        "ds_read_b128 v[28:31], %[xb] offset:0\n\t"
        "s_waitcnt vmcnt(8) lgkmcnt(5)\n\t"
        "v_mfma_f32_16x16x32_bf16 v[48:51], v[192:195], v[32:35], v[48:51]\n\t"
        "v_mfma_f32_16x16x32_bf16 v[68:71], v[196:199], v[32:35], v[68:71]\n\t"
        "ds_read_b128 v[32:35], %[xb] offset:2048\n\t"
        "s_waitcnt lgkmcnt(5)\n\t"
        "v_mfma_f32_16x16x32_bf16 v[52:55], v[192:195], v[36:39], v[52:55]\n\t"
        "v_mfma_f32_16x16x32_bf16 v[72:75], v[196:199], v[36:39], v[72:75]\n\t"
        "buffer_load_dwordx4 v[232:235], %[iv], %[ssrd], %[si] offen offset:2048 sc1\n\t"
        "buffer_load_dwordx4 v[236:239], %[iv], %[ssrd], %[si] offen offset:3072 sc1\n\t"
        "ds_read_b128 v[36:39], %[xb] offset:4096\n\t"
        "s_waitcnt lgkmcnt(5)\n\t"
        "v_mfma_f32_16x16x32_bf16 v[56:59], v[192:195], v[40:43], v[56:59]\n\t"
        "v_mfma_f32_16x16x32_bf16 v[76:79], v[196:199], v[40:43], v[76:79]\n\t"
        "ds_read_b128 v[40:43], %[xb] offset:6144\n\t"
        "s_waitcnt lgkmcnt(5)\n\t"
        "v_mfma_f32_16x16x32_bf16 v[60:63], v[192:195], v[44:47], v[60:63]\n\t"
        "v_mfma_f32_16x16x32_bf16 v[80:83], v[196:199], v[44:47], v[80:83]\n\t"
        "ds_read_b128 v[44:47], %[xb] offset:8192\n\t"
        "s_waitcnt lgkmcnt(5)\n\t"
        "v_mfma_f32_16x16x32_bf16 v[64:67], v[192:195], v[24:27], v[64:67]\n\t"
        "v_mfma_f32_16x16x32_bf16 v[84:87], v[196:199], v[24:27], v[84:87]\n\t"
        "ds_read_b128 v[24:27], %[xa] offset:10240\n\t"
        "s_waitcnt vmcnt(8) lgkmcnt(5)\n\t"
        "v_mfma_f32_16x16x32_bf16 v[48:51], v[200:203], v[28:31], v[48:51]\n\t"
        "v_mfma_f32_16x16x32_bf16 v[68:71], v[204:207], v[28:31], v[68:71]\n\t"
        "ds_read_b128 v[28:31], %[xa] offset:12288\n\t"
        "s_waitcnt lgkmcnt(5)\n\t"
        "v_mfma_f32_16x16x32_bf16 v[52:55], v[200:203], v[32:35], v[52:55]\n\t"
        "v_mfma_f32_16x16x32_bf16 v[72:75], v[204:207], v[32:35], v[72:75]\n\t"
        "s_add_u32 %[si], %[si], 4096\n\t"
        "buffer_load_dwordx4 v[240:243], %[iv], %[ssrd], %[si] offen offset:0 sc1\n\t"
        "buffer_load_dwordx4 v[244:247], %[iv], %[ssrd], %[si] offen offset:1024 sc1\n\t"
        "ds_read_b128 v[32:35], %[xa] offset:14336\n\t"
        "s_waitcnt lgkmcnt(5)\n\t"
        "v_mfma_f32_16x16x32_bf16 v[56:59], v[200:203], v[36:39], v[56:59]\n\t"
        "v_mfma_f32_16x16x32_bf16 v[76:79], v[204:207], v[36:39], v[76:79]\n\t"
        "ds_read_b128 v[36:39], %[xa] offset:16384\n\t"
        "s_waitcnt lgkmcnt(5)\n\t"
        "v_mfma_f32_16x16x32_bf16 v[60:63], v[200:203], v[40:43], v[60:63]\n\t"
        "v_mfma_f32_16x16x32_bf16 v[80:83], v[204:207], v[40:43], v[80:83]\n\t"
        "ds_read_b128 v[40:43], %[xa] offset:18432\n\t"
        "s_waitcnt lgkmcnt(5)\n\t"
        "v_mfma_f32_16x16x32_bf16 v[64:67], v[200:203], v[44:47], v[64:67]\n\t"
        "v_mfma_f32_16x16x32_bf16 v[84:87], v[204:207], v[44:47], v[84:87]\n\t"
        "ds_read_b128 v[44:47], %[xb] offset:10240\n\t"
        "s_setprio 0\n\t"
        "s_waitcnt vmcnt(8) lgkmcnt(5)\n\t"
        "v_mfma_f32_16x16x32_bf16 v[48:51], v[208:211], v[24:27], v[48:51]\n\t"
        "v_mfma_f32_16x16x32_bf16 v[68:71], v[212:215], v[24:27], v[68:71]\n\t"
        "ds_read_b128 v[24:27], %[xb] offset:12288\n\t"
        "s_waitcnt lgkmcnt(5)\n\t"
        "v_mfma_f32_16x16x32_bf16 v[52:55], v[208:211], v[28:31], v[52:55]\n\t"
        "v_mfma_f32_16x16x32_bf16 v[72:75], v[212:215], v[28:31], v[72:75]\n\t"
        "buffer_load_dwordx4 v[248:251], %[iv], %[ssrd], %[si] offen offset:2048 sc1\n\t"
        "buffer_load_dwordx4 v[252:255], %[iv], %[ssrd], %[si] offen offset:3072 sc1\n\t"
        "ds_read_b128 v[28:31], %[xb] offset:14336\n\t"
        "s_waitcnt lgkmcnt(5)\n\t"
        "v_mfma_f32_16x16x32_bf16 v[56:59], v[208:211], v[32:35], v[56:59]\n\t"
        "v_mfma_f32_16x16x32_bf16 v[76:79], v[212:215], v[32:35], v[76:79]\n\t"
        "ds_read_b128 v[32:35], %[xb] offset:16384\n\t"
        "s_waitcnt lgkmcnt(5)\n\t"
        "v_mfma_f32_16x16x32_bf16 v[60:63], v[208:211], v[36:39], v[60:63]\n\t"
        "v_mfma_f32_16x16x32_bf16 v[80:83], v[212:215], v[36:39], v[80:83]\n\t"
        "ds_read_b128 v[36:39], %[xb] offset:18432\n\t"
        "s_waitcnt lgkmcnt(5)\n\t"
        "v_mfma_f32_16x16x32_bf16 v[64:67], v[208:211], v[40:43], v[64:67]\n\t"
        "v_mfma_f32_16x16x32_bf16 v[84:87], v[212:215], v[40:43], v[84:87]\n\t"
        "ds_read_b128 v[40:43], %[xa] offset:20480\n\t"
        "s_waitcnt vmcnt(8) lgkmcnt(5)\n\t"
        "v_mfma_f32_16x16x32_bf16 v[48:51], v[216:219], v[44:47], v[48:51]\n\t"
        "v_mfma_f32_16x16x32_bf16 v[68:71], v[220:223], v[44:47], v[68:71]\n\t"
        "ds_read_b128 v[44:47], %[xa] offset:22528\n\t"
        "s_waitcnt lgkmcnt(5)\n\t"
        "v_mfma_f32_16x16x32_bf16 v[52:55], v[216:219], v[24:27], v[52:55]\n\t"
        "v_mfma_f32_16x16x32_bf16 v[72:75], v[220:223], v[24:27], v[72:75]\n\t"
        "buffer_load_dword %[fl], %[fo], %[ssrd], 0 offen sc1\n\t"
        "ds_read_b128 v[24:27], %[xa] offset:24576\n\t"
        "s_waitcnt lgkmcnt(5)\n\t"
        "v_mfma_f32_16x16x32_bf16 v[56:59], v[216:219], v[28:31], v[56:59]\n\t"
        "v_mfma_f32_16x16x32_bf16 v[76:79], v[220:223], v[28:31], v[76:79]\n\t"
        "ds_read_b128 v[28:31], %[xa] offset:26624\n\t"
        "s_waitcnt lgkmcnt(5)\n\t"
        "v_mfma_f32_16x16x32_bf16 v[60:63], v[216:219], v[32:35], v[60:63]\n\t"
        "v_mfma_f32_16x16x32_bf16 v[80:83], v[220:223], v[32:35], v[80:83]\n\t"
        "ds_read_b128 v[32:35], %[xa] offset:28672\n\t"
        "s_waitcnt lgkmcnt(5)\n\t"
        "v_mfma_f32_16x16x32_bf16 v[64:67], v[216:219], v[36:39], v[64:67]\n\t"
        "v_mfma_f32_16x16x32_bf16 v[84:87], v[220:223], v[36:39], v[84:87]\n\t"
        "ds_read_b128 v[36:39], %[xb] offset:20480\n\t"
        "s_waitcnt vmcnt(7) lgkmcnt(5)\n\t"
        "v_mfma_f32_16x16x32_bf16 v[48:51], v[224:227], v[40:43], v[48:51]\n\t"
        "v_mfma_f32_16x16x32_bf16 v[68:71], v[228:231], v[40:43], v[68:71]\n\t"
        "ds_read_b128 v[40:43], %[xb] offset:22528\n\t"
        "s_waitcnt lgkmcnt(5)\n\t"
        "v_mfma_f32_16x16x32_bf16 v[52:55], v[224:227], v[44:47], v[52:55]\n\t"
        "v_mfma_f32_16x16x32_bf16 v[72:75], v[228:231], v[44:47], v[72:75]\n\t"
        "ds_read_b128 v[44:47], %[xb] offset:24576\n\t"
        "s_waitcnt lgkmcnt(5)\n\t"
        "v_mfma_f32_16x16x32_bf16 v[56:59], v[224:227], v[24:27], v[56:59]\n\t"
        "v_mfma_f32_16x16x32_bf16 v[76:79], v[228:231], v[24:27], v[76:79]\n\t"
        "ds_read_b128 v[24:27], %[xb] offset:26624\n\t"
        "s_waitcnt lgkmcnt(5)\n\t"
        "v_mfma_f32_16x16x32_bf16 v[60:63], v[224:227], v[28:31], v[60:63]\n\t"
        "v_mfma_f32_16x16x32_bf16 v[80:83], v[228:231], v[28:31], v[80:83]\n\t"
        "ds_read_b128 v[28:31], %[xb] offset:28672\n\t"
        "s_waitcnt lgkmcnt(5)\n\t"
        "v_mfma_f32_16x16x32_bf16 v[64:67], v[224:227], v[32:35], v[64:67]\n\t"
        "v_mfma_f32_16x16x32_bf16 v[84:87], v[228:231], v[32:35], v[84:87]\n\t"
        "ds_read_b128 v[32:35], %[xa] offset:30720\n\t"
        "s_waitcnt vmcnt(5) lgkmcnt(5)\n\t"
        "v_mfma_f32_16x16x32_bf16 v[48:51], v[232:235], v[36:39], v[48:51]\n\t"
        "v_mfma_f32_16x16x32_bf16 v[68:71], v[236:239], v[36:39], v[68:71]\n\t"
        "ds_read_b128 v[36:39], %[xa] offset:32768\n\t"
        "s_waitcnt lgkmcnt(5)\n\t"
        "v_mfma_f32_16x16x32_bf16 v[52:55], v[232:235], v[40:43], v[52:55]\n\t"
        "v_mfma_f32_16x16x32_bf16 v[72:75], v[236:239], v[40:43], v[72:75]\n\t"
        "ds_read_b128 v[40:43], %[xa] offset:34816\n\t"
        "s_waitcnt lgkmcnt(5)\n\t"
        "v_mfma_f32_16x16x32_bf16 v[56:59], v[232:235], v[44:47], v[56:59]\n\t"
        "v_mfma_f32_16x16x32_bf16 v[76:79], v[236:239], v[44:47], v[76:79]\n\t"
        "ds_read_b128 v[44:47], %[xa] offset:36864\n\t"
        "s_waitcnt lgkmcnt(5)\n\t"
        "v_mfma_f32_16x16x32_bf16 v[60:63], v[232:235], v[24:27], v[60:63]\n\t"
        "v_mfma_f32_16x16x32_bf16 v[80:83], v[236:239], v[24:27], v[80:83]\n\t"
        "ds_read_b128 v[24:27], %[xa] offset:38912\n\t"
        "s_waitcnt lgkmcnt(5)\n\t"
        "v_mfma_f32_16x16x32_bf16 v[64:67], v[232:235], v[28:31], v[64:67]\n\t"
        "v_mfma_f32_16x16x32_bf16 v[84:87], v[236:239], v[28:31], v[84:87]\n\t"
        "ds_read_b128 v[28:31], %[xb] offset:30720\n\t"
        "s_waitcnt vmcnt(3) lgkmcnt(5)\n\t"
        "v_mfma_f32_16x16x32_bf16 v[48:51], v[240:243], v[32:35], v[48:51]\n\t"
        "v_mfma_f32_16x16x32_bf16 v[68:71], v[244:247], v[32:35], v[68:71]\n\t"
        "ds_read_b128 v[32:35], %[xb] offset:32768\n\t"
        "s_waitcnt lgkmcnt(5)\n\t"
        "v_mfma_f32_16x16x32_bf16 v[52:55], v[240:243], v[36:39], v[52:55]\n\t"
        "v_mfma_f32_16x16x32_bf16 v[72:75], v[244:247], v[36:39], v[72:75]\n\t"
        "ds_read_b128 v[36:39], %[xb] offset:34816\n\t"
        "v_and_b32 %[x0], 0xf0, v107\n\t"
        "v_lshrrev_b32 %[x0], 1, %[x0]\n\t"
        "v_add_u32 %[x0], 153344, %[x0]\n\t"
        "v_lshrrev_b32 %[x1], 8, v107\n\t"
        "v_lshl_add_u32 %[x1], %[x1], 5, %[scs]\n\t"
        "s_waitcnt lgkmcnt(5)\n\t"
        "v_mfma_f32_16x16x32_bf16 v[56:59], v[240:243], v[40:43], v[56:59]\n\t"
        "v_mfma_f32_16x16x32_bf16 v[76:79], v[244:247], v[40:43], v[76:79]\n\t"
        "ds_read_b128 v[40:43], %[xb] offset:36864\n\t"
        "s_waitcnt lgkmcnt(5)\n\t"
        "v_mfma_f32_16x16x32_bf16 v[60:63], v[240:243], v[44:47], v[60:63]\n\t"
        "v_mfma_f32_16x16x32_bf16 v[80:83], v[244:247], v[44:47], v[80:83]\n\t"
        "ds_read_b128 v[44:47], %[xb] offset:38912\n\t"
        "s_waitcnt lgkmcnt(5)\n\t"
        "v_mfma_f32_16x16x32_bf16 v[64:67], v[240:243], v[24:27], v[64:67]\n\t"
        "v_mfma_f32_16x16x32_bf16 v[84:87], v[244:247], v[24:27], v[84:87]\n\t"
        "s_waitcnt vmcnt(1) lgkmcnt(4)\n\t"
        "v_mfma_f32_16x16x32_bf16 v[48:51], v[248:251], v[28:31], v[48:51]\n\t"
        "v_mfma_f32_16x16x32_bf16 v[68:71], v[252:255], v[28:31], v[68:71]\n\t"
        "s_waitcnt lgkmcnt(3)\n\t"
        "v_mfma_f32_16x16x32_bf16 v[52:55], v[248:251], v[32:35], v[52:55]\n\t"
        "v_mfma_f32_16x16x32_bf16 v[72:75], v[252:255], v[32:35], v[72:75]\n\t"
        "ds_read_b64 v[34:35], %[x2] offset:%[omc]\n\t"
        "ds_read_b64 v[24:25], %[x0] offset:0\n\t"
        "ds_read_b64 v[26:27], %[x0] offset:128\n\t"
        "ds_read_b64 v[28:29], %[x0] offset:256\n\t"
        "ds_read_b64 v[30:31], %[x0] offset:384\n\t"
        "ds_read_b64 v[32:33], %[x0] offset:512\n\t"
        "s_waitcnt lgkmcnt(8)\n\t"
        "v_mfma_f32_16x16x32_bf16 v[56:59], v[248:251], v[36:39], v[56:59]\n\t"
        "v_mfma_f32_16x16x32_bf16 v[76:79], v[252:255], v[36:39], v[76:79]\n\t"
        "s_waitcnt lgkmcnt(7)\n\t"
        "v_mfma_f32_16x16x32_bf16 v[60:63], v[248:251], v[40:43], v[60:63]\n\t"
        "v_mfma_f32_16x16x32_bf16 v[80:83], v[252:255], v[40:43], v[80:83]\n\t"
        "s_waitcnt lgkmcnt(6)\n\t"
        "v_mfma_f32_16x16x32_bf16 v[64:67], v[248:251], v[44:47], v[64:67]\n\t"
        "v_mfma_f32_16x16x32_bf16 v[84:87], v[252:255], v[44:47], v[84:87]\n\t"
        "v_mov_b32_dpp v36, v48 row_newbcast:0 row_mask:0xf bank_mask:0xf bound_ctrl:1\n\t"
        "v_mov_b32_dpp v37, v49 row_newbcast:0 row_mask:0xf bank_mask:0xf bound_ctrl:1\n\t"
        "v_mov_b32_dpp v38, v50 row_newbcast:0 row_mask:0xf bank_mask:0xf bound_ctrl:1\n\t"
        "v_mov_b32_dpp v39, v51 row_newbcast:0 row_mask:0xf bank_mask:0xf bound_ctrl:1\n\t"
        "v_mov_b32_dpp v40, v68 row_newbcast:0 row_mask:0xf bank_mask:0xf bound_ctrl:1\n\t"
        "v_mov_b32_dpp v41, v69 row_newbcast:0 row_mask:0xf bank_mask:0xf bound_ctrl:1\n\t"
        "v_mov_b32_dpp v42, v70 row_newbcast:0 row_mask:0xf bank_mask:0xf bound_ctrl:1\n\t"
        "v_mov_b32_dpp v43, v71 row_newbcast:0 row_mask:0xf bank_mask:0xf bound_ctrl:1\n\t"
        "Lm_%=:\n\t"
        "s_waitcnt vmcnt(0) lgkmcnt(0)\n\t"
        "v_readfirstlane_b32 %[sf], v34\n\t"
        "v_cmp_ne_u32 vcc, %[sexp], %[fl]\n\t"
        "s_cmp_lt_u32 %[sf], 3\n\t"
        "s_cbranch_scc1 Ln_%=\n\t"
        "s_cbranch_vccnz Ln_%=\n\t"
        "Lo_%=:\n\t"
        "s_mov_b64 exec, %[sm16]\n\t"
        "ds_write_b128 %[x1], v[36:39]\n\t"
        "ds_write_b128 %[x1], v[40:43] offset:16\n\t"
        "s_mov_b64 exec, -1\n\t"
        "v_fma_f32 v48, -v24, v36, v48\n\t"
        "v_fma_f32 v49, -v24, v37, v49\n\t"
        "v_fma_f32 v50, -v24, v38, v50\n\t"
        "v_fma_f32 v51, -v24, v39, v51\n\t"
        "v_fma_f32 v68, -v24, v40, v68\n\t"
        "v_fma_f32 v69, -v24, v41, v69\n\t"
        "v_fma_f32 v70, -v24, v42, v70\n\t"
        "v_fma_f32 v71, -v24, v43, v71\n\t"
        "v_mul_f32 v48, v25, v48\n\t"
        "v_mul_f32 v49, v25, v49\n\t"
        "v_mul_f32 v50, v25, v50\n\t"
        "v_mul_f32 v51, v25, v51\n\t"
        "v_mul_f32 v68, v25, v68\n\t"
        "v_mul_f32 v69, v25, v69\n\t"
        "v_mul_f32 v70, v25, v70\n\t"
        "v_mul_f32 v71, v25, v71\n\t"
        "v_cvt_pk_bf16_f32 v48, v48, v68\n\t"
        "v_cvt_pk_bf16_f32 v49, v49, v69\n\t"
        "v_cvt_pk_bf16_f32 v50, v50, v70\n\t"
        "v_cvt_pk_bf16_f32 v51, v51, v71\n\t"
        "v_add_u32 v68, v110, v111\n\t"
        "v_add_u32 v69, v68, v111\n\t"
        "v_add_u32 v70, v69, v111\n\t"
        "v_add_u32 v71, v70, v111\n\t"
        "v_fma_f32 v52, -v26, v36, v52\n\t"
        "v_fma_f32 v53, -v26, v37, v53\n\t"
        "v_fma_f32 v54, -v26, v38, v54\n\t"
        "v_fma_f32 v55, -v26, v39, v55\n\t"
        "v_fma_f32 v72, -v26, v40, v72\n\t"
        "v_fma_f32 v73, -v26, v41, v73\n\t"
        "v_fma_f32 v74, -v26, v42, v74\n\t"
        "v_fma_f32 v75, -v26, v43, v75\n\t"
        "v_mul_f32 v52, v27, v52\n\t"
        "v_mul_f32 v53, v27, v53\n\t"
        "v_mul_f32 v54, v27, v54\n\t"
        "v_mul_f32 v55, v27, v55\n\t"
        "v_mul_f32 v72, v27, v72\n\t"
        "v_mul_f32 v73, v27, v73\n\t"
        "v_mul_f32 v74, v27, v74\n\t"
        "v_mul_f32 v75, v27, v75\n\t"
        "v_cvt_pk_bf16_f32 v52, v52, v72\n\t"
        "v_cvt_pk_bf16_f32 v53, v53, v73\n\t"
        "v_cvt_pk_bf16_f32 v54, v54, v74\n\t"
        "v_cvt_pk_bf16_f32 v55, v55, v75\n\t"
        "v_fma_f32 v56, -v28, v36, v56\n\t"
        "v_fma_f32 v57, -v28, v37, v57\n\t"
        "v_fma_f32 v58, -v28, v38, v58\n\t"
        "v_fma_f32 v59, -v28, v39, v59\n\t"
        "v_fma_f32 v76, -v28, v40, v76\n\t"
        "v_fma_f32 v77, -v28, v41, v77\n\t"
        "v_fma_f32 v78, -v28, v42, v78\n\t"
        "v_fma_f32 v79, -v28, v43, v79\n\t"
        "v_mul_f32 v56, v29, v56\n\t"
        "v_mul_f32 v57, v29, v57\n\t"
        "v_mul_f32 v58, v29, v58\n\t"
        "v_mul_f32 v59, v29, v59\n\t"
        "v_mul_f32 v76, v29, v76\n\t"
        "v_mul_f32 v77, v29, v77\n\t"
        "v_mul_f32 v78, v29, v78\n\t"
        "v_mul_f32 v79, v29, v79\n\t"
        "v_cvt_pk_bf16_f32 v56, v56, v76\n\t"
        "v_cvt_pk_bf16_f32 v57, v57, v77\n\t"
        "v_cvt_pk_bf16_f32 v58, v58, v78\n\t"
        "v_cvt_pk_bf16_f32 v59, v59, v79\n\t"
        "v_fma_f32 v60, -v30, v36, v60\n\t"
        "v_fma_f32 v61, -v30, v37, v61\n\t"
        "v_fma_f32 v62, -v30, v38, v62\n\t"
        "v_fma_f32 v63, -v30, v39, v63\n\t"
        "v_fma_f32 v80, -v30, v40, v80\n\t"
        "v_fma_f32 v81, -v30, v41, v81\n\t"
        "v_fma_f32 v82, -v30, v42, v82\n\t"
        "v_fma_f32 v83, -v30, v43, v83\n\t"
        "v_mul_f32 v60, v31, v60\n\t"
        "v_mul_f32 v61, v31, v61\n\t"
        "v_mul_f32 v62, v31, v62\n\t"
        "v_mul_f32 v63, v31, v63\n\t"
        "v_mul_f32 v80, v31, v80\n\t"
        "v_mul_f32 v81, v31, v81\n\t"
        "v_mul_f32 v82, v31, v82\n\t"
        "v_mul_f32 v83, v31, v83\n\t"
        "v_cvt_pk_bf16_f32 v60, v60, v80\n\t"
        "v_cvt_pk_bf16_f32 v61, v61, v81\n\t"
        "v_cvt_pk_bf16_f32 v62, v62, v82\n\t"
        "v_cvt_pk_bf16_f32 v63, v63, v83\n\t"
        "v_fma_f32 v64, -v32, v36, v64\n\t"
        "v_fma_f32 v65, -v32, v37, v65\n\t"
        "v_fma_f32 v66, -v32, v38, v66\n\t"
        "v_fma_f32 v67, -v32, v39, v67\n\t"
        "v_fma_f32 v84, -v32, v40, v84\n\t"
        "v_fma_f32 v85, -v32, v41, v85\n\t"
        "v_fma_f32 v86, -v32, v42, v86\n\t"
        "v_fma_f32 v87, -v32, v43, v87\n\t"
        "v_mul_f32 v64, v33, v64\n\t"
        "v_mul_f32 v65, v33, v65\n\t"
        "v_mul_f32 v66, v33, v66\n\t"
        "v_mul_f32 v67, v33, v67\n\t"
        "v_mul_f32 v84, v33, v84\n\t"
        "v_mul_f32 v85, v33, v85\n\t"
        "v_mul_f32 v86, v33, v86\n\t"
        "v_mul_f32 v87, v33, v87\n\t"
        "v_cvt_pk_bf16_f32 v64, v64, v84\n\t"
        "v_cvt_pk_bf16_f32 v65, v65, v85\n\t"
        "v_cvt_pk_bf16_f32 v66, v66, v86\n\t"
        "v_cvt_pk_bf16_f32 v67, v67, v87\n\t"
        "Lu_%=:\n\t"
        "v_readfirstlane_b32 %[sf], v35\n\t"
        "s_cmp_ge_u32 %[sf], 7\n\t"
        "s_cbranch_scc1 Lv_%=\n\t"
        "s_cmp_lt_i32 %[scn], 1\n\t"
        "s_cbranch_scc1 Lv_%=\n\t"
        "s_sub_u32 %[scn], %[scn], 1\n\t"
        "s_sleep 4\n\t"
        "ds_read_b32 v35, %[x2] offset:%[omc]+4\n\t"
        "s_waitcnt lgkmcnt(0)\n\t"
        "s_branch Lu_%=\n\t"
        "Lv_%=:\n\t"
        "s_mov_b64 exec, %[sva]\n\t"
        "ds_write_b128 v68, v[52:55]\n\t"
        "ds_write_b128 v69, v[56:59]\n\t"
        "ds_write_b128 v70, v[60:63]\n\t"
        "ds_write_b128 v71, v[64:67]\n\t"
        "s_mov_b64 exec, %[sv0]\n\t"
        "ds_write_b128 v110, v[48:51]\n\t"
        "s_mov_b64 exec, -1\n\t"
        "s_branch Le_%=\n\t"
        "Ln_%=:\n\t"
        "s_cmp_lt_i32 %[scn], 1\n\t"
        "s_cbranch_scc1 Lo_%=\n\t"
        "s_sub_u32 %[scn], %[scn], 1\n\t"
        "buffer_load_dword %[fl], %[fo], %[ssrd], 0 offen sc1\n\t"
        "ds_read_b64 v[34:35], %[x2] offset:%[omc]\n\t"
        "ds_read_b64 v[24:25], %[x0] offset:0\n\t"
        "ds_read_b64 v[26:27], %[x0] offset:128\n\t"
        "ds_read_b64 v[28:29], %[x0] offset:256\n\t"
        "ds_read_b64 v[30:31], %[x0] offset:384\n\t"
        "ds_read_b64 v[32:33], %[x0] offset:512\n\t"
        "s_waitcnt vmcnt(0)\n\t"
        "s_branch Lm_%=\n\t"
        "Le_%=:\n\t"
        : [xa] "+v"(xa), [xb] "=&v"(xb), [si] "+s"(si), [fl] "=&v"(fl), [sf] "=&s"(sf), [scn] "+s"(cn), [x0] "+v"(xt[0]), [x1] "+v"(xt[1]), [x2] "+v"(xt[2])
        : [ssrd] "s"(srd), [iv] "v"(iv), [fo] "v"(fo), [sexp] "s"(fexp), [scs] "s"(cs), [sva] "s"(va), [sv0] "s"(v0), [sm16] "s"(0x0001000100010001ull), [omc] "n"(BC_MC)
        : "memory", "scc", "vcc", "v255", "v24", "v25", "v26", "v27", "v28", "v29", "v30", "v31", "v32", "v33", "v34", "v35", "v36", "v37", "v38", "v39", "v40", "v41", "v42", "v43", "v44", "v45", "v46", "v47");
}

DEV void gemm1a(u32x4 srd, u32 ho, u32 hi, u32 tag, int fb, u32 cs, u64 va, u64 v0, u32& sf, u32& cn) {
    int xa, ma; u32 one, fl;
    asm volatile(
        "v_add_u32 %[xa], 36864, v107\n\t"
        "v_mov_b32 %[one], 1\n\t"
        "v_lshrrev_b32 %[fl], 8, v107\n\t"
        "v_lshl_add_u32 %[fl], %[fl], 5, %[scs]\n\t"
        "ds_read_b128 v[48:51], %[fl] offset:0\n\t"
        "ds_read_b128 v[68:71], %[fl] offset:16\n\t"
        "v_and_b32 %[ma], 0xf0, v107\n\t"
        "v_lshrrev_b32 %[ma], 1, %[ma]\n\t"
        "v_add_u32 %[ma], 153344, %[ma]\n\t"
        "v_mov_b32 v81, %[stag]\n\t"
        "v_mov_b32 v86, %[stag]\n\t"
        "ds_read_b128 v[24:27], v107 offset:0\n\t"
        "ds_read_b128 v[28:31], v107 offset:1024\n\t"
        "ds_read_b128 v[32:35], v107 offset:2048\n\t"
        "ds_read_b128 v[36:39], v107 offset:3072\n\t"
        "ds_read_b32 %[fl], %[fb] offset:%[og0]\n\t"
        "ds_read_b128 v[40:43], v107 offset:4096\n\t"
        "ds_read_b128 v[44:47], v107 offset:5120\n\t"
        "s_setprio 3\n\t"
        "s_waitcnt lgkmcnt(6)\n\t"
        "v_mfma_f32_16x16x32_bf16 v[72:75], v[112:115], v[24:27], 0\n\t"
        "v_mfma_f32_16x16x32_bf16 v[76:79], v[116:119], v[24:27], 0\n\t"
        "ds_read_b128 v[24:27], v107 offset:6144\n\t"
        "s_waitcnt lgkmcnt(6)\n\t"
        "v_mfma_f32_16x16x32_bf16 v[72:75], v[120:123], v[28:31], v[72:75]\n\t"
        "v_mfma_f32_16x16x32_bf16 v[76:79], v[124:127], v[28:31], v[76:79]\n\t"
        "ds_read_b128 v[28:31], v107 offset:7168\n\t"
        "s_waitcnt lgkmcnt(6)\n\t"
        "v_mfma_f32_16x16x32_bf16 v[72:75], v[128:131], v[32:35], v[72:75]\n\t"
        "v_mfma_f32_16x16x32_bf16 v[76:79], v[132:135], v[32:35], v[76:79]\n\t"
        "ds_read_b128 v[32:35], v107 offset:8192\n\t"
        "s_waitcnt lgkmcnt(6)\n\t"
        "v_mfma_f32_16x16x32_bf16 v[72:75], v[136:139], v[36:39], v[72:75]\n\t"
        "v_mfma_f32_16x16x32_bf16 v[76:79], v[140:143], v[36:39], v[76:79]\n\t"
        "s_waitcnt lgkmcnt(5)\n\t"
        "Lc9_%=:\n\t"
        "v_readfirstlane_b32 %[sf], %[fl]\n\t"
        "s_cmp_lt_u32 %[sf], 2\n\t"
        "s_cbranch_scc1 Ls9_%=\n\t"
        "Lg9_%=:\n\t"
        "ds_read_b128 v[36:39], v107 offset:9216\n\t"
        "s_waitcnt lgkmcnt(5)\n\t"
        "v_mfma_f32_16x16x32_bf16 v[72:75], v[144:147], v[40:43], v[72:75]\n\t"
        "v_mfma_f32_16x16x32_bf16 v[76:79], v[148:151], v[40:43], v[76:79]\n\t"
        "ds_read_b128 v[40:43], v107 offset:10240\n\t"
        "s_waitcnt lgkmcnt(5)\n\t"
        "v_mfma_f32_16x16x32_bf16 v[72:75], v[152:155], v[44:47], v[72:75]\n\t"
        "v_mfma_f32_16x16x32_bf16 v[76:79], v[156:159], v[44:47], v[76:79]\n\t"
        "ds_read_b128 v[44:47], v107 offset:11264\n\t"
        "s_waitcnt lgkmcnt(5)\n\t"
        "v_mfma_f32_16x16x32_bf16 v[72:75], v[160:163], v[24:27], v[72:75]\n\t"
        "v_mfma_f32_16x16x32_bf16 v[76:79], v[164:167], v[24:27], v[76:79]\n\t"
        "ds_read_b128 v[24:27], v107 offset:12288\n\t"
        "s_waitcnt lgkmcnt(5)\n\t"
        "v_mfma_f32_16x16x32_bf16 v[72:75], v[168:171], v[28:31], v[72:75]\n\t"
        "v_mfma_f32_16x16x32_bf16 v[76:79], v[172:175], v[28:31], v[76:79]\n\t"
        "ds_read_b32 %[fl], %[fb] offset:%[og0]\n\t"
        "ds_read_b128 v[28:31], v107 offset:13312\n\t"
        "s_waitcnt lgkmcnt(6)\n\t"
        "v_mfma_f32_16x16x32_bf16 v[72:75], v[176:179], v[32:35], v[72:75]\n\t"
        "v_mfma_f32_16x16x32_bf16 v[76:79], v[180:183], v[32:35], v[76:79]\n\t"
        "ds_read_b128 v[32:35], v107 offset:14336\n\t"
        "s_waitcnt lgkmcnt(6)\n\t"
        "v_mfma_f32_16x16x32_bf16 v[72:75], v[184:187], v[36:39], v[72:75]\n\t"
        "v_mfma_f32_16x16x32_bf16 v[76:79], v[188:191], v[36:39], v[76:79]\n\t"
        "ds_read_b128 v[36:39], v107 offset:15360\n\t"
        "s_waitcnt lgkmcnt(6)\n\t"
        "v_mfma_f32_16x16x32_bf16 v[72:75], v[192:195], v[40:43], v[72:75]\n\t"
        "v_mfma_f32_16x16x32_bf16 v[76:79], v[196:199], v[40:43], v[76:79]\n\t"
        "ds_read_b128 v[40:43], v107 offset:16384\n\t"
        "s_waitcnt lgkmcnt(6)\n\t"
        "v_mfma_f32_16x16x32_bf16 v[72:75], v[200:203], v[44:47], v[72:75]\n\t"
        "v_mfma_f32_16x16x32_bf16 v[76:79], v[204:207], v[44:47], v[76:79]\n\t"
        "ds_read_b128 v[44:47], v107 offset:17408\n\t"
        "s_waitcnt lgkmcnt(6)\n\t"
        "v_mfma_f32_16x16x32_bf16 v[72:75], v[208:211], v[24:27], v[72:75]\n\t"
        "v_mfma_f32_16x16x32_bf16 v[76:79], v[212:215], v[24:27], v[76:79]\n\t"
        "s_waitcnt lgkmcnt(5)\n\t"
        "Lc18_%=:\n\t"
        "v_readfirstlane_b32 %[sf], %[fl]\n\t"
        "s_cmp_lt_u32 %[sf], 3\n\t"
        "s_cbranch_scc1 Ls18_%=\n\t"
        "Lg18_%=:\n\t"
        "ds_read_b128 v[24:27], v107 offset:18432\n\t"
        "s_waitcnt lgkmcnt(5)\n\t"
        "v_mfma_f32_16x16x32_bf16 v[72:75], v[216:219], v[28:31], v[72:75]\n\t"
        "v_mfma_f32_16x16x32_bf16 v[76:79], v[220:223], v[28:31], v[76:79]\n\t"
        "ds_read_b128 v[28:31], v107 offset:19456\n\t"
        "ds_read_b64 v[12:13], %[fb] offset:%[omc]\n\t"
        "ds_read_b64 v[14:15], %[ma] offset:512\n\t"
        "s_waitcnt lgkmcnt(7)\n\t"
        "v_mfma_f32_16x16x32_bf16 v[72:75], v[224:227], v[32:35], v[72:75]\n\t"
        "v_mfma_f32_16x16x32_bf16 v[76:79], v[228:231], v[32:35], v[76:79]\n\t"
        "ds_read_b128 v[32:35], v107 offset:20480\n\t"
        "s_waitcnt lgkmcnt(7)\n\t"
        "v_mfma_f32_16x16x32_bf16 v[72:75], v[232:235], v[36:39], v[72:75]\n\t"
        "v_mfma_f32_16x16x32_bf16 v[76:79], v[236:239], v[36:39], v[76:79]\n\t"
        "ds_read_b128 v[36:39], v107 offset:21504\n\t"
        "s_waitcnt lgkmcnt(7)\n\t"
        "v_mfma_f32_16x16x32_bf16 v[72:75], v[240:243], v[40:43], v[72:75]\n\t"
        "v_mfma_f32_16x16x32_bf16 v[76:79], v[244:247], v[40:43], v[76:79]\n\t"
        "ds_read_b32 %[fl], %[fb] offset:%[og0]\n\t"
        "ds_read_b128 v[40:43], v107 offset:22528\n\t"
        "s_waitcnt lgkmcnt(8)\n\t"
        "v_mfma_f32_16x16x32_bf16 v[72:75], v[248:251], v[44:47], v[72:75]\n\t"
        "v_mfma_f32_16x16x32_bf16 v[76:79], v[252:255], v[44:47], v[76:79]\n\t"
        "ds_read_b128 v[44:47], v107 offset:23552\n\t"
        "s_waitcnt lgkmcnt(8)\n\t"
        "v_mfma_f32_16x16x32_bf16 v[52:55], v[112:115], v[24:27], 0\n\t"
        "v_mfma_f32_16x16x32_bf16 v[56:59], v[116:119], v[24:27], 0\n\t"
        "ds_read_b128 v[24:27], v107 offset:24576\n\t"
        "s_waitcnt lgkmcnt(8)\n\t"
        "v_mfma_f32_16x16x32_bf16 v[52:55], v[120:123], v[28:31], v[52:55]\n\t"
        "s_waitcnt lgkmcnt(7)\n\t"
        "s_waitcnt lgkmcnt(6)\n\t"
        "Lm_%=:\n\t"
        "v_readfirstlane_b32 %[sf], v12\n\t"
        "s_cmp_lt_u32 %[sf], 4\n\t"
        "s_cbranch_scc1 Ln_%=\n\t"
        "Lo_%=:\n\t"
        "v_mfma_f32_16x16x32_bf16 v[56:59], v[124:127], v[28:31], v[56:59]\n\t"
        "ds_read_b128 v[28:31], v107 offset:25600\n\t"
        "s_waitcnt lgkmcnt(6)\n\t"
        "v_mfma_f32_16x16x32_bf16 v[52:55], v[128:131], v[32:35], v[52:55]\n\t"
        "v_fma_f32 v72, -v14, v48, v72\n\t"
        "v_fma_f32 v73, -v14, v49, v73\n\t"
        "v_fma_f32 v74, -v14, v50, v74\n\t"
        "v_fma_f32 v75, -v14, v51, v75\n\t"
        "v_fma_f32 v76, -v14, v68, v76\n\t"
        "v_fma_f32 v77, -v14, v69, v77\n\t"
        "v_mfma_f32_16x16x32_bf16 v[56:59], v[132:135], v[32:35], v[56:59]\n\t"
        "v_fma_f32 v78, -v14, v70, v78\n\t"
        "v_fma_f32 v79, -v14, v71, v79\n\t"
        "v_mul_f32 v72, v15, v72\n\t"
        "v_mul_f32 v73, v15, v73\n\t"
        "v_mul_f32 v74, v15, v74\n\t"
        "v_mul_f32 v75, v15, v75\n\t"
        "ds_read_b128 v[32:35], v107 offset:26624\n\t"
        "s_waitcnt lgkmcnt(6)\n\t"
        "v_mfma_f32_16x16x32_bf16 v[52:55], v[136:139], v[36:39], v[52:55]\n\t"
        "v_mul_f32 v76, v15, v76\n\t"
        "v_mul_f32 v77, v15, v77\n\t"
        "v_mul_f32 v78, v15, v78\n\t"
        "v_mul_f32 v79, v15, v79\n\t"
        "v_cvt_pk_bf16_f32 v82, v72, v76\n\t"
        "v_cvt_pk_bf16_f32 v83, v73, v77\n\t"
        "v_mfma_f32_16x16x32_bf16 v[56:59], v[140:143], v[36:39], v[56:59]\n\t"
        "v_cvt_pk_bf16_f32 v84, v74, v78\n\t"
        "v_cvt_pk_bf16_f32 v85, v75, v79\n\t"
        "s_cmp_eq_u64 %[sv0], 0\n\t"
        "s_cbranch_scc1 Lh0_%=\n\t"
        "buffer_store_dwordx4 v[80:83], v107, %[ssrd], %[sho] offen sc1\n\t"
        "buffer_store_dwordx4 v[84:87], v107, %[ssrd], %[sho] offen offset:1024 sc1\n\t"
        "Lh0_%=:\n\t"
        "s_waitcnt lgkmcnt(5)\n\t"
        "Lc27_%=:\n\t"
        "v_readfirstlane_b32 %[sf], %[fl]\n\t"
        "s_cmp_lt_u32 %[sf], 4\n\t"
        "s_cbranch_scc1 Ls27_%=\n\t"
        "Lg27_%=:\n\t"
        "ds_read_b128 v[36:39], v107 offset:27648\n\t"
        "s_waitcnt lgkmcnt(5)\n\t"
        "v_mfma_f32_16x16x32_bf16 v[52:55], v[144:147], v[40:43], v[52:55]\n\t"
        "v_mfma_f32_16x16x32_bf16 v[56:59], v[148:151], v[40:43], v[56:59]\n\t"
        "ds_read_b128 v[40:43], v107 offset:28672\n\t"
        "s_waitcnt lgkmcnt(5)\n\t"
        "v_mfma_f32_16x16x32_bf16 v[52:55], v[152:155], v[44:47], v[52:55]\n\t"
        "v_mfma_f32_16x16x32_bf16 v[56:59], v[156:159], v[44:47], v[56:59]\n\t"
        "ds_read_b128 v[44:47], v107 offset:29696\n\t"
        "s_setprio 2\n\t"
        "s_waitcnt lgkmcnt(5)\n\t"
        "v_mfma_f32_16x16x32_bf16 v[52:55], v[160:163], v[24:27], v[52:55]\n\t"
        "v_mfma_f32_16x16x32_bf16 v[56:59], v[164:167], v[24:27], v[56:59]\n\t"
        "ds_read_b128 v[24:27], v107 offset:30720\n\t"
        "s_waitcnt lgkmcnt(5)\n\t"
        "v_mfma_f32_16x16x32_bf16 v[52:55], v[168:171], v[28:31], v[52:55]\n\t"
        "v_mfma_f32_16x16x32_bf16 v[56:59], v[172:175], v[28:31], v[56:59]\n\t"
        "ds_read_b32 %[fl], %[fb] offset:%[og0]\n\t"
        "ds_read_b128 v[28:31], v107 offset:31744\n\t"
        "s_waitcnt lgkmcnt(6)\n\t"
        "v_mfma_f32_16x16x32_bf16 v[52:55], v[176:179], v[32:35], v[52:55]\n\t"
        "v_mfma_f32_16x16x32_bf16 v[56:59], v[180:183], v[32:35], v[56:59]\n\t"
        "ds_read_b128 v[32:35], v107 offset:32768\n\t"
        "s_waitcnt lgkmcnt(6)\n\t"
        "v_mfma_f32_16x16x32_bf16 v[52:55], v[184:187], v[36:39], v[52:55]\n\t"
        "v_mfma_f32_16x16x32_bf16 v[56:59], v[188:191], v[36:39], v[56:59]\n\t"
        "ds_read_b128 v[36:39], v107 offset:33792\n\t"
        "s_waitcnt lgkmcnt(6)\n\t"
        "v_mfma_f32_16x16x32_bf16 v[52:55], v[192:195], v[40:43], v[52:55]\n\t"
        "v_mfma_f32_16x16x32_bf16 v[56:59], v[196:199], v[40:43], v[56:59]\n\t"
        "ds_read_b128 v[40:43], v107 offset:34816\n\t"
        "s_waitcnt lgkmcnt(6)\n\t"
        "v_mfma_f32_16x16x32_bf16 v[52:55], v[200:203], v[44:47], v[52:55]\n\t"
        "v_mfma_f32_16x16x32_bf16 v[56:59], v[204:207], v[44:47], v[56:59]\n\t"
        "ds_read_b128 v[44:47], v107 offset:35840\n\t"
        "s_waitcnt lgkmcnt(6)\n\t"
        "v_mfma_f32_16x16x32_bf16 v[52:55], v[208:211], v[24:27], v[52:55]\n\t"
        "v_mfma_f32_16x16x32_bf16 v[56:59], v[212:215], v[24:27], v[56:59]\n\t"
        "s_waitcnt lgkmcnt(5)\n\t"
        "Lc36_%=:\n\t"
        "v_readfirstlane_b32 %[sf], %[fl]\n\t"
        "s_cmp_lt_u32 %[sf], 5\n\t"
        "s_cbranch_scc1 Ls36_%=\n\t"
        "Lg36_%=:\n\t"
        "s_mov_b64 exec, 1\n\t"
        "ds_add_u32 %[fb], %[one] offset:%[omc]+4\n\t"
        "s_mov_b64 exec, -1\n\t"
        "ds_read_b128 v[24:27], %[xa] offset:0\n\t"
        "s_waitcnt lgkmcnt(6)\n\t"
        "v_mfma_f32_16x16x32_bf16 v[52:55], v[216:219], v[28:31], v[52:55]\n\t"
        "v_mfma_f32_16x16x32_bf16 v[56:59], v[220:223], v[28:31], v[56:59]\n\t"
        "ds_read_b128 v[28:31], %[xa] offset:1024\n\t"
        "ds_read_b64 v[14:15], %[ma] offset:128\n\t"
        "s_waitcnt lgkmcnt(7)\n\t"
        "v_mfma_f32_16x16x32_bf16 v[52:55], v[224:227], v[32:35], v[52:55]\n\t"
        "v_mfma_f32_16x16x32_bf16 v[56:59], v[228:231], v[32:35], v[56:59]\n\t"
        "ds_read_b128 v[32:35], %[xa] offset:2048\n\t"
        "s_waitcnt lgkmcnt(7)\n\t"
        "v_mfma_f32_16x16x32_bf16 v[52:55], v[232:235], v[36:39], v[52:55]\n\t"
        "v_mfma_f32_16x16x32_bf16 v[56:59], v[236:239], v[36:39], v[56:59]\n\t"
        "ds_read_b128 v[36:39], %[xa] offset:3072\n\t"
        "s_waitcnt lgkmcnt(7)\n\t"
        "v_mfma_f32_16x16x32_bf16 v[52:55], v[240:243], v[40:43], v[52:55]\n\t"
        "v_mfma_f32_16x16x32_bf16 v[56:59], v[244:247], v[40:43], v[56:59]\n\t"
        "ds_read_b32 %[fl], %[fb] offset:%[og0]\n\t"
        "ds_read_b128 v[40:43], %[xa] offset:4096\n\t"
        "s_waitcnt lgkmcnt(8)\n\t"
        "v_mfma_f32_16x16x32_bf16 v[52:55], v[248:251], v[44:47], v[52:55]\n\t"
        "v_mfma_f32_16x16x32_bf16 v[56:59], v[252:255], v[44:47], v[56:59]\n\t"
        "ds_read_b128 v[44:47], %[xa] offset:5120\n\t"
        "s_waitcnt lgkmcnt(7)\n\t"
        "v_mfma_f32_16x16x32_bf16 v[72:75], v[112:115], v[24:27], 0\n\t"
        "v_mfma_f32_16x16x32_bf16 v[76:79], v[116:119], v[24:27], 0\n\t"
        "ds_read_b128 v[24:27], %[xa] offset:6144\n\t"
        "s_waitcnt lgkmcnt(7)\n\t"
        "v_mfma_f32_16x16x32_bf16 v[72:75], v[120:123], v[28:31], v[72:75]\n\t"
        "v_mfma_f32_16x16x32_bf16 v[76:79], v[124:127], v[28:31], v[76:79]\n\t"
        "s_waitcnt lgkmcnt(6)\n\t"
        "ds_read_b128 v[28:31], %[xa] offset:7168\n\t"
        "s_waitcnt lgkmcnt(6)\n\t"
        "v_mfma_f32_16x16x32_bf16 v[72:75], v[128:131], v[32:35], v[72:75]\n\t"
        "v_fma_f32 v52, -v14, v48, v52\n\t"
        "v_fma_f32 v53, -v14, v49, v53\n\t"
        "v_mfma_f32_16x16x32_bf16 v[76:79], v[132:135], v[32:35], v[76:79]\n\t"
        "v_fma_f32 v54, -v14, v50, v54\n\t"
        "v_fma_f32 v55, -v14, v51, v55\n\t"
        "ds_read_b128 v[32:35], %[xa] offset:8192\n\t"
        "s_waitcnt lgkmcnt(6)\n\t"
        "v_mfma_f32_16x16x32_bf16 v[72:75], v[136:139], v[36:39], v[72:75]\n\t"
        "v_fma_f32 v56, -v14, v68, v56\n\t"
        "v_fma_f32 v57, -v14, v69, v57\n\t"
        "v_mfma_f32_16x16x32_bf16 v[76:79], v[140:143], v[36:39], v[76:79]\n\t"
        "v_fma_f32 v58, -v14, v70, v58\n\t"
        "v_fma_f32 v59, -v14, v71, v59\n\t"
        "s_waitcnt lgkmcnt(5)\n\t"
        "Lc45_%=:\n\t"
        "v_readfirstlane_b32 %[sf], %[fl]\n\t"
        "s_cmp_lt_u32 %[sf], 6\n\t"
        "s_cbranch_scc1 Ls45_%=\n\t"
        "Lg45_%=:\n\t"
        "ds_read_b128 v[36:39], %[xa] offset:9216\n\t"
        "s_waitcnt lgkmcnt(5)\n\t"
        "v_mfma_f32_16x16x32_bf16 v[72:75], v[144:147], v[40:43], v[72:75]\n\t"
        "v_mul_f32 v52, v15, v52\n\t"
        "v_mul_f32 v53, v15, v53\n\t"
        "v_mfma_f32_16x16x32_bf16 v[76:79], v[148:151], v[40:43], v[76:79]\n\t"
        "v_mul_f32 v54, v15, v54\n\t"
        "v_mul_f32 v55, v15, v55\n\t"
        "ds_read_b128 v[40:43], %[xa] offset:10240\n\t"
        "s_waitcnt lgkmcnt(5)\n\t"
        "v_mfma_f32_16x16x32_bf16 v[72:75], v[152:155], v[44:47], v[72:75]\n\t"
        "v_mul_f32 v56, v15, v56\n\t"
        "v_mul_f32 v57, v15, v57\n\t"
        "v_mfma_f32_16x16x32_bf16 v[76:79], v[156:159], v[44:47], v[76:79]\n\t"
        "v_mul_f32 v58, v15, v58\n\t"
        "v_mul_f32 v59, v15, v59\n\t"
        "ds_read_b128 v[44:47], %[xa] offset:11264\n\t"
        "s_waitcnt lgkmcnt(5)\n\t"
        "v_mfma_f32_16x16x32_bf16 v[72:75], v[160:163], v[24:27], v[72:75]\n\t"
        "v_cvt_pk_bf16_f32 v60, v52, v56\n\t"
        "v_cvt_pk_bf16_f32 v61, v53, v57\n\t"
        "v_mfma_f32_16x16x32_bf16 v[76:79], v[164:167], v[24:27], v[76:79]\n\t"
        "v_cvt_pk_bf16_f32 v62, v54, v58\n\t"
        "v_cvt_pk_bf16_f32 v63, v55, v59\n\t"
        "ds_read_b128 v[24:27], %[xa] offset:12288\n\t"
        "s_waitcnt lgkmcnt(5)\n\t"
        "v_mfma_f32_16x16x32_bf16 v[72:75], v[168:171], v[28:31], v[72:75]\n\t"
        "v_mfma_f32_16x16x32_bf16 v[76:79], v[172:175], v[28:31], v[76:79]\n\t"
        "ds_read_b32 %[fl], %[fb] offset:%[og0]\n\t"
        "ds_read_b128 v[28:31], %[xa] offset:13312\n\t"
        "s_waitcnt lgkmcnt(6)\n\t"
        "v_mfma_f32_16x16x32_bf16 v[72:75], v[176:179], v[32:35], v[72:75]\n\t"
        "v_mfma_f32_16x16x32_bf16 v[76:79], v[180:183], v[32:35], v[76:79]\n\t"
        "ds_read_b128 v[32:35], %[xa] offset:14336\n\t"
        "s_waitcnt lgkmcnt(6)\n\t"
        "v_mfma_f32_16x16x32_bf16 v[72:75], v[184:187], v[36:39], v[72:75]\n\t"
        "v_mfma_f32_16x16x32_bf16 v[76:79], v[188:191], v[36:39], v[76:79]\n\t"
        "ds_read_b128 v[36:39], %[xa] offset:15360\n\t"
        "s_waitcnt lgkmcnt(6)\n\t"
        "v_mfma_f32_16x16x32_bf16 v[72:75], v[192:195], v[40:43], v[72:75]\n\t"
        "v_mfma_f32_16x16x32_bf16 v[76:79], v[196:199], v[40:43], v[76:79]\n\t"
        "ds_read_b128 v[40:43], %[xa] offset:16384\n\t"
        "s_waitcnt lgkmcnt(6)\n\t"
        "v_mfma_f32_16x16x32_bf16 v[72:75], v[200:203], v[44:47], v[72:75]\n\t"
        "v_mfma_f32_16x16x32_bf16 v[76:79], v[204:207], v[44:47], v[76:79]\n\t"
        "ds_read_b128 v[44:47], %[xa] offset:17408\n\t"
        "s_setprio 1\n\t"
        "s_waitcnt lgkmcnt(6)\n\t"
        "v_mfma_f32_16x16x32_bf16 v[72:75], v[208:211], v[24:27], v[72:75]\n\t"
        "v_mfma_f32_16x16x32_bf16 v[76:79], v[212:215], v[24:27], v[76:79]\n\t"
        "s_waitcnt lgkmcnt(5)\n\t"
        "Lc54_%=:\n\t"
        "v_readfirstlane_b32 %[sf], %[fl]\n\t"
        "s_cmp_lt_u32 %[sf], 7\n\t"
        "s_cbranch_scc1 Ls54_%=\n\t"
        "Lg54_%=:\n\t"
        "s_mov_b64 exec, 1\n\t"
        "ds_add_u32 %[fb], %[one] offset:%[oc2]\n\t"
        "s_mov_b64 exec, -1\n\t"
        "ds_read_b128 v[24:27], %[xa] offset:18432\n\t"
        "s_waitcnt lgkmcnt(6)\n\t"
        "v_mfma_f32_16x16x32_bf16 v[72:75], v[216:219], v[28:31], v[72:75]\n\t"
        "v_mfma_f32_16x16x32_bf16 v[76:79], v[220:223], v[28:31], v[76:79]\n\t"
        "ds_read_b128 v[28:31], %[xa] offset:19456\n\t"
        "ds_read_b64 v[14:15], %[ma] offset:256\n\t"
        "s_waitcnt lgkmcnt(7)\n\t"
        "v_mfma_f32_16x16x32_bf16 v[72:75], v[224:227], v[32:35], v[72:75]\n\t"
        "v_mfma_f32_16x16x32_bf16 v[76:79], v[228:231], v[32:35], v[76:79]\n\t"
        "ds_read_b128 v[32:35], %[xa] offset:20480\n\t"
        "s_waitcnt lgkmcnt(7)\n\t"
        "v_mfma_f32_16x16x32_bf16 v[72:75], v[232:235], v[36:39], v[72:75]\n\t"
        "v_mfma_f32_16x16x32_bf16 v[76:79], v[236:239], v[36:39], v[76:79]\n\t"
        "ds_read_b128 v[36:39], %[xa] offset:21504\n\t"
        "s_waitcnt lgkmcnt(7)\n\t"
        "v_mfma_f32_16x16x32_bf16 v[72:75], v[240:243], v[40:43], v[72:75]\n\t"
        "v_mfma_f32_16x16x32_bf16 v[76:79], v[244:247], v[40:43], v[76:79]\n\t"
        "ds_read_b32 %[fl], %[fb] offset:%[og0]\n\t"
        "ds_read_b128 v[40:43], %[xa] offset:22528\n\t"
        "s_waitcnt lgkmcnt(8)\n\t"
        "v_mfma_f32_16x16x32_bf16 v[72:75], v[248:251], v[44:47], v[72:75]\n\t"
        "v_mfma_f32_16x16x32_bf16 v[76:79], v[252:255], v[44:47], v[76:79]\n\t"
        "ds_read_b128 v[44:47], %[xa] offset:23552\n\t"
        "s_waitcnt lgkmcnt(7)\n\t"
        "v_mfma_f32_16x16x32_bf16 v[52:55], v[112:115], v[24:27], 0\n\t"
        "v_mfma_f32_16x16x32_bf16 v[56:59], v[116:119], v[24:27], 0\n\t"
        "ds_read_b128 v[24:27], %[xa] offset:24576\n\t"
        "s_waitcnt lgkmcnt(7)\n\t"
        "v_mfma_f32_16x16x32_bf16 v[52:55], v[120:123], v[28:31], v[52:55]\n\t"
        "v_mfma_f32_16x16x32_bf16 v[56:59], v[124:127], v[28:31], v[56:59]\n\t"
        "s_waitcnt lgkmcnt(6)\n\t"
        "ds_read_b128 v[28:31], %[xa] offset:25600\n\t"
        "s_waitcnt lgkmcnt(6)\n\t"
        "v_mfma_f32_16x16x32_bf16 v[52:55], v[128:131], v[32:35], v[52:55]\n\t"
        "v_fma_f32 v72, -v14, v48, v72\n\t"
        "v_fma_f32 v73, -v14, v49, v73\n\t"
        "v_mfma_f32_16x16x32_bf16 v[56:59], v[132:135], v[32:35], v[56:59]\n\t"
        "v_fma_f32 v74, -v14, v50, v74\n\t"
        "v_fma_f32 v75, -v14, v51, v75\n\t"
        "ds_read_b128 v[32:35], %[xa] offset:26624\n\t"
        "s_waitcnt lgkmcnt(6)\n\t"
        "v_mfma_f32_16x16x32_bf16 v[52:55], v[136:139], v[36:39], v[52:55]\n\t"
        "v_fma_f32 v76, -v14, v68, v76\n\t"
        "v_fma_f32 v77, -v14, v69, v77\n\t"
        "v_mfma_f32_16x16x32_bf16 v[56:59], v[140:143], v[36:39], v[56:59]\n\t"
        "v_fma_f32 v78, -v14, v70, v78\n\t"
        "v_fma_f32 v79, -v14, v71, v79\n\t"
        "s_waitcnt lgkmcnt(5)\n\t"
        "Lc63_%=:\n\t"
        "v_readfirstlane_b32 %[sf], %[fl]\n\t"
        "s_cmp_lt_u32 %[sf], 8\n\t"
        "s_cbranch_scc1 Ls63_%=\n\t"
        "Lg63_%=:\n\t"
        "ds_read_b128 v[36:39], %[xa] offset:27648\n\t"
        "s_waitcnt lgkmcnt(5)\n\t"
        "v_mfma_f32_16x16x32_bf16 v[52:55], v[144:147], v[40:43], v[52:55]\n\t"
        "v_mul_f32 v72, v15, v72\n\t"
        "v_mul_f32 v73, v15, v73\n\t"
        "v_mfma_f32_16x16x32_bf16 v[56:59], v[148:151], v[40:43], v[56:59]\n\t"
        "v_mul_f32 v74, v15, v74\n\t"
        "v_mul_f32 v75, v15, v75\n\t"
        "ds_read_b32 v13, %[fb] offset:%[omc]+4\n\t"
        "s_cmp_eq_u64 %[sv0], 0\n\t"
        "s_cbranch_scc1 Lh1_%=\n\t"
        "buffer_load_dwordx4 v[16:19], v107, %[ssrd], %[shi] offen sc1\n\t"
        "buffer_load_dwordx4 v[20:23], v107, %[ssrd], %[shi] offen offset:1024 sc1\n\t"
        "Lh1_%=:\n\t"
        "ds_read_b128 v[40:43], %[xa] offset:28672\n\t"
        "s_waitcnt lgkmcnt(6)\n\t"
        "v_mfma_f32_16x16x32_bf16 v[52:55], v[152:155], v[44:47], v[52:55]\n\t"
        "v_mul_f32 v76, v15, v76\n\t"
        "v_mul_f32 v77, v15, v77\n\t"
        "v_mfma_f32_16x16x32_bf16 v[56:59], v[156:159], v[44:47], v[56:59]\n\t"
        "v_mul_f32 v78, v15, v78\n\t"
        "v_mul_f32 v79, v15, v79\n\t"
        "ds_read_b128 v[44:47], %[xa] offset:29696\n\t"
        "s_waitcnt lgkmcnt(6)\n\t"
        "v_mfma_f32_16x16x32_bf16 v[52:55], v[160:163], v[24:27], v[52:55]\n\t"
        "v_cvt_pk_bf16_f32 v64, v72, v76\n\t"
        "v_cvt_pk_bf16_f32 v65, v73, v77\n\t"
        "v_mfma_f32_16x16x32_bf16 v[56:59], v[164:167], v[24:27], v[56:59]\n\t"
        "v_cvt_pk_bf16_f32 v66, v74, v78\n\t"
        "v_cvt_pk_bf16_f32 v67, v75, v79\n\t"
        "ds_read_b128 v[24:27], %[xa] offset:30720\n\t"
        "s_waitcnt lgkmcnt(6)\n\t"
        "v_mfma_f32_16x16x32_bf16 v[52:55], v[168:171], v[28:31], v[52:55]\n\t"
        "v_add_u32 v72, v110, v111\n\t"
        "v_add_u32 v73, v72, v111\n\t"
        "v_mfma_f32_16x16x32_bf16 v[56:59], v[172:175], v[28:31], v[56:59]\n\t"
        "v_add_u32 v74, v73, v111\n\t"
        "v_add_u32 v75, v74, v111\n\t"
        "ds_read_b128 v[28:31], %[xa] offset:31744\n\t"
        "s_setprio 0\n\t"
        "s_waitcnt lgkmcnt(6)\n\t"
        "v_mfma_f32_16x16x32_bf16 v[52:55], v[176:179], v[32:35], v[52:55]\n\t"
        "v_mfma_f32_16x16x32_bf16 v[56:59], v[180:183], v[32:35], v[56:59]\n\t"
        "ds_read_b128 v[32:35], %[xa] offset:32768\n\t"
        "s_waitcnt lgkmcnt(6)\n\t"
        "v_mfma_f32_16x16x32_bf16 v[52:55], v[184:187], v[36:39], v[52:55]\n\t"
        "v_mfma_f32_16x16x32_bf16 v[56:59], v[188:191], v[36:39], v[56:59]\n\t"
        "s_waitcnt lgkmcnt(5)\n\t"
        "Lu_%=:\n\t"
        "v_readfirstlane_b32 %[sf], v13\n\t"
        "s_cmp_lt_u32 %[sf], 14\n\t"
        "s_cbranch_scc1 Lw_%=\n\t"
        "Lv_%=:\n\t"
        "s_mov_b64 exec, %[sva]\n\t"
        "ds_write_b128 v75, v[82:85]\n\t"
        "ds_write_b128 v72, v[60:63]\n\t"
        "ds_write_b128 v73, v[64:67]\n\t"
        "s_mov_b64 exec, -1\n\t"
        "ds_read_b128 v[36:39], %[xa] offset:33792\n\t"
        "s_waitcnt lgkmcnt(8)\n\t"
        "v_mfma_f32_16x16x32_bf16 v[52:55], v[192:195], v[40:43], v[52:55]\n\t"
        "v_mfma_f32_16x16x32_bf16 v[56:59], v[196:199], v[40:43], v[56:59]\n\t"
        "ds_read_b128 v[40:43], %[xa] offset:34816\n\t"
        "s_waitcnt lgkmcnt(8)\n\t"
        "v_mfma_f32_16x16x32_bf16 v[52:55], v[200:203], v[44:47], v[52:55]\n\t"
        "v_mfma_f32_16x16x32_bf16 v[56:59], v[204:207], v[44:47], v[56:59]\n\t"
        "ds_read_b128 v[44:47], %[xa] offset:35840\n\t"
        "s_waitcnt lgkmcnt(8)\n\t"
        "v_mfma_f32_16x16x32_bf16 v[52:55], v[208:211], v[24:27], v[52:55]\n\t"
        "v_mfma_f32_16x16x32_bf16 v[56:59], v[212:215], v[24:27], v[56:59]\n\t"
        "s_mov_b64 exec, 1\n\t"
        "ds_add_u32 %[fb], %[one] offset:%[oc1]\n\t"
        "s_mov_b64 exec, -1\n\t"
        "s_waitcnt lgkmcnt(8)\n\t"
        "v_mfma_f32_16x16x32_bf16 v[52:55], v[216:219], v[28:31], v[52:55]\n\t"
        "v_mfma_f32_16x16x32_bf16 v[56:59], v[220:223], v[28:31], v[56:59]\n\t"
        "ds_read_b64 v[14:15], %[ma] offset:384\n\t"
        "s_waitcnt lgkmcnt(8)\n\t"
        "v_mfma_f32_16x16x32_bf16 v[52:55], v[224:227], v[32:35], v[52:55]\n\t"
        "v_mfma_f32_16x16x32_bf16 v[56:59], v[228:231], v[32:35], v[56:59]\n\t"
        "s_waitcnt lgkmcnt(4)\n\t"
        "v_mfma_f32_16x16x32_bf16 v[52:55], v[232:235], v[36:39], v[52:55]\n\t"
        "v_mfma_f32_16x16x32_bf16 v[56:59], v[236:239], v[36:39], v[56:59]\n\t"
        "ds_read_b32 v12, %[fb] offset:%[ogo]\n\t"
        "s_waitcnt lgkmcnt(4)\n\t"
        "v_mfma_f32_16x16x32_bf16 v[52:55], v[240:243], v[40:43], v[52:55]\n\t"
        "v_mfma_f32_16x16x32_bf16 v[56:59], v[244:247], v[40:43], v[56:59]\n\t"
        "s_waitcnt lgkmcnt(3)\n\t"
        "v_mfma_f32_16x16x32_bf16 v[52:55], v[248:251], v[44:47], v[52:55]\n\t"
        "v_mfma_f32_16x16x32_bf16 v[56:59], v[252:255], v[44:47], v[56:59]\n\t"
        "s_waitcnt lgkmcnt(0)\n\t"
        "v_readfirstlane_b32 %[sf], v12\n\t"
        "s_nop 6\n\t"
        "v_fma_f32 v52, -v14, v48, v52\n\t"
        "v_fma_f32 v53, -v14, v49, v53\n\t"
        "v_fma_f32 v54, -v14, v50, v54\n\t"
        "v_fma_f32 v55, -v14, v51, v55\n\t"
        "v_fma_f32 v56, -v14, v68, v56\n\t"
        "v_fma_f32 v57, -v14, v69, v57\n\t"
        "v_fma_f32 v58, -v14, v70, v58\n\t"
        "v_fma_f32 v59, -v14, v71, v59\n\t"
        "v_mul_f32 v52, v15, v52\n\t"
        "v_mul_f32 v53, v15, v53\n\t"
        "v_mul_f32 v54, v15, v54\n\t"
        "v_mul_f32 v55, v15, v55\n\t"
        "v_mul_f32 v56, v15, v56\n\t"
        "v_mul_f32 v57, v15, v57\n\t"
        "v_mul_f32 v58, v15, v58\n\t"
        "v_mul_f32 v59, v15, v59\n\t"
        "v_cvt_pk_bf16_f32 v52, v52, v56\n\t"
        "v_cvt_pk_bf16_f32 v53, v53, v57\n\t"
        "v_cvt_pk_bf16_f32 v54, v54, v58\n\t"
        "v_cvt_pk_bf16_f32 v55, v55, v59\n\t"
        "s_mov_b64 exec, %[sva]\n\t"
        "ds_write_b128 v74, v[52:55]\n\t"
        "s_mov_b64 exec, -1\n\t"
        "s_cmp_eq_u64 %[sv0], 0\n\t"
        "s_cbranch_scc1 Lhe_%=\n\t"
        "Lhw_%=:\n\t"
        "s_waitcnt vmcnt(0)\n\t"
        "v_xor_b32 v16, %[stag], v17\n\t"
        "v_xor_b32 v23, %[stag], v22\n\t"
        "v_or_b32 v16, v16, v23\n\t"
        "v_cmp_ne_u32 vcc, 0, v16\n\t"
        "s_cbranch_vccnz Lhr_%=\n\t"
        "Lhg_%=:\n\t"
        "s_mov_b64 exec, %[sv0]\n\t"
        "ds_write_b128 v110, v[18:21]\n\t"
        "s_mov_b64 exec, -1\n\t"
        "Lhe_%=:\n\t"
        "s_branch Le_%=\n\t"
        "Lhr_%=:\n\t"
        "s_cmp_lt_i32 %[scn], 1\n\t"
        "s_cbranch_scc1 Lhg_%=\n\t"
        "s_sub_u32 %[scn], %[scn], 16\n\t"
        "buffer_load_dwordx4 v[16:19], v107, %[ssrd], %[shi] offen sc1\n\t"
        "buffer_load_dwordx4 v[20:23], v107, %[ssrd], %[shi] offen offset:1024 sc1\n\t"
        "s_waitcnt vmcnt(0)\n\t"
        "s_branch Lhw_%=\n\t"
        "Lw_%=:\n\t"
        "s_cmp_lt_i32 %[scn], 1\n\t"
        "s_cbranch_scc1 Lv_%=\n\t"
        "s_sub_u32 %[scn], %[scn], 1\n\t"
        "s_sleep 2\n\t"
        "ds_read_b32 v13, %[fb] offset:%[omc]+4\n\t"
        "s_waitcnt lgkmcnt(0)\n\t"
        "s_branch Lu_%=\n\t"
        "Ls9_%=:\n\t"
        "s_cmp_lt_i32 %[scn], 1\n\t"
        "s_cbranch_scc1 Lg9_%=\n\t"
        "s_sub_u32 %[scn], %[scn], 1\n\t"
        "s_sleep 1\n\t"
        "ds_read_b32 %[fl], %[fb] offset:%[og0]\n\t"
        "s_waitcnt lgkmcnt(0)\n\t"
        "s_branch Lc9_%=\n\t"
        "Ls18_%=:\n\t"
        "s_cmp_lt_i32 %[scn], 1\n\t"
        "s_cbranch_scc1 Lg18_%=\n\t"
        "s_sub_u32 %[scn], %[scn], 1\n\t"
        "s_sleep 1\n\t"
        "ds_read_b32 %[fl], %[fb] offset:%[og0]\n\t"
        "s_waitcnt lgkmcnt(0)\n\t"
        "s_branch Lc18_%=\n\t"
        "Ls27_%=:\n\t"
        "s_cmp_lt_i32 %[scn], 1\n\t"
        "s_cbranch_scc1 Lg27_%=\n\t"
        "s_sub_u32 %[scn], %[scn], 1\n\t"
        "s_sleep 1\n\t"
        "ds_read_b32 %[fl], %[fb] offset:%[og0]\n\t"
        "s_waitcnt lgkmcnt(0)\n\t"
        "s_branch Lc27_%=\n\t"
        "Ls36_%=:\n\t"
        "s_cmp_lt_i32 %[scn], 1\n\t"
        "s_cbranch_scc1 Lg36_%=\n\t"
        "s_sub_u32 %[scn], %[scn], 1\n\t"
        "s_sleep 1\n\t"
        "ds_read_b32 %[fl], %[fb] offset:%[og0]\n\t"
        "s_waitcnt lgkmcnt(0)\n\t"
        "s_branch Lc36_%=\n\t"
        "Ls45_%=:\n\t"
        "s_cmp_lt_i32 %[scn], 1\n\t"
        "s_cbranch_scc1 Lg45_%=\n\t"
        "s_sub_u32 %[scn], %[scn], 1\n\t"
        "s_sleep 1\n\t"
        "ds_read_b32 %[fl], %[fb] offset:%[og0]\n\t"
        "s_waitcnt lgkmcnt(0)\n\t"
        "s_branch Lc45_%=\n\t"
        "Ls54_%=:\n\t"
        "s_cmp_lt_i32 %[scn], 1\n\t"
        "s_cbranch_scc1 Lg54_%=\n\t"
        "s_sub_u32 %[scn], %[scn], 1\n\t"
        "s_sleep 1\n\t"
        "ds_read_b32 %[fl], %[fb] offset:%[og0]\n\t"
        "s_waitcnt lgkmcnt(0)\n\t"
        "s_branch Lc54_%=\n\t"
        "Ls63_%=:\n\t"
        "s_cmp_lt_i32 %[scn], 1\n\t"
        "s_cbranch_scc1 Lg63_%=\n\t"
        "s_sub_u32 %[scn], %[scn], 1\n\t"
        "s_sleep 1\n\t"
        "ds_read_b32 %[fl], %[fb] offset:%[og0]\n\t"
        "s_waitcnt lgkmcnt(0)\n\t"
        "s_branch Lc63_%=\n\t"
        "Ln_%=:\n\t"
        "s_cmp_lt_i32 %[scn], 1\n\t"
        "s_cbranch_scc1 Lo_%=\n\t"
        "s_sub_u32 %[scn], %[scn], 1\n\t"
        "s_sleep 1\n\t"
        "ds_read_b64 v[12:13], %[fb] offset:%[omc]\n\t"
        "ds_read_b64 v[14:15], %[ma] offset:512\n\t"
        "s_waitcnt lgkmcnt(0)\n\t"
        "s_branch Lm_%=\n\t"
        "Le_%=:\n\t"
        : [xa] "=&v"(xa), [ma] "=&v"(ma), [one] "=&v"(one), [fl] "=&v"(fl), [sf] "=&s"(sf), [scn] "+s"(cn)
        : [ssrd] "s"(srd), [sho] "s"(ho), [shi] "s"(hi), [stag] "s"(tag), [fb] "v"(fb), [scs] "s"(cs), [sva] "s"(va), [sv0] "s"(v0), [ogo] "n"(BC_GO), [omc] "n"(BC_MC), [oc1] "n"(BC_C1), [oc2] "n"(BC_C2), [og0] "n"(BC_G0)
        : "memory", "scc", "vcc", "v12", "v13", "v14", "v15", "v16", "v17", "v18", "v19", "v20", "v21", "v22", "v23", "v24", "v25", "v26", "v27", "v28", "v29", "v30", "v31", "v32", "v33", "v34", "v35", "v36", "v37", "v38", "v39", "v40", "v41", "v42", "v43", "v44", "v45", "v46", "v47");
}

DEV void gather(u32x4 srd, u32 base) {
    u32 so, a, t;
    asm volatile(
        "s_mov_b32 m0, 0\n\t"
        "s_add_u32 %[so], %[sb], 55296\n\t"
        "v_mov_b32 %[a], 153984\n\t"
        "s_nop 0\n\t"
        "buffer_load_dwordx4 v107, %[ssrd], %[so] offen offset:0 sc1 lds\n\t"
        "buffer_load_dwordx4 v107, %[ssrd], %[so] offen offset:1024 sc1 lds\n\t"
        "buffer_load_dwordx4 v107, %[ssrd], %[so] offen offset:2048 sc1 lds\n\t"
        "buffer_load_dwordx4 v107, %[ssrd], %[so] offen offset:3072 sc1 lds\n\t"
        "s_mov_b32 m0, 4096\n\t"
        "s_add_u32 %[so], %[so], 4096\n\t"
        "buffer_load_dwordx4 v107, %[ssrd], %[so] offen offset:0 sc1 lds\n\t"
        "buffer_load_dwordx4 v107, %[ssrd], %[so] offen offset:1024 sc1 lds\n\t"
        "buffer_load_dwordx4 v107, %[ssrd], %[so] offen offset:2048 sc1 lds\n\t"
        "buffer_load_dwordx4 v107, %[ssrd], %[so] offen offset:3072 sc1 lds\n\t"
        "s_mov_b32 m0, 8192\n\t"
        "s_add_u32 %[so], %[so], 4096\n\t"
        "buffer_load_dwordx4 v107, %[ssrd], %[so] offen offset:0 sc1 lds\n\t"
        "buffer_load_dwordx4 v107, %[ssrd], %[so] offen offset:1024 sc1 lds\n\t"
        "buffer_load_dwordx4 v107, %[ssrd], %[so] offen offset:2048 sc1 lds\n\t"
        "buffer_load_dwordx4 v107, %[ssrd], %[so] offen offset:3072 sc1 lds\n\t"
        "s_mov_b32 m0, 12288\n\t"
        "s_add_u32 %[so], %[so], 4096\n\t"
        "buffer_load_dwordx4 v107, %[ssrd], %[so] offen offset:0 sc1 lds\n\t"
        "buffer_load_dwordx4 v107, %[ssrd], %[so] offen offset:1024 sc1 lds\n\t"
        "buffer_load_dwordx4 v107, %[ssrd], %[so] offen offset:2048 sc1 lds\n\t"
        "buffer_load_dwordx4 v107, %[ssrd], %[so] offen offset:3072 sc1 lds\n\t"
        "s_mov_b32 m0, 16384\n\t"
        "s_add_u32 %[so], %[so], 4096\n\t"
        "buffer_load_dwordx4 v107, %[ssrd], %[so] offen offset:0 sc1 lds\n\t"
        "buffer_load_dwordx4 v107, %[ssrd], %[so] offen offset:1024 sc1 lds\n\t"
        "s_mov_b32 m0, 18432\n\t"
        "s_mov_b32 %[so], %[sb]\n\t"
        "buffer_load_dwordx4 v107, %[ssrd], %[so] offen offset:0 sc1 lds\n\t"
        "buffer_load_dwordx4 v107, %[ssrd], %[so] offen offset:1024 sc1 lds\n\t"
        "buffer_load_dwordx4 v107, %[ssrd], %[so] offen offset:2048 sc1 lds\n\t"
        "buffer_load_dwordx4 v107, %[ssrd], %[so] offen offset:3072 sc1 lds\n\t"
        "s_mov_b32 m0, 22528\n\t"
        "s_add_u32 %[so], %[so], 4096\n\t"
        "buffer_load_dwordx4 v107, %[ssrd], %[so] offen offset:0 sc1 lds\n\t"
        "buffer_load_dwordx4 v107, %[ssrd], %[so] offen offset:1024 sc1 lds\n\t"
        "buffer_load_dwordx4 v107, %[ssrd], %[so] offen offset:2048 sc1 lds\n\t"
        "buffer_load_dwordx4 v107, %[ssrd], %[so] offen offset:3072 sc1 lds\n\t"
        "s_mov_b32 m0, 26624\n\t"
        "s_add_u32 %[so], %[so], 4096\n\t"
        "buffer_load_dwordx4 v107, %[ssrd], %[so] offen offset:0 sc1 lds\n\t"
        "buffer_load_dwordx4 v107, %[ssrd], %[so] offen offset:1024 sc1 lds\n\t"
        "buffer_load_dwordx4 v107, %[ssrd], %[so] offen offset:2048 sc1 lds\n\t"
        "buffer_load_dwordx4 v107, %[ssrd], %[so] offen offset:3072 sc1 lds\n\t"
        "s_mov_b32 m0, 30720\n\t"
        "s_add_u32 %[so], %[so], 4096\n\t"
        "buffer_load_dwordx4 v107, %[ssrd], %[so] offen offset:0 sc1 lds\n\t"
        "buffer_load_dwordx4 v107, %[ssrd], %[so] offen offset:1024 sc1 lds\n\t"
        "buffer_load_dwordx4 v107, %[ssrd], %[so] offen offset:2048 sc1 lds\n\t"
        "buffer_load_dwordx4 v107, %[ssrd], %[so] offen offset:3072 sc1 lds\n\t"
        "s_mov_b32 m0, 34816\n\t"
        "s_add_u32 %[so], %[so], 4096\n\t"
        "buffer_load_dwordx4 v107, %[ssrd], %[so] offen offset:0 sc1 lds\n\t"
        "buffer_load_dwordx4 v107, %[ssrd], %[so] offen offset:1024 sc1 lds\n\t"
        "v_mov_b32 %[t], 1\n\t"
        "s_waitcnt vmcnt(27)\n\t"
        "ds_write_b32 %[a], %[t] offset:%[og0]\n\t"
        "buffer_load_dwordx4 v107, %[ssrd], %[so] offen offset:2048 sc1 lds\n\t"
        "buffer_load_dwordx4 v107, %[ssrd], %[so] offen offset:3072 sc1 lds\n\t"
        "s_mov_b32 m0, 38912\n\t"
        "s_add_u32 %[so], %[so], 4096\n\t"
        "buffer_load_dwordx4 v107, %[ssrd], %[so] offen offset:0 sc1 lds\n\t"
        "buffer_load_dwordx4 v107, %[ssrd], %[so] offen offset:1024 sc1 lds\n\t"
        "buffer_load_dwordx4 v107, %[ssrd], %[so] offen offset:2048 sc1 lds\n\t"
        "buffer_load_dwordx4 v107, %[ssrd], %[so] offen offset:3072 sc1 lds\n\t"
        "s_mov_b32 m0, 43008\n\t"
        "s_add_u32 %[so], %[so], 4096\n\t"
        "buffer_load_dwordx4 v107, %[ssrd], %[so] offen offset:0 sc1 lds\n\t"
        "buffer_load_dwordx4 v107, %[ssrd], %[so] offen offset:1024 sc1 lds\n\t"
        "buffer_load_dwordx4 v107, %[ssrd], %[so] offen offset:2048 sc1 lds\n\t"
        "v_mov_b32 %[t], 2\n\t"
        "s_waitcnt vmcnt(27)\n\t"
        "ds_write_b32 %[a], %[t] offset:%[og0]\n\t"
        "buffer_load_dwordx4 v107, %[ssrd], %[so] offen offset:3072 sc1 lds\n\t"
        "s_mov_b32 m0, 47104\n\t"
        "s_add_u32 %[so], %[so], 4096\n\t"
        "buffer_load_dwordx4 v107, %[ssrd], %[so] offen offset:0 sc1 lds\n\t"
        "buffer_load_dwordx4 v107, %[ssrd], %[so] offen offset:1024 sc1 lds\n\t"
        "buffer_load_dwordx4 v107, %[ssrd], %[so] offen offset:2048 sc1 lds\n\t"
        "buffer_load_dwordx4 v107, %[ssrd], %[so] offen offset:3072 sc1 lds\n\t"
        "s_mov_b32 m0, 51200\n\t"
        "s_add_u32 %[so], %[so], 4096\n\t"
        "buffer_load_dwordx4 v107, %[ssrd], %[so] offen offset:0 sc1 lds\n\t"
        "buffer_load_dwordx4 v107, %[ssrd], %[so] offen offset:1024 sc1 lds\n\t"
        "buffer_load_dwordx4 v107, %[ssrd], %[so] offen offset:2048 sc1 lds\n\t"
        "buffer_load_dwordx4 v107, %[ssrd], %[so] offen offset:3072 sc1 lds\n\t"
        "v_mov_b32 %[t], 3\n\t"
        "s_waitcnt vmcnt(27)\n\t"
        "ds_write_b32 %[a], %[t] offset:%[og0]\n\t"
        "s_mov_b32 m0, 55296\n\t"
        "s_add_u32 %[so], %[so], 4096\n\t"
        "buffer_load_dwordx4 v107, %[ssrd], %[so] offen offset:0 sc1 lds\n\t"
        "buffer_load_dwordx4 v107, %[ssrd], %[so] offen offset:1024 sc1 lds\n\t"
        "buffer_load_dwordx4 v107, %[ssrd], %[so] offen offset:2048 sc1 lds\n\t"
        "buffer_load_dwordx4 v107, %[ssrd], %[so] offen offset:3072 sc1 lds\n\t"
        "s_mov_b32 m0, 59392\n\t"
        "s_add_u32 %[so], %[so], 4096\n\t"
        "buffer_load_dwordx4 v107, %[ssrd], %[so] offen offset:0 sc1 lds\n\t"
        "buffer_load_dwordx4 v107, %[ssrd], %[so] offen offset:1024 sc1 lds\n\t"
        "buffer_load_dwordx4 v107, %[ssrd], %[so] offen offset:2048 sc1 lds\n\t"
        "buffer_load_dwordx4 v107, %[ssrd], %[so] offen offset:3072 sc1 lds\n\t"
        "s_mov_b32 m0, 63488\n\t"
        "s_add_u32 %[so], %[so], 4096\n\t"
        "buffer_load_dwordx4 v107, %[ssrd], %[so] offen offset:0 sc1 lds\n\t"
        "v_mov_b32 %[t], 4\n\t"
        "s_waitcnt vmcnt(27)\n\t"
        "ds_write_b32 %[a], %[t] offset:%[og0]\n\t"
        "buffer_load_dwordx4 v107, %[ssrd], %[so] offen offset:1024 sc1 lds\n\t"
        "buffer_load_dwordx4 v107, %[ssrd], %[so] offen offset:2048 sc1 lds\n\t"
        "buffer_load_dwordx4 v107, %[ssrd], %[so] offen offset:3072 sc1 lds\n\t"
        "s_mov_b32 m0, 67584\n\t"
        "s_add_u32 %[so], %[so], 4096\n\t"
        "buffer_load_dwordx4 v107, %[ssrd], %[so] offen offset:0 sc1 lds\n\t"
        "buffer_load_dwordx4 v107, %[ssrd], %[so] offen offset:1024 sc1 lds\n\t"
        "buffer_load_dwordx4 v107, %[ssrd], %[so] offen offset:2048 sc1 lds\n\t"
        "buffer_load_dwordx4 v107, %[ssrd], %[so] offen offset:3072 sc1 lds\n\t"
        "s_mov_b32 m0, 71680\n\t"
        "s_add_u32 %[so], %[so], 4096\n\t"
        "buffer_load_dwordx4 v107, %[ssrd], %[so] offen offset:0 sc1 lds\n\t"
        "buffer_load_dwordx4 v107, %[ssrd], %[so] offen offset:1024 sc1 lds\n\t"
        "v_mov_b32 %[t], 5\n\t"
        "s_waitcnt vmcnt(27)\n\t"
        "ds_write_b32 %[a], %[t] offset:%[og0]\n\t"
        "v_mov_b32 %[t], 6\n\t"
        "s_waitcnt vmcnt(18)\n\t"
        "ds_write_b32 %[a], %[t] offset:%[og0]\n\t"
        "v_mov_b32 %[t], 7\n\t"
        "s_waitcnt vmcnt(9)\n\t"
        "ds_write_b32 %[a], %[t] offset:%[og0]\n\t"
        "v_mov_b32 %[t], 8\n\t"
        "s_waitcnt vmcnt(0)\n\t"
        "ds_write_b32 %[a], %[t] offset:%[og0]\n\t"
        : [so] "=&s"(so), [a] "=&v"(a), [t] "=&v"(t) : [ssrd] "s"(srd), [sb] "s"(base), [og0] "n"(BC_G0) : "memory", "scc");
}

DEV void gemm1b(int fb, int xr, int xq, u32 w1, u64 va, u64 v0, u32& cn) {
    u32 sf, st; u64 mw, mx;
    asm volatile(
        "v_lshrrev_b32 v36, 8, v107\n\t"
        "v_mov_b32 v37, %[lxr]\n\t"
        "v_lshl_add_u32 v36, v36, 4, v37\n\t"
        "v_lshrrev_b32 v37, 4, v111\n\t"
        "Lw_%=:\n\t"
        "ds_read_b32 v34, %[fb] offset:%[ogo]\n\t"
        "s_sub_u32 %[scn], %[scn], 1\n\t"
        "s_waitcnt lgkmcnt(0)\n\t"
        "v_readfirstlane_b32 %[sf], v34\n\t"
        "s_cmp_eq_u32 %[sf], 1\n\t"
        "s_cbranch_scc1 Le_%=\n\t"
        "s_cmp_lt_i32 %[scn], 1\n\t"
        "s_cbranch_scc1 Le_%=\n\t"
        "s_cmp_lg_u32 %[sf], 0\n\t"
        "s_cbranch_scc1 Lg_%=\n\t"
        "s_sleep 1\n\t"
        "s_branch Lw_%=\n\t"
        "Lg_%=:\n\t"
        "ds_read_b128 v[52:55], v36 offset:0\n\t"
        "ds_read_b128 v[56:59], v36 offset:64\n\t"
        "ds_read_b128 v[60:63], v36 offset:128\n\t"
        "ds_read_b128 v[64:67], v36 offset:192\n\t"
        "ds_read_b128 v[72:75], v36 offset:256\n\t"
        "ds_read_b128 v[76:79], v36 offset:320\n\t"
        "ds_read_b128 v[80:83], v36 offset:384\n\t"
        "ds_read_b128 v[84:87], v36 offset:448\n\t"
        "s_lshr_b32 %[sf], %[sf], 8\n\t"
        "s_and_b32 %[st], %[sf], 15\n\t"
        "s_lshl_b64 %[smx], %[sm16], %[st]\n\t"
        "s_cmp_ge_u32 %[sf], 16\n\t"
        "s_cselect_b64 %[smw], %[sva], %[sv0]\n\t"
        "s_lshr_b32 %[st], %[sf], 4\n\t"
        "s_cmp_eq_u32 %[st], %[sw1]\n\t"
        "s_cselect_b64 %[smx], %[smx], 0\n\t"
        "s_and_b64 %[smw], %[smw], %[sm16]\n\t"
        "s_cbranch_scc0 Le_%=\n\t"
        "s_waitcnt lgkmcnt(7)\n\t"
        "v_mfma_f32_16x16x32_bf16 v[24:27], v[112:115], v[52:55], 0\n\t"
        "v_mfma_f32_16x16x32_bf16 v[28:31], v[116:119], v[52:55], 0\n\t"
        "ds_read_b128 v[52:55], v36 offset:512\n\t"
        "s_waitcnt lgkmcnt(7)\n\t"
        "v_mfma_f32_16x16x32_bf16 v[24:27], v[120:123], v[56:59], v[24:27]\n\t"
        "v_mfma_f32_16x16x32_bf16 v[28:31], v[124:127], v[56:59], v[28:31]\n\t"
        "ds_read_b128 v[56:59], v36 offset:576\n\t"
        "v_mad_u32_u24 v37, v37, %[sf], v110\n\t"
        "s_waitcnt lgkmcnt(7)\n\t"
        "v_mfma_f32_16x16x32_bf16 v[24:27], v[128:131], v[60:63], v[24:27]\n\t"
        "v_mfma_f32_16x16x32_bf16 v[28:31], v[132:135], v[60:63], v[28:31]\n\t"
        "ds_read_b128 v[60:63], v36 offset:640\n\t"
        "s_waitcnt lgkmcnt(7)\n\t"
        "v_mfma_f32_16x16x32_bf16 v[24:27], v[136:139], v[64:67], v[24:27]\n\t"
        "v_mfma_f32_16x16x32_bf16 v[28:31], v[140:143], v[64:67], v[28:31]\n\t"
        "ds_read_b128 v[64:67], v36 offset:704\n\t"
        "s_waitcnt lgkmcnt(7)\n\t"
        "v_mfma_f32_16x16x32_bf16 v[24:27], v[144:147], v[72:75], v[24:27]\n\t"
        "v_mfma_f32_16x16x32_bf16 v[28:31], v[148:151], v[72:75], v[28:31]\n\t"
        "ds_read_b128 v[72:75], v36 offset:768\n\t"
        "s_waitcnt lgkmcnt(7)\n\t"
        "v_mfma_f32_16x16x32_bf16 v[24:27], v[152:155], v[76:79], v[24:27]\n\t"
        "v_mfma_f32_16x16x32_bf16 v[28:31], v[156:159], v[76:79], v[28:31]\n\t"
        "ds_read_b128 v[76:79], v36 offset:832\n\t"
        "s_waitcnt lgkmcnt(7)\n\t"
        "v_mfma_f32_16x16x32_bf16 v[24:27], v[160:163], v[80:83], v[24:27]\n\t"
        "v_mfma_f32_16x16x32_bf16 v[28:31], v[164:167], v[80:83], v[28:31]\n\t"
        "ds_read_b128 v[80:83], v36 offset:896\n\t"
        "s_waitcnt lgkmcnt(7)\n\t"
        "v_mfma_f32_16x16x32_bf16 v[24:27], v[168:171], v[84:87], v[24:27]\n\t"
        "v_mfma_f32_16x16x32_bf16 v[28:31], v[172:175], v[84:87], v[28:31]\n\t"
        "ds_read_b128 v[84:87], v36 offset:960\n\t"
        "s_waitcnt lgkmcnt(7)\n\t"
        "v_mfma_f32_16x16x32_bf16 v[24:27], v[176:179], v[52:55], v[24:27]\n\t"
        "v_mfma_f32_16x16x32_bf16 v[28:31], v[180:183], v[52:55], v[28:31]\n\t"
        "ds_read_b128 v[52:55], v36 offset:1024\n\t"
        "s_waitcnt lgkmcnt(7)\n\t"
        "v_mfma_f32_16x16x32_bf16 v[24:27], v[184:187], v[56:59], v[24:27]\n\t"
        "v_mfma_f32_16x16x32_bf16 v[28:31], v[188:191], v[56:59], v[28:31]\n\t"
        "ds_read_b128 v[56:59], v36 offset:1088\n\t"
        "s_waitcnt lgkmcnt(7)\n\t"
        "v_mfma_f32_16x16x32_bf16 v[24:27], v[192:195], v[60:63], v[24:27]\n\t"
        "v_mfma_f32_16x16x32_bf16 v[28:31], v[196:199], v[60:63], v[28:31]\n\t"
        "s_waitcnt lgkmcnt(6)\n\t"
        "v_mfma_f32_16x16x32_bf16 v[24:27], v[200:203], v[64:67], v[24:27]\n\t"
        "v_mfma_f32_16x16x32_bf16 v[28:31], v[204:207], v[64:67], v[28:31]\n\t"
        "s_waitcnt lgkmcnt(5)\n\t"
        "v_mfma_f32_16x16x32_bf16 v[24:27], v[208:211], v[72:75], v[24:27]\n\t"
        "v_mfma_f32_16x16x32_bf16 v[28:31], v[212:215], v[72:75], v[28:31]\n\t"
        "ds_read_b32 v34, %[fb] offset:%[ops]\n\t"
        "ds_read_b64 v[32:33], %[fb] offset:%[opm]\n\t"
        "s_waitcnt lgkmcnt(6)\n\t"
        "v_mfma_f32_16x16x32_bf16 v[24:27], v[216:219], v[76:79], v[24:27]\n\t"
        "v_mfma_f32_16x16x32_bf16 v[28:31], v[220:223], v[76:79], v[28:31]\n\t"
        "s_waitcnt lgkmcnt(5)\n\t"
        "v_mfma_f32_16x16x32_bf16 v[24:27], v[224:227], v[80:83], v[24:27]\n\t"
        "v_mfma_f32_16x16x32_bf16 v[28:31], v[228:231], v[80:83], v[28:31]\n\t"
        "s_waitcnt lgkmcnt(4)\n\t"
        "v_mfma_f32_16x16x32_bf16 v[24:27], v[232:235], v[84:87], v[24:27]\n\t"
        "v_mfma_f32_16x16x32_bf16 v[28:31], v[236:239], v[84:87], v[28:31]\n\t"
        "s_waitcnt lgkmcnt(3)\n\t"
        "v_mfma_f32_16x16x32_bf16 v[24:27], v[240:243], v[52:55], v[24:27]\n\t"
        "v_mfma_f32_16x16x32_bf16 v[28:31], v[244:247], v[52:55], v[28:31]\n\t"
        "s_waitcnt lgkmcnt(2)\n\t"
        "v_mfma_f32_16x16x32_bf16 v[24:27], v[248:251], v[56:59], v[24:27]\n\t"
        "v_mfma_f32_16x16x32_bf16 v[28:31], v[252:255], v[56:59], v[28:31]\n\t"
        "Lm_%=:\n\t"
        "s_waitcnt lgkmcnt(0)\n\t"
        "v_readfirstlane_b32 %[st], v34\n\t"
        "s_cmp_ge_u32 %[st], %[sf]\n\t"
        "s_cbranch_scc1 Lo_%=\n\t"
        "s_cmp_lt_i32 %[scn], 1\n\t"
        "s_cbranch_scc1 Lo_%=\n\t"
        "s_sub_u32 %[scn], %[scn], 1\n\t"
        "ds_read_b32 v34, %[fb] offset:%[ops]\n\t"
        "ds_read_b64 v[32:33], %[fb] offset:%[opm]\n\t"
        "s_branch Lm_%=\n\t"
        "Lo_%=:\n\t"
        "s_mov_b64 exec, %[smx]\n\t"
        "ds_read_b128 v[88:91], %[xr]\n\t"
        "ds_read_b128 v[92:95], %[xr] offset:64\n\t"
        "ds_read_b64 v[96:97], %[xq]\n\t"
        "s_mov_b64 exec, -1\n\t"
        "v_fma_f32 v24, -v32, v48, v24\n\t"
        "v_fma_f32 v25, -v32, v49, v25\n\t"
        "v_fma_f32 v26, -v32, v50, v26\n\t"
        "v_fma_f32 v27, -v32, v51, v27\n\t"
        "v_fma_f32 v28, -v32, v68, v28\n\t"
        "v_fma_f32 v29, -v32, v69, v29\n\t"
        "v_fma_f32 v30, -v32, v70, v30\n\t"
        "v_fma_f32 v31, -v32, v71, v31\n\t"
        "v_mul_f32 v24, v33, v24\n\t"
        "v_mul_f32 v25, v33, v25\n\t"
        "v_mul_f32 v26, v33, v26\n\t"
        "v_mul_f32 v27, v33, v27\n\t"
        "v_mul_f32 v28, v33, v28\n\t"
        "v_mul_f32 v29, v33, v29\n\t"
        "v_mul_f32 v30, v33, v30\n\t"
        "v_mul_f32 v31, v33, v31\n\t"
        "v_cvt_pk_bf16_f32 v24, v24, v28\n\t"
        "v_cvt_pk_bf16_f32 v25, v25, v29\n\t"
        "v_cvt_pk_bf16_f32 v26, v26, v30\n\t"
        "v_cvt_pk_bf16_f32 v27, v27, v31\n\t"
        "s_mov_b64 exec, %[smw]\n\t"
        "ds_write_b128 v37, v[24:27]\n\t"
        "s_mov_b64 exec, -1\n\t"
        "Le_%=:\n\t"
        "s_waitcnt lgkmcnt(0)\n\t"
        : [sf] "=&s"(sf), [st] "=&s"(st), [smw] "=&s"(mw), [smx] "=&s"(mx), [scn] "+s"(cn)
        : [fb] "v"(fb), [xr] "v"(xr), [xq] "v"(xq), [sw1] "s"(w1), [sva] "s"(va), [sv0] "s"(v0), [sm16] "s"(0x0001000100010001ull), [ogo] "n"(BC_GO), [ops] "n"(BC_PST),
          [opm] "n"(BC_PM), [lxr] "n"(L_XR)
        : "memory", "scc", "v24", "v25", "v26", "v27", "v28", "v29", "v30", "v31", "v32", "v33", "v34", "v35", "v36", "v37");
}

DEV void mid(const MidS& a, u32 tag, u64 gb, u32 gl, u32 wt, u32& cn) {
    u32 ob4, ob4x, cd;
    asm volatile(
        "s_mov_b32 m0, %[sldx]\n\t"
        "s_nop 3\n\t"
        "s_waitcnt lgkmcnt(0)\n\t"
        "s_barrier\n\t"
        "ds_read_b64 v[36:37], v102 offset:0\n\t"
        "ds_read_b32 v38, v103 offset:0\n\t"
        "ds_read_b64 v[20:21], v100 offset:0\n\t"
        "ds_read_b32 v22, v101 offset:0\n\t"
        "ds_read_b64 v[24:25], v100 offset:4864\n\t"
        "ds_read_b32 v26, v101 offset:4864\n\t"
        "ds_read_b64 v[40:41], v102 offset:40\n\t"
        "ds_read_b32 v42, v103 offset:4\n\t"
        "ds_read_b64 v[28:29], v100 offset:40\n\t"
        "ds_read_b32 v30, v101 offset:4\n\t"
        "ds_read_b64 v[32:33], v100 offset:4904\n\t"
        "ds_read_b32 v34, v101 offset:4868\n\t"
        "ds_read_b64_tr_b16 v[52:53], v105\n\t"
        "ds_read_b64_tr_b16 v[54:55], v105 offset:4864\n\t"
        "s_bitcmp1_b32 %[sskip], 0\n\t"
        "s_cbranch_scc1 Le0_%=\n\t"
        "buffer_load_dwordx4 v107, %[ssrd], %[swx] offen sc1 lds\n\t"
        "buffer_load_dwordx4 v107, %[ssrd], %[swx] offen offset:1024 sc1 lds\n\t"
        "Le0_%=:\n\t"
        "s_mov_b32 s92, 0xaaaaaaaa\n\t"
        "s_mov_b32 s93, 0xaaaaaaaa\n\t"
        "s_mov_b32 s94, 0xcccccccc\n\t"
        "s_mov_b32 s95, 0xcccccccc\n\t"
        "s_mov_b32 s96, 0xfff0ffff\n\t"
        "s_mov_b32 s97, 0xf000ff00\n\t"
        "s_mov_b32 s98, 0xffc0fffc\n\t"
        "s_mov_b32 s99, 0xc000fc00\n\t"
        "v_mov_b32 v23, 0\n\t"
        "v_mov_b32 v27, 0\n\t"
        "v_mov_b32 v31, 0\n\t"
        "v_mov_b32 v35, 0\n\t"
        "v_mov_b32 v39, 0\n\t"
        "v_mov_b32 v43, 0\n\t"
        "v_mov_b32 v60, %[sc0]\n\t"
        "v_mov_b32 v61, %[sc0]\n\t"
        "v_mov_b32 v62, %[sc0]\n\t"
        "v_mov_b32 v63, %[sc0]\n\t"
        "v_mov_b32 v68, %[sc0]\n\t"
        "v_mov_b32 v69, %[sc0]\n\t"
        "v_mov_b32 v70, %[sc0]\n\t"
        "v_mov_b32 v71, %[sc0]\n\t"
        "s_waitcnt lgkmcnt(10)\n\t"
        "v_mfma_f32_16x16x32_bf16 v[60:63], v[20:23], v[36:39], v[60:63]\n\t"
        "ds_read_b64_tr_b16 v[44:45], v104\n\t"
        "ds_read_b64_tr_b16 v[46:47], v104 offset:4864\n\t"
        "s_waitcnt lgkmcnt(10)\n\t"
        "v_mfma_f32_16x16x32_bf16 v[64:67], v[24:27], v[36:39], 0\n\t"
        "ds_read_b64_tr_b16 v[48:49], v104 offset:40\n\t"
        "ds_read_b64_tr_b16 v[50:51], v104 offset:4904\n\t"
        "s_waitcnt lgkmcnt(8)\n\t"
        "v_mfma_f32_16x16x32_bf16 v[68:71], v[28:31], v[40:43], v[68:71]\n\t"
        "s_waitcnt lgkmcnt(6)\n\t"
        "v_mfma_f32_16x16x32_bf16 v[72:75], v[32:35], v[40:43], 0\n\t"
        "s_cbranch_scc1 Le1_%=\n\t"
        "buffer_load_dwordx4 v107, %[ssrd], %[swx] offen offset:2048 sc1 lds\n\t"
        "buffer_load_dwordx4 v107, %[ssrd], %[swx] offen offset:3072 sc1 lds\n\t"
        "Le1_%=:\n\t"
        "s_andn2_b32 %[sc], 0x100, %[smsk]\n\t"
        "v_lshrrev_b32 v16, 4, v107\n\t"
        "s_add_u32 %[sc], %[sc], 162560\n\t"
        "v_and_b32 v16, 0x30, v16\n\t"
        "v_mov_b32 v8, %[sc]\n\t"
        "v_add_u32 v6, %[sc], v16\n\t"
        "v_add_u32 v12, 155168, v107\n\t"
        "ds_read_b64 v[4:5], v6 offset:8\n\t"
        "ds_read_b64 v[6:7], v6 offset:80\n\t"
        "ds_read_b96 v[8:10], v8 offset:64\n\t"
        "ds_read_b96 v[12:14], v12\n\t"
        "v_mov_b32 v56, 0x0c0c0100\n\t"
        "v_mov_b32 v57, 0x01000c0c\n\t"
        "v_mov_b32 v58, 0x0c0c0c0c\n\t"
        "v_cndmask_b32 v56, v56, v57, s[92:93]\n\t"
        "v_add_u32 v57, 0x0202, v56\n\t"
        "v_add_u32 v59, 0x02020000, v56\n\t"
        "v_cndmask_b32 v57, v57, v59, s[92:93]\n\t"
        "v_cndmask_b32 v76, v56, v58, s[96:97]\n\t"
        "v_cndmask_b32 v78, v58, v56, s[96:97]\n\t"
        "v_cndmask_b32 v77, v57, v58, s[98:99]\n\t"
        "v_cndmask_b32 v79, v58, v57, s[98:99]\n\t"
        "v_mov_b32 v17, 0x3f803f80\n\t"
        "v_cndmask_b32 v28, v60, v61, s[92:93]\n\t"
        "v_cndmask_b32 v36, v68, v69, s[92:93]\n\t"
        "v_cndmask_b32 v29, v62, v63, s[92:93]\n\t"
        "v_cndmask_b32 v37, v70, v71, s[92:93]\n\t"
        "v_cndmask_b32 v30, v64, v65, s[92:93]\n\t"
        "v_cndmask_b32 v38, v72, v73, s[92:93]\n\t"
        "v_cndmask_b32 v31, v66, v67, s[92:93]\n\t"
        "v_cndmask_b32 v39, v74, v75, s[92:93]\n\t"
        "v_cndmask_b32 v28, v28, v30, s[96:97]\n\t"
        "v_cndmask_b32 v36, v36, v38, s[96:97]\n\t"
        "v_cndmask_b32 v29, v29, v31, s[98:99]\n\t"
        "v_cndmask_b32 v37, v37, v39, s[98:99]\n\t"
        "v_max_f32 v30, v28, v29\n\t"
        "v_max_f32 v38, v36, v37\n\t"
        "v_max_f32 v31, v28, v29\n\t"
        "v_max_f32 v39, v36, v37\n\t"
        "s_waitcnt lgkmcnt(8)\n\t"
        "v_permlane16_swap_b32 v30, v31\n\t"
        "v_permlane16_swap_b32 v38, v39\n\t"
        "v_max_f32 v32, v30, v31\n\t"
        "v_max_f32 v40, v38, v39\n\t"
        "v_max_f32 v33, v30, v31\n\t"
        "v_max_f32 v41, v38, v39\n\t"
        "v_cndmask_b32 v56, v17, v52, s[94:95]\n\t"
        "v_permlane32_swap_b32 v32, v33\n\t"
        "v_permlane32_swap_b32 v40, v41\n\t"
        "v_max_f32 v30, v32, v33\n\t"
        "v_max_f32 v38, v40, v41\n\t"
        "v_sub_f32 v28, v28, v30\n\t"
        "v_sub_f32 v36, v36, v38\n\t"
        "v_sub_f32 v29, v29, v30\n\t"
        "v_sub_f32 v37, v37, v38\n\t"
        "v_mul_f32 v28, 0x3eae1a93, v28\n\t"
        "v_mul_f32 v36, 0x3eae1a93, v36\n\t"
        "v_mul_f32 v29, 0x3eae1a93, v29\n\t"
        "v_mul_f32 v37, 0x3eae1a93, v37\n\t"
        "v_exp_f32 v28, v28\n\t"
        "v_exp_f32 v36, v36\n\t"
        "v_exp_f32 v29, v29\n\t"
        "v_exp_f32 v37, v37\n\t"
        "v_cndmask_b32 v57, v17, v53, s[94:95]\n\t"
        "v_cndmask_b32 v58, v17, v54, s[94:95]\n\t"
        "v_cndmask_b32 v59, v17, v55, s[94:95]\n\t"
        "v_cndmask_b32 v52, v52, v17, s[94:95]\n\t"
        "v_cndmask_b32 v53, v53, v17, s[94:95]\n\t"
        "v_cndmask_b32 v54, v54, v17, s[94:95]\n\t"
        "v_cndmask_b32 v55, v55, v17, s[94:95]\n\t"
        "v_cvt_pk_bf16_f32 v28, v28, v29\n\t"
        "v_cvt_pk_bf16_f32 v36, v36, v37\n\t"
        "v_perm_b32 v20, v28, v28, v76\n\t"
        "v_perm_b32 v21, v28, v28, v77\n\t"
        "v_perm_b32 v22, v28, v28, v78\n\t"
        "v_perm_b32 v23, v28, v28, v79\n\t"
        "v_perm_b32 v24, v36, v36, v76\n\t"
        "v_perm_b32 v25, v36, v36, v77\n\t"
        "v_perm_b32 v26, v36, v36, v78\n\t"
        "v_perm_b32 v27, v36, v36, v79\n\t"
        "s_waitcnt lgkmcnt(0)\n\t"
        "v_mfma_f32_16x16x32_bf16 v[64:67], v[52:55], v[20:23], 0\n\t"
        "v_mfma_f32_16x16x32_bf16 v[60:63], v[44:47], v[20:23], 0\n\t"
        "v_mfma_f32_16x16x32_bf16 v[72:75], v[56:59], v[24:27], 0\n\t"
        "v_mfma_f32_16x16x32_bf16 v[68:71], v[48:51], v[24:27], 0\n\t"
        "v_add_u32 v16, 163232, v16\n\t"
        "v_mov_b32 v39, 163424\n\t"
        "s_mov_b32 vcc_lo, 0x22222222\n\t"
        "s_mov_b32 vcc_hi, 0x22222222\n\t"
        "v_cndmask_b32 v16, v39, v16, vcc\n\t"
        "ds_read_b128 v[28:31], v16 offset:0\n\t"
        "ds_read_b128 v[32:35], v16 offset:64\n\t"
        "ds_read_b128 v[40:43], v16 offset:128\n\t"
        "ds_read_b32 v39, v39 offset:144\n\t"
        "v_mov_b32 v86, 0\n\t"
        "v_mov_b32 v87, 0\n\t"
        "v_rcp_f32 v36, v66\n\t"
        "s_nop 0\n\t"
        "v_mul_f32 v60, v60, v36\n\t"
        "v_mul_f32 v61, v61, v36\n\t"
        "v_mul_f32 v62, v62, v36\n\t"
        "v_mul_f32 v63, v63, v36\n\t"
        "v_rcp_f32 v37, v72\n\t"
        "v_cvt_pk_bf16_f32 v80, v60, v61\n\t"
        "v_cvt_pk_bf16_f32 v81, v62, v63\n\t"
        "v_mul_f32 v68, v68, v37\n\t"
        "v_mul_f32 v69, v69, v37\n\t"
        "v_mul_f32 v70, v70, v37\n\t"
        "v_mul_f32 v71, v71, v37\n\t"
        "v_min_u32 v80, -2, v80\n\t"
        "v_min_u32 v81, -2, v81\n\t"
        "v_cvt_pk_bf16_f32 v82, v68, v69\n\t"
        "v_cvt_pk_bf16_f32 v83, v70, v71\n\t"
        "v_mul_f32 v64, v64, v36\n\t"
        "v_mul_f32 v65, v65, v36\n\t"
        "v_min_u32 v82, -2, v82\n\t"
        "v_min_u32 v83, -2, v83\n\t"
        "buffer_store_dwordx4 v[80:83], v107, %[ssrd], %[sst] offen sc1\n\t"
        "v_mul_f32 v74, v74, v37\n\t"
        "v_mul_f32 v75, v75, v37\n\t"
        "v_mfma_f32_16x16x32_bf16 v[52:55], v[80:83], v[80:83], 0\n\t"
        "v_cvt_pk_bf16_f32 v84, v64, v65\n\t"
        "v_cvt_pk_bf16_f32 v85, v74, v75\n\t"
        "v_mfma_f32_16x16x32_bf16 v[44:47], v[4:7], v[80:83], 0\n\t"
        "v_min_u32 v84, -2, v84\n\t"
        "v_min_u32 v85, -2, v85\n\t"
        "v_mov_b32 v11, 0\n\t"
        "s_cmp_eq_u32 %[smsk], 0\n\t"
        "v_mov_b32 v98, 0\n\t"
        "v_mov_b32 v99, 0\n\t"
        "v_mfma_f32_16x16x32_bf16 v[44:47], v[8:11], v[84:87], v[44:47]\n\t"
        "v_cndmask_b32 v52, v52, v53, s[92:93]\n\t"
        "v_cndmask_b32 v54, v54, v55, s[92:93]\n\t"
        "s_waitcnt lgkmcnt(0)\n\t"
        "s_cbranch_scc1 Lf1_%=\n\t"
        "v_mfma_f32_16x16x32_bf16 v[56:59], v[28:31], v[88:91], 0\n\t"
        "v_mfma_f32_16x16x32_bf16 v[56:59], v[32:35], v[92:95], v[56:59]\n\t"
        "v_mfma_f32_16x16x32_bf16 v[56:59], v[40:43], v[96:99], v[56:59]\n\t"
        "v_cndmask_b32 v52, v52, v54, s[94:95]\n\t"
        "v_and_b32 v17, 0xf0, v107\n\t"
        "v_mul_f32 v52, v52, v10\n\t"
        "s_mov_b32 exec_lo, 0xf0000f\n\t"
        "s_mov_b32 exec_hi, 0xf0000f00\n\t"
        "v_min_u32 v83, -2, v52\n\t"
        "s_nop 1\n\t"
        "v_fma_f32 v56, -v39, v56, v57\n\t"
        "v_add_f32 v44, v44, v56\n\t"
        "v_min_u32 v82, -2, v44\n\t"
        "buffer_store_dwordx4 v[82:85], v17, %[ssrd], %[stt] offen sc1\n\t"
        "s_branch Lf2_%=\n\t"
        "Lf1_%=:\n\t"
        "v_cndmask_b32 v52, v52, v54, s[94:95]\n\t"
        "v_and_b32 v17, 0xf0, v107\n\t"
        "v_mul_f32 v52, v52, v10\n\t"
        "s_mov_b32 exec_lo, 0xf0000f\n\t"
        "s_mov_b32 exec_hi, 0xf0000f00\n\t"
        "v_min_u32 v87, -2, v52\n\t"
        "v_min_u32 v86, -2, v44\n\t"
        "buffer_store_dwordx4 v[84:87], v17, %[ssrd], %[stt] offen sc1\n\t"
        "s_setprio 1\n\t"
        "Lf2_%=:\n\t"
        "s_mov_b64 exec, -1\n\t"
        "s_mov_b64 s[96:97], %[sgb]\n\t"
        "s_mov_b32 s98, 589824\n\t"
        "s_mov_b32 s99, 0x20000\n\t"
        "s_mov_b32 m0, %[sgl]\n\t"
        "s_add_u32 %[sob4], %[sob], 4096\n\t"
        "s_add_u32 %[sob4x], %[swi], 4096\n\t"
        "s_cmp_eq_u32 %[sgl], 0\n\t"
        "s_waitcnt vmcnt(2)\n\t"
        "s_barrier\n\t"
        "s_cbranch_scc1 L5_%=\n\t"
        "buffer_load_dwordx4 v12, s[96:99], 0 offen offset:0 lds\n\t"
        "buffer_load_dwordx4 v13, s[96:99], 0 offen offset:1024 lds\n\t"
        "buffer_load_dwordx4 v14, s[96:99], 0 offen offset:2048 lds\n\t"
        "L5_%=:\n\t"
        "s_mov_b32 m0, %[sld]\n\t"
        "s_cmp_eq_u32 %[sskip], 1\n\t"
        "s_cbranch_scc1 L6_%=\n\t"
        "buffer_load_dwordx4 v107, %[ssrd], %[swi] offen offset:0 sc1 lds\n\t"
        "buffer_load_dwordx4 v107, %[ssrd], %[swi] offen offset:1024 sc1 lds\n\t"
        "buffer_load_dwordx4 v107, %[ssrd], %[swi] offen offset:2048 sc1 lds\n\t"
        "buffer_load_dwordx4 v107, %[ssrd], %[swi] offen offset:3072 sc1 lds\n\t"
        "s_add_u32 m0, m0, 4096\n\t"
        "s_andn2_b32 %[sc], 0x1800, %[sstat]\n\t"
        "buffer_load_dwordx4 v107, %[ssrd], %[sob4x] offen offset:0 sc1 lds\n\t"
        "buffer_load_dwordx4 v107, %[ssrd], %[sob4x] offen offset:1024 sc1 lds\n\t"
        "s_cbranch_scc1 L6_%=\n\t"
        "buffer_load_dwordx4 v107, %[ssrd], %[sob4x] offen offset:2048 sc1 lds\n\t"
        "L6_%=:\n\t"
        "s_cmp_eq_u32 %[smsk], 0\n\t"
        "s_cbranch_scc0 Lk0_%=\n\t"
        "s_cmp_lg_u32 %[sskip], 1\n\t"
        "s_cbranch_scc1 L9_%=\n\t"
        "s_sleep 3\n\t"
        "L9_%=:\n\t"
        "s_cmp_ge_u32 %[sld], 18432\n\t"
        "s_cselect_b32 s98, 1, 0\n\t"
        "buffer_load_dwordx4 v[4:7], v108, %[ssrd], %[sob] offen offset:0 sc1\n\t"
        "buffer_load_dwordx4 v[8:11], v108, %[ssrd], %[sob] offen offset:256 sc1\n\t"
        "buffer_load_dwordx4 v[68:71], v108, %[ssrd], %[sob] offen offset:2048 sc1\n\t"
        "buffer_load_dwordx4 v[72:75], v108, %[ssrd], %[sob] offen offset:2304 sc1\n\t"
        "buffer_load_dwordx4 v[40:43], v107, %[ssrd], %[sob] offen offset:0 sc1\n\t"
        "buffer_load_dwordx4 v[44:47], v107, %[ssrd], %[sob] offen offset:1024 sc1\n\t"
        "buffer_load_dwordx4 v[48:51], v107, %[ssrd], %[sob] offen offset:2048 sc1\n\t"
        "buffer_load_dwordx4 v[52:55], v107, %[ssrd], %[sob] offen offset:3072 sc1\n\t"
        "buffer_load_dwordx4 v[56:59], v107, %[ssrd], %[sob4] offen offset:0 sc1\n\t"
        "buffer_load_dwordx4 v[60:63], v107, %[ssrd], %[sob4] offen offset:1024 sc1\n\t"
        "buffer_load_dwordx4 v[64:67], v107, %[ssrd], %[sob4] offen offset:2048 sc1\n\t"
        "s_lshr_b32 s96, %[sstat], 9\n\t"
        "s_and_b32 s96, s96, 12\n\t"
        "v_bfe_u32 v13, v107, 4, 4\n\t"
        "s_add_u32 s97, s96, 153984\n\t"
        "v_mov_b32 v3, s97\n\t"
        "s_lshl_b32 s96, s96, 4\n\t"
        "s_add_u32 s96, s96, 163572\n\t"
        "v_lshl_add_u32 v12, v13, 2, s96\n\t"
        "ds_read_b32 v12, v12\n\t"
        "s_lshr_b32 s96, %[sstat], 7\n\t"
        "s_and_b32 s96, s96, 0x7f0\n\t"
        "s_xor_b32 s96, s96, 0x400\n\t"
        "v_xnor_b32 v13, s96, v13\n\t"
        "s_movk_i32 s96, 0x7ff\n\t"
        "s_lshl_b32 s97, %[swt], 6\n\t"
        "v_lshl_add_u32 v2, v107, 6, %[sxf]\n\t"
        "v_mov_b32 v15, 153984\n\t"
        "v_mov_b32 v16, 1\n\t"
        "v_add_u32 v18, 75008, v107\n\t"
        "v_add_u32 v14, 75008, v106\n\t"
        "ds_read_b128 v[76:79], v18 offset:0\n\t"
        "ds_read_b128 v[80:83], v18 offset:1024\n\t"
        "ds_read_b128 v[84:87], v18 offset:2048\n\t"
        "ds_read_b128 v[88:91], v18 offset:3072\n\t"
        "ds_read_b128 v[92:95], v14 offset:0\n\t"
        "ds_read_b128 v[96:99], v18 offset:4608\n\t"
        "s_waitcnt vmcnt(11)\n\t"
        "s_mov_b64 exec, 1\n\t"
        "ds_add_u32 v15, v16 offset:60\n\t"
        "s_mov_b64 exec, -1\n\t"
        "s_waitcnt vmcnt(6)\n\t"
        "v_max3_u32 v17, v40, v41, v42\n\t"
        "v_max3_u32 v19, v4, v5, v10\n\t"
        "v_max3_u32 v17, v17, v6, v7\n\t"
        "v_max3_u32 v19, v19, v11, v68\n\t"
        "v_max3_u32 v17, v17, v8, v9\n\t"
        "v_max3_u32 v19, v19, v69, v74\n\t"
        "v_max3_u32 v17, v17, v70, v71\n\t"
        "v_max3_u32 v19, v19, v75, v73\n\t"
        "v_max3_u32 v17, v17, v72, v43\n\t"
        "v_max_u32 v17, v17, v19\n\t"
        "v_cmp_eq_u32 vcc, -1, v17\n\t"
        "s_cbranch_vccnz Lx01_%=\n\t"
        "Ld01_%=:\n\t"
        "s_waitcnt lgkmcnt(6)\n\t"
        "v_mfma_f32_16x16x32_bf16 v[20:23], v[76:79], v[40:43], 0\n\t"
        "v_add_f32 v4, v4, v10\n\t"
        "v_add_f32 v5, v5, v11\n\t"
        "v_add_f32 v68, v68, v74\n\t"
        "v_add_f32 v69, v69, v75\n\t"
        "v_add_f32 v10, v4, v68\n\t"
        "v_add_f32 v4, v4, v68\n\t"
        "v_add_f32 v11, v5, v69\n\t"
        "v_add_f32 v5, v5, v69\n\t"
        "v_permlane16_swap_b32 v10, v4\n\t"
        "s_nop 0\n\t"
        "v_permlane16_swap_b32 v11, v5\n\t"
        "v_add_f32 v68, v10, v4\n\t"
        "v_add_f32 v4, v10, v4\n\t"
        "v_add_f32 v69, v11, v5\n\t"
        "v_add_f32 v5, v11, v5\n\t"
        "ds_read_b128 v[76:79], v18 offset:5632\n\t"
        "s_waitcnt lgkmcnt(6)\n\t"
        "v_mfma_f32_16x16x32_bf16 v[24:27], v[80:83], v[40:43], 0\n\t"
        "v_permlane32_swap_b32 v69, v5\n\t"
        "v_permlane32_swap_b32 v68, v4\n\t"
        "v_add_f32 v5, v69, v5\n\t"
        "v_add_f32 v5, v5, v12\n\t"
        "v_rsq_f32 v5, v5\n\t"
        "v_add_f32 v4, v68, v4\n\t"
        "v_mul_f32 v4, v4, v5\n\t"
        "v_ashrrev_i32 v5, 31, v4\n\t"
        "v_or_b32 v5, 0x80000000, v5\n\t"
        "v_xor_b32 v4, v4, v5\n\t"
        "v_bfi_b32 v4, s96, v13, v4\n\t"
        "v_mov_b32 v5, %[stag]\n\t"
        "ds_read_b128 v[80:83], v18 offset:6656\n\t"
        "s_waitcnt lgkmcnt(6)\n\t"
        "v_mfma_f32_16x16x32_bf16 v[28:31], v[84:87], v[40:43], 0\n\t"
        "s_waitcnt vmcnt(5)\n\t"
        "v_max3_u32 v17, v44, v45, v46\n\t"
        "v_max_u32 v17, v17, v47\n\t"
        "v_cmp_eq_u32 vcc, -1, v17\n\t"
        "v_max_u32_dpp v4, v4, v4 row_ror:8 row_mask:0xf bank_mask:0xf\n\t"
        "s_nop 1\n\t"
        "v_max_u32_dpp v4, v4, v4 row_ror:4 row_mask:0xf bank_mask:0xf\n\t"
        "s_nop 1\n\t"
        "v_max_u32_dpp v4, v4, v4 row_ror:2 row_mask:0xf bank_mask:0xf\n\t"
        "s_nop 1\n\t"
        "v_max_u32_dpp v4, v4, v4 row_ror:1 row_mask:0xf bank_mask:0xf\n\t"
        "s_mov_b64 exec, 15\n\t"
        "buffer_store_dwordx2 v[4:5], v2, %[ssrd], 0 offen sc1\n\t"
        "v_and_or_b32 v10, v4, 15, s97\n\t"
        "ds_write_b32 v3, v10 offset:112\n\t"
        "s_mov_b64 exec, -1\n\t"
        "ds_read_b128 v[84:87], v18 offset:7680\n\t"
        "s_waitcnt lgkmcnt(7)\n\t"
        "v_mfma_f32_16x16x32_bf16 v[32:35], v[88:91], v[40:43], 0\n\t"
        "ds_read_b128 v[88:91], v14 offset:4608\n\t"
        "s_waitcnt lgkmcnt(7)\n\t"
        "v_mfma_f32_16x16x32_bf16 v[36:39], v[92:95], v[40:43], 0\n\t"
        "s_cbranch_vccnz Lx11_%=\n\t"
        "Ld11_%=:\n\t"
        "ds_read_b32 v16, v15 offset:60\n\t"
        "ds_read_b128 v[92:95], v18 offset:9216\n\t"
        "ds_read_b128 v[40:43], v18 offset:10240\n\t"
        "s_waitcnt lgkmcnt(9)\n\t"
        "v_mfma_f32_16x16x32_bf16 v[20:23], v[96:99], v[44:47], v[20:23]\n\t"
        "ds_read_b128 v[96:99], v18 offset:11264\n\t"
        "s_waitcnt lgkmcnt(8)\n\t"
        "v_mfma_f32_16x16x32_bf16 v[24:27], v[76:79], v[44:47], v[24:27]\n\t"
        "ds_read_b128 v[76:79], v18 offset:12288\n\t"
        "s_waitcnt lgkmcnt(8)\n\t"
        "v_mfma_f32_16x16x32_bf16 v[28:31], v[80:83], v[44:47], v[28:31]\n\t"
        "s_waitcnt vmcnt(5)\n\t"
        "v_max3_u32 v17, v48, v49, v50\n\t"
        "v_max_u32 v17, v17, v51\n\t"
        "v_cmp_eq_u32 vcc, -1, v17\n\t"
        "ds_read_b128 v[80:83], v14 offset:9216\n\t"
        "s_waitcnt lgkmcnt(7)\n\t"
        "v_mfma_f32_16x16x32_bf16 v[32:35], v[84:87], v[44:47], v[32:35]\n\t"
        "s_waitcnt lgkmcnt(5)\n\t"
        "Lwc10_%=:\n\t"
        "v_readfirstlane_b32 %[sob4x], v16\n\t"
        "s_cmp_lt_u32 %[sob4x], %[swt]\n\t"
        "s_cbranch_scc1 Lws10_%=\n\t"
        "Lwg10_%=:\n\t"
        "ds_read_b128 v[84:87], v18 offset:13824\n\t"
        "v_mfma_f32_16x16x32_bf16 v[36:39], v[88:91], v[44:47], v[36:39]\n\t"
        "s_cbranch_vccnz Lx21_%=\n\t"
        "Ld21_%=:\n\t"
        "ds_read_b128 v[88:91], v18 offset:14848\n\t"
        "ds_read_b128 v[44:47], v18 offset:15872\n\t"
        "s_waitcnt lgkmcnt(7)\n\t"
        "v_mfma_f32_16x16x32_bf16 v[20:23], v[92:95], v[48:51], v[20:23]\n\t"
        "ds_read_b128 v[92:95], v18 offset:16896\n\t"
        "s_waitcnt lgkmcnt(7)\n\t"
        "v_mfma_f32_16x16x32_bf16 v[24:27], v[40:43], v[48:51], v[24:27]\n\t"
        "ds_read_b128 v[40:43], v14 offset:13824\n\t"
        "s_waitcnt lgkmcnt(7)\n\t"
        "v_mfma_f32_16x16x32_bf16 v[28:31], v[96:99], v[48:51], v[28:31]\n\t"
        "s_waitcnt vmcnt(4)\n\t"
        "v_max3_u32 v17, v52, v53, v54\n\t"
        "v_max_u32 v17, v17, v55\n\t"
        "v_cmp_eq_u32 vcc, -1, v17\n\t"
        "ds_read_b128 v[96:99], v18 offset:18432\n\t"
        "s_waitcnt lgkmcnt(7)\n\t"
        "v_mfma_f32_16x16x32_bf16 v[32:35], v[76:79], v[48:51], v[32:35]\n\t"
        "ds_read_b128 v[76:79], v18 offset:19456\n\t"
        "s_waitcnt lgkmcnt(7)\n\t"
        "v_mfma_f32_16x16x32_bf16 v[36:39], v[80:83], v[48:51], v[36:39]\n\t"
        "s_cbranch_vccnz Lx31_%=\n\t"
        "Ld31_%=:\n\t"
        "ds_read_b128 v[80:83], v18 offset:20480\n\t"
        "ds_read_b128 v[48:51], v18 offset:21504\n\t"
        "s_waitcnt lgkmcnt(8)\n\t"
        "v_mfma_f32_16x16x32_bf16 v[20:23], v[84:87], v[52:55], v[20:23]\n\t"
        "ds_read_b128 v[84:87], v14 offset:18432\n\t"
        "s_waitcnt lgkmcnt(8)\n\t"
        "v_mfma_f32_16x16x32_bf16 v[24:27], v[88:91], v[52:55], v[24:27]\n\t"
        "ds_read_b128 v[88:91], v107 offset:0\n\t"
        "s_waitcnt lgkmcnt(8)\n\t"
        "v_mfma_f32_16x16x32_bf16 v[28:31], v[44:47], v[52:55], v[28:31]\n\t"
        "s_waitcnt vmcnt(3)\n\t"
        "v_max3_u32 v17, v56, v57, v58\n\t"
        "v_max_u32 v17, v17, v59\n\t"
        "v_cmp_eq_u32 vcc, -1, v17\n\t"
        "ds_read_b128 v[44:47], v107 offset:1024\n\t"
        "s_waitcnt lgkmcnt(8)\n\t"
        "v_mfma_f32_16x16x32_bf16 v[32:35], v[92:95], v[52:55], v[32:35]\n\t"
        "ds_read_b128 v[92:95], v107 offset:2048\n\t"
        "s_waitcnt lgkmcnt(8)\n\t"
        "v_mfma_f32_16x16x32_bf16 v[36:39], v[40:43], v[52:55], v[36:39]\n\t"
        "s_cbranch_vccnz Lx41_%=\n\t"
        "Ld41_%=:\n\t"
        "ds_read_b128 v[40:43], v107 offset:3072\n\t"
        "s_waitcnt lgkmcnt(8)\n\t"
        "v_mfma_f32_16x16x32_bf16 v[20:23], v[96:99], v[56:59], v[20:23]\n\t"
        "ds_read_b128 v[52:55], v106 offset:0\n\t"
        "s_waitcnt lgkmcnt(8)\n\t"
        "v_mfma_f32_16x16x32_bf16 v[24:27], v[76:79], v[56:59], v[24:27]\n\t"
        "ds_read_b128 v[96:99], v107 offset:4608\n\t"
        "s_waitcnt lgkmcnt(8)\n\t"
        "v_mfma_f32_16x16x32_bf16 v[28:31], v[80:83], v[56:59], v[28:31]\n\t"
        "s_waitcnt vmcnt(2)\n\t"
        "v_max3_u32 v17, v60, v61, v62\n\t"
        "v_max_u32 v17, v17, v63\n\t"
        "v_cmp_eq_u32 vcc, -1, v17\n\t"
        "ds_read_b128 v[76:79], v107 offset:5632\n\t"
        "s_waitcnt lgkmcnt(8)\n\t"
        "v_mfma_f32_16x16x32_bf16 v[32:35], v[48:51], v[56:59], v[32:35]\n\t"
        "ds_read_b128 v[80:83], v107 offset:6656\n\t"
        "s_waitcnt lgkmcnt(8)\n\t"
        "v_mfma_f32_16x16x32_bf16 v[36:39], v[84:87], v[56:59], v[36:39]\n\t"
        "s_cbranch_vccnz Lx51_%=\n\t"
        "Ld51_%=:\n\t"
        "ds_read_b128 v[48:51], v107 offset:7680\n\t"
        "s_waitcnt lgkmcnt(8)\n\t"
        "v_mfma_f32_16x16x32_bf16 v[20:23], v[88:91], v[60:63], v[20:23]\n\t"
        "ds_read_b128 v[84:87], v106 offset:4608\n\t"
        "s_waitcnt lgkmcnt(8)\n\t"
        "v_mfma_f32_16x16x32_bf16 v[24:27], v[44:47], v[60:63], v[24:27]\n\t"
        "ds_read_b128 v[56:59], v107 offset:9216\n\t"
        "s_waitcnt lgkmcnt(8)\n\t"
        "v_mfma_f32_16x16x32_bf16 v[28:31], v[92:95], v[60:63], v[28:31]\n\t"
        "s_waitcnt vmcnt(1)\n\t"
        "v_max3_u32 v17, v64, v65, v66\n\t"
        "v_max_u32 v17, v17, v67\n\t"
        "v_cmp_eq_u32 vcc, -1, v17\n\t"
        "ds_read_b128 v[88:91], v107 offset:10240\n\t"
        "s_waitcnt lgkmcnt(8)\n\t"
        "v_mfma_f32_16x16x32_bf16 v[32:35], v[40:43], v[60:63], v[32:35]\n\t"
        "ds_read_b128 v[44:47], v107 offset:11264\n\t"
        "s_waitcnt lgkmcnt(8)\n\t"
        "v_mfma_f32_16x16x32_bf16 v[36:39], v[52:55], v[60:63], v[36:39]\n\t"
        "s_cbranch_vccnz Lx61_%=\n\t"
        "Ld61_%=:\n\t"
        "ds_read_b128 v[92:95], v107 offset:12288\n\t"
        "s_waitcnt lgkmcnt(8)\n\t"
        "v_mfma_f32_16x16x32_bf16 v[20:23], v[96:99], v[64:67], v[20:23]\n\t"
        "ds_read_b128 v[40:43], v106 offset:9216\n\t"
        "s_waitcnt lgkmcnt(8)\n\t"
        "v_mfma_f32_16x16x32_bf16 v[24:27], v[76:79], v[64:67], v[24:27]\n\t"
        "ds_read_b128 v[52:55], v107 offset:13824\n\t"
        "s_waitcnt lgkmcnt(8)\n\t"
        "v_mfma_f32_16x16x32_bf16 v[28:31], v[80:83], v[64:67], v[28:31]\n\t"
        "ds_read_b128 v[60:63], v107 offset:14848\n\t"
        "s_waitcnt lgkmcnt(8)\n\t"
        "v_mfma_f32_16x16x32_bf16 v[32:35], v[48:51], v[64:67], v[32:35]\n\t"
        "ds_read_b128 v[96:99], v107 offset:15872\n\t"
        "s_waitcnt lgkmcnt(8)\n\t"
        "v_mfma_f32_16x16x32_bf16 v[36:39], v[84:87], v[64:67], v[36:39]\n\t"
        "ds_read_b128 v[76:79], v107 offset:16896\n\t"
        "s_waitcnt lgkmcnt(8)\n\t"
        "v_mfma_f32_16x16x32_bf16 v[20:23], v[56:59], v[6:9], v[20:23]\n\t"
        "ds_read_b128 v[80:83], v106 offset:13824\n\t"
        "s_waitcnt lgkmcnt(8)\n\t"
        "v_mfma_f32_16x16x32_bf16 v[24:27], v[88:91], v[6:9], v[24:27]\n\t"
        "s_waitcnt lgkmcnt(7)\n\t"
        "v_mfma_f32_16x16x32_bf16 v[28:31], v[44:47], v[6:9], v[28:31]\n\t"
        "s_waitcnt lgkmcnt(6)\n\t"
        "v_mfma_f32_16x16x32_bf16 v[32:35], v[92:95], v[6:9], v[32:35]\n\t"
        "s_waitcnt lgkmcnt(5)\n\t"
        "v_mfma_f32_16x16x32_bf16 v[36:39], v[40:43], v[6:9], v[36:39]\n\t"
        "s_waitcnt lgkmcnt(4)\n\t"
        "v_mfma_f32_16x16x32_bf16 v[20:23], v[52:55], v[70:73], v[20:23]\n\t"
        "s_waitcnt lgkmcnt(3)\n\t"
        "v_mfma_f32_16x16x32_bf16 v[24:27], v[60:63], v[70:73], v[24:27]\n\t"
        "v_add_u32 v18, %[sscr], v107\n\t"
        "s_waitcnt lgkmcnt(2)\n\t"
        "v_mfma_f32_16x16x32_bf16 v[28:31], v[96:99], v[70:73], v[28:31]\n\t"
        "s_waitcnt lgkmcnt(1)\n\t"
        "v_mfma_f32_16x16x32_bf16 v[32:35], v[76:79], v[70:73], v[32:35]\n\t"
        "s_waitcnt lgkmcnt(0)\n\t"
        "v_mfma_f32_16x16x32_bf16 v[36:39], v[80:83], v[70:73], v[36:39]\n\t"
        "v_mov_b32 v16, 1\n\t"
        "ds_write_b128 v18, v[20:23] offset:0\n\t"
        "ds_write_b128 v18, v[24:27] offset:1024\n\t"
        "s_nop 0\n\t"
        "ds_write_b128 v18, v[28:31] offset:2048\n\t"
        "s_nop 0\n\t"
        "ds_write_b128 v18, v[32:35] offset:3072\n\t"
        "s_mov_b32 exec_hi, 0\n\t"
        "ds_write_b128 v18, v[36:39] offset:4096\n\t"
        "s_mov_b32 exec_lo, 1\n\t"
        "ds_add_u32 v15, v16 offset:24\n\t"
        "s_mov_b64 exec, -1\n\t"
        "s_branch Lend2_%=\n\t"
        "Lx01_%=:\n\t"
        "s_cmp_eq_u32 %[scn], 0\n\t"
        "s_cbranch_scc1 Ld01_%=\n\t"
        "s_sub_u32 %[scn], %[scn], 1\n\t"
        "buffer_load_dwordx4 v[4:7], v108, %[ssrd], %[sob] offen offset:0 sc1\n\t"
        "buffer_load_dwordx4 v[8:11], v108, %[ssrd], %[sob] offen offset:256 sc1\n\t"
        "buffer_load_dwordx4 v[68:71], v108, %[ssrd], %[sob] offen offset:2048 sc1\n\t"
        "buffer_load_dwordx4 v[72:75], v108, %[ssrd], %[sob] offen offset:2304 sc1\n\t"
        "buffer_load_dwordx4 v[40:43], v107, %[ssrd], %[sob] offen offset:0 sc1\n\t"
        "buffer_load_dwordx4 v[44:47], v107, %[ssrd], %[sob] offen offset:1024 sc1\n\t"
        "buffer_load_dwordx4 v[48:51], v107, %[ssrd], %[sob] offen offset:2048 sc1\n\t"
        "buffer_load_dwordx4 v[52:55], v107, %[ssrd], %[sob] offen offset:3072 sc1\n\t"
        "buffer_load_dwordx4 v[56:59], v107, %[ssrd], %[sob4] offen offset:0 sc1\n\t"
        "buffer_load_dwordx4 v[60:63], v107, %[ssrd], %[sob4] offen offset:1024 sc1\n\t"
        "buffer_load_dwordx4 v[64:67], v107, %[ssrd], %[sob4] offen offset:2048 sc1\n\t"
        "s_mov_b32 s98, 1\n\t"
        "s_waitcnt vmcnt(6)\n\t"
        "v_max3_u32 v17, v40, v41, v42\n\t"
        "v_max3_u32 v19, v4, v5, v10\n\t"
        "v_max3_u32 v17, v17, v6, v7\n\t"
        "v_max3_u32 v19, v19, v11, v68\n\t"
        "v_max3_u32 v17, v17, v8, v9\n\t"
        "v_max3_u32 v19, v19, v69, v74\n\t"
        "v_max3_u32 v17, v17, v70, v71\n\t"
        "v_max3_u32 v19, v19, v75, v73\n\t"
        "v_max3_u32 v17, v17, v72, v43\n\t"
        "v_max_u32 v17, v17, v19\n\t"
        "v_cmp_eq_u32 vcc, -1, v17\n\t"
        "s_cbranch_vccnz Lx01_%=\n\t"
        "s_branch Ld01_%=\n\t"
        "Lx11_%=:\n\t"
        "s_cmp_eq_u32 %[scn], 0\n\t"
        "s_cbranch_scc1 Ld11_%=\n\t"
        "s_sub_u32 %[scn], %[scn], 1\n\t"
        "buffer_load_dwordx4 v[44:47], v107, %[ssrd], %[sob] offen offset:1024 sc1\n\t"
        "buffer_load_dwordx4 v[48:51], v107, %[ssrd], %[sob] offen offset:2048 sc1\n\t"
        "buffer_load_dwordx4 v[52:55], v107, %[ssrd], %[sob] offen offset:3072 sc1\n\t"
        "buffer_load_dwordx4 v[56:59], v107, %[ssrd], %[sob4] offen offset:0 sc1\n\t"
        "buffer_load_dwordx4 v[60:63], v107, %[ssrd], %[sob4] offen offset:1024 sc1\n\t"
        "buffer_load_dwordx4 v[64:67], v107, %[ssrd], %[sob4] offen offset:2048 sc1\n\t"
        "s_mov_b32 s98, 1\n\t"
        "s_mov_b64 exec, 15\n\t"
        "buffer_store_dwordx2 v[4:5], v2, %[ssrd], 0 offen sc1\n\t"
        "s_mov_b64 exec, -1\n\t"
        "s_waitcnt vmcnt(6)\n\t"
        "v_max3_u32 v17, v44, v45, v46\n\t"
        "v_max_u32 v17, v17, v47\n\t"
        "v_cmp_eq_u32 vcc, -1, v17\n\t"
        "s_cbranch_vccnz Lx11_%=\n\t"
        "s_branch Ld11_%=\n\t"
        "Lx21_%=:\n\t"
        "s_cmp_eq_u32 %[scn], 0\n\t"
        "s_cbranch_scc1 Ld21_%=\n\t"
        "s_sub_u32 %[scn], %[scn], 1\n\t"
        "buffer_load_dwordx4 v[48:51], v107, %[ssrd], %[sob] offen offset:2048 sc1\n\t"
        "buffer_load_dwordx4 v[52:55], v107, %[ssrd], %[sob] offen offset:3072 sc1\n\t"
        "buffer_load_dwordx4 v[56:59], v107, %[ssrd], %[sob4] offen offset:0 sc1\n\t"
        "buffer_load_dwordx4 v[60:63], v107, %[ssrd], %[sob4] offen offset:1024 sc1\n\t"
        "buffer_load_dwordx4 v[64:67], v107, %[ssrd], %[sob4] offen offset:2048 sc1\n\t"
        "s_mov_b32 s98, 1\n\t"
        "s_mov_b64 exec, 15\n\t"
        "buffer_store_dwordx2 v[4:5], v2, %[ssrd], 0 offen sc1\n\t"
        "s_mov_b64 exec, -1\n\t"
        "s_waitcnt vmcnt(5)\n\t"
        "v_max3_u32 v17, v48, v49, v50\n\t"
        "v_max_u32 v17, v17, v51\n\t"
        "v_cmp_eq_u32 vcc, -1, v17\n\t"
        "s_cbranch_vccnz Lx21_%=\n\t"
        "s_branch Ld21_%=\n\t"
        "Lx31_%=:\n\t"
        "s_cmp_eq_u32 %[scn], 0\n\t"
        "s_cbranch_scc1 Ld31_%=\n\t"
        "s_sub_u32 %[scn], %[scn], 1\n\t"
        "buffer_load_dwordx4 v[52:55], v107, %[ssrd], %[sob] offen offset:3072 sc1\n\t"
        "buffer_load_dwordx4 v[56:59], v107, %[ssrd], %[sob4] offen offset:0 sc1\n\t"
        "buffer_load_dwordx4 v[60:63], v107, %[ssrd], %[sob4] offen offset:1024 sc1\n\t"
        "buffer_load_dwordx4 v[64:67], v107, %[ssrd], %[sob4] offen offset:2048 sc1\n\t"
        "s_mov_b32 s98, 1\n\t"
        "s_mov_b64 exec, 15\n\t"
        "buffer_store_dwordx2 v[4:5], v2, %[ssrd], 0 offen sc1\n\t"
        "s_mov_b64 exec, -1\n\t"
        "s_waitcnt vmcnt(4)\n\t"
        "v_max3_u32 v17, v52, v53, v54\n\t"
        "v_max_u32 v17, v17, v55\n\t"
        "v_cmp_eq_u32 vcc, -1, v17\n\t"
        "s_cbranch_vccnz Lx31_%=\n\t"
        "s_branch Ld31_%=\n\t"
        "Lx41_%=:\n\t"
        "s_cmp_eq_u32 %[scn], 0\n\t"
        "s_cbranch_scc1 Ld41_%=\n\t"
        "s_sub_u32 %[scn], %[scn], 1\n\t"
        "buffer_load_dwordx4 v[56:59], v107, %[ssrd], %[sob4] offen offset:0 sc1\n\t"
        "buffer_load_dwordx4 v[60:63], v107, %[ssrd], %[sob4] offen offset:1024 sc1\n\t"
        "buffer_load_dwordx4 v[64:67], v107, %[ssrd], %[sob4] offen offset:2048 sc1\n\t"
        "s_mov_b32 s98, 1\n\t"
        "s_mov_b64 exec, 15\n\t"
        "buffer_store_dwordx2 v[4:5], v2, %[ssrd], 0 offen sc1\n\t"
        "s_mov_b64 exec, -1\n\t"
        "s_waitcnt vmcnt(3)\n\t"
        "v_max3_u32 v17, v56, v57, v58\n\t"
        "v_max_u32 v17, v17, v59\n\t"
        "v_cmp_eq_u32 vcc, -1, v17\n\t"
        "s_cbranch_vccnz Lx41_%=\n\t"
        "s_branch Ld41_%=\n\t"
        "Lx51_%=:\n\t"
        "s_cmp_eq_u32 %[scn], 0\n\t"
        "s_cbranch_scc1 Ld51_%=\n\t"
        "s_sub_u32 %[scn], %[scn], 1\n\t"
        "buffer_load_dwordx4 v[60:63], v107, %[ssrd], %[sob4] offen offset:1024 sc1\n\t"
        "buffer_load_dwordx4 v[64:67], v107, %[ssrd], %[sob4] offen offset:2048 sc1\n\t"
        "s_mov_b32 s98, 1\n\t"
        "s_mov_b64 exec, 15\n\t"
        "buffer_store_dwordx2 v[4:5], v2, %[ssrd], 0 offen sc1\n\t"
        "s_mov_b64 exec, -1\n\t"
        "s_waitcnt vmcnt(2)\n\t"
        "v_max3_u32 v17, v60, v61, v62\n\t"
        "v_max_u32 v17, v17, v63\n\t"
        "v_cmp_eq_u32 vcc, -1, v17\n\t"
        "s_cbranch_vccnz Lx51_%=\n\t"
        "s_branch Ld51_%=\n\t"
        "Lx61_%=:\n\t"
        "s_cmp_eq_u32 %[scn], 0\n\t"
        "s_cbranch_scc1 Ld61_%=\n\t"
        "s_sub_u32 %[scn], %[scn], 1\n\t"
        "buffer_load_dwordx4 v[64:67], v107, %[ssrd], %[sob4] offen offset:2048 sc1\n\t"
        "s_mov_b32 s98, 1\n\t"
        "s_mov_b64 exec, 15\n\t"
        "buffer_store_dwordx2 v[4:5], v2, %[ssrd], 0 offen sc1\n\t"
        "s_mov_b64 exec, -1\n\t"
        "s_waitcnt vmcnt(1)\n\t"
        "v_max3_u32 v17, v64, v65, v66\n\t"
        "v_max_u32 v17, v17, v67\n\t"
        "v_cmp_eq_u32 vcc, -1, v17\n\t"
        "s_cbranch_vccnz Lx61_%=\n\t"
        "s_branch Ld61_%=\n\t"
        "Lws10_%=:\n\t"
        "s_cmp_eq_u32 %[scn], 0\n\t"
        "s_cbranch_scc1 Lwg10_%=\n\t"
        "s_sub_u32 %[scn], %[scn], 1\n\t"
        "s_sleep 1\n\t"
        "ds_read_b32 v16, v15 offset:60\n\t"
        "s_waitcnt lgkmcnt(0)\n\t"
        "s_branch Lwc10_%=\n\t"
        "Lx00_%=:\n\t"
        "s_cmp_eq_u32 %[scn], 0\n\t"
        "s_cbranch_scc1 Ld00_%=\n\t"
        "s_sub_u32 %[scn], %[scn], 1\n\t"
        "buffer_load_dwordx4 v[40:43], v107, %[ssrd], %[sob] offen offset:0 sc1\n\t"
        "buffer_load_dwordx4 v[44:47], v107, %[ssrd], %[sob] offen offset:1024 sc1\n\t"
        "buffer_load_dwordx4 v[48:51], v107, %[ssrd], %[sob] offen offset:2048 sc1\n\t"
        "buffer_load_dwordx4 v[52:55], v107, %[ssrd], %[sob] offen offset:3072 sc1\n\t"
        "buffer_load_dwordx4 v[56:59], v107, %[ssrd], %[sob4] offen offset:0 sc1\n\t"
        "buffer_load_dwordx4 v[60:63], v107, %[ssrd], %[sob4] offen offset:1024 sc1\n\t"
        "buffer_load_dwordx4 v[64:67], v107, %[ssrd], %[sob4] offen offset:2048 sc1\n\t"
        "buffer_load_dwordx4 v[6:9], v108, %[ssrd], %[sob] offen offset:0 sc1\n\t"
        "buffer_load_dwordx4 v[70:73], v108, %[ssrd], %[sob] offen offset:1024 sc1\n\t"
        "s_waitcnt vmcnt(8)\n\t"
        "v_max3_u32 v17, v40, v41, v42\n\t"
        "v_max_u32 v17, v17, v43\n\t"
        "v_cmp_eq_u32 vcc, -1, v17\n\t"
        "s_cbranch_vccnz Lx00_%=\n\t"
        "s_branch Ld00_%=\n\t"
        "Lx10_%=:\n\t"
        "s_cmp_eq_u32 %[scn], 0\n\t"
        "s_cbranch_scc1 Ld10_%=\n\t"
        "s_sub_u32 %[scn], %[scn], 1\n\t"
        "buffer_load_dwordx4 v[44:47], v107, %[ssrd], %[sob] offen offset:1024 sc1\n\t"
        "buffer_load_dwordx4 v[48:51], v107, %[ssrd], %[sob] offen offset:2048 sc1\n\t"
        "buffer_load_dwordx4 v[52:55], v107, %[ssrd], %[sob] offen offset:3072 sc1\n\t"
        "buffer_load_dwordx4 v[56:59], v107, %[ssrd], %[sob4] offen offset:0 sc1\n\t"
        "buffer_load_dwordx4 v[60:63], v107, %[ssrd], %[sob4] offen offset:1024 sc1\n\t"
        "buffer_load_dwordx4 v[64:67], v107, %[ssrd], %[sob4] offen offset:2048 sc1\n\t"
        "buffer_load_dwordx4 v[6:9], v108, %[ssrd], %[sob] offen offset:0 sc1\n\t"
        "buffer_load_dwordx4 v[70:73], v108, %[ssrd], %[sob] offen offset:1024 sc1\n\t"
        "s_waitcnt vmcnt(7)\n\t"
        "v_max3_u32 v17, v44, v45, v46\n\t"
        "v_max_u32 v17, v17, v47\n\t"
        "v_cmp_eq_u32 vcc, -1, v17\n\t"
        "s_cbranch_vccnz Lx10_%=\n\t"
        "s_branch Ld10_%=\n\t"
        "Lx20_%=:\n\t"
        "s_cmp_eq_u32 %[scn], 0\n\t"
        "s_cbranch_scc1 Ld20_%=\n\t"
        "s_sub_u32 %[scn], %[scn], 1\n\t"
        "buffer_load_dwordx4 v[48:51], v107, %[ssrd], %[sob] offen offset:2048 sc1\n\t"
        "buffer_load_dwordx4 v[52:55], v107, %[ssrd], %[sob] offen offset:3072 sc1\n\t"
        "buffer_load_dwordx4 v[56:59], v107, %[ssrd], %[sob4] offen offset:0 sc1\n\t"
        "buffer_load_dwordx4 v[60:63], v107, %[ssrd], %[sob4] offen offset:1024 sc1\n\t"
        "buffer_load_dwordx4 v[64:67], v107, %[ssrd], %[sob4] offen offset:2048 sc1\n\t"
        "buffer_load_dwordx4 v[6:9], v108, %[ssrd], %[sob] offen offset:0 sc1\n\t"
        "buffer_load_dwordx4 v[70:73], v108, %[ssrd], %[sob] offen offset:1024 sc1\n\t"
        "s_waitcnt vmcnt(6)\n\t"
        "v_max3_u32 v17, v48, v49, v50\n\t"
        "v_max_u32 v17, v17, v51\n\t"
        "v_cmp_eq_u32 vcc, -1, v17\n\t"
        "s_cbranch_vccnz Lx20_%=\n\t"
        "s_branch Ld20_%=\n\t"
        "Lx30_%=:\n\t"
        "s_cmp_eq_u32 %[scn], 0\n\t"
        "s_cbranch_scc1 Ld30_%=\n\t"
        "s_sub_u32 %[scn], %[scn], 1\n\t"
        "buffer_load_dwordx4 v[52:55], v107, %[ssrd], %[sob] offen offset:3072 sc1\n\t"
        "buffer_load_dwordx4 v[56:59], v107, %[ssrd], %[sob4] offen offset:0 sc1\n\t"
        "buffer_load_dwordx4 v[60:63], v107, %[ssrd], %[sob4] offen offset:1024 sc1\n\t"
        "buffer_load_dwordx4 v[64:67], v107, %[ssrd], %[sob4] offen offset:2048 sc1\n\t"
        "buffer_load_dwordx4 v[6:9], v108, %[ssrd], %[sob] offen offset:0 sc1\n\t"
        "buffer_load_dwordx4 v[70:73], v108, %[ssrd], %[sob] offen offset:1024 sc1\n\t"
        "s_waitcnt vmcnt(5)\n\t"
        "v_max3_u32 v17, v52, v53, v54\n\t"
        "v_max_u32 v17, v17, v55\n\t"
        "v_cmp_eq_u32 vcc, -1, v17\n\t"
        "s_cbranch_vccnz Lx30_%=\n\t"
        "s_branch Ld30_%=\n\t"
        "Lx40_%=:\n\t"
        "s_cmp_eq_u32 %[scn], 0\n\t"
        "s_cbranch_scc1 Ld40_%=\n\t"
        "s_sub_u32 %[scn], %[scn], 1\n\t"
        "buffer_load_dwordx4 v[56:59], v107, %[ssrd], %[sob4] offen offset:0 sc1\n\t"
        "buffer_load_dwordx4 v[60:63], v107, %[ssrd], %[sob4] offen offset:1024 sc1\n\t"
        "buffer_load_dwordx4 v[64:67], v107, %[ssrd], %[sob4] offen offset:2048 sc1\n\t"
        "buffer_load_dwordx4 v[6:9], v108, %[ssrd], %[sob] offen offset:0 sc1\n\t"
        "buffer_load_dwordx4 v[70:73], v108, %[ssrd], %[sob] offen offset:1024 sc1\n\t"
        "s_waitcnt vmcnt(4)\n\t"
        "v_max3_u32 v17, v56, v57, v58\n\t"
        "v_max_u32 v17, v17, v59\n\t"
        "v_cmp_eq_u32 vcc, -1, v17\n\t"
        "s_cbranch_vccnz Lx40_%=\n\t"
        "s_branch Ld40_%=\n\t"
        "Lx50_%=:\n\t"
        "s_cmp_eq_u32 %[scn], 0\n\t"
        "s_cbranch_scc1 Ld50_%=\n\t"
        "s_sub_u32 %[scn], %[scn], 1\n\t"
        "buffer_load_dwordx4 v[60:63], v107, %[ssrd], %[sob4] offen offset:1024 sc1\n\t"
        "buffer_load_dwordx4 v[64:67], v107, %[ssrd], %[sob4] offen offset:2048 sc1\n\t"
        "buffer_load_dwordx4 v[6:9], v108, %[ssrd], %[sob] offen offset:0 sc1\n\t"
        "buffer_load_dwordx4 v[70:73], v108, %[ssrd], %[sob] offen offset:1024 sc1\n\t"
        "s_waitcnt vmcnt(3)\n\t"
        "v_max3_u32 v17, v60, v61, v62\n\t"
        "v_max_u32 v17, v17, v63\n\t"
        "v_cmp_eq_u32 vcc, -1, v17\n\t"
        "s_cbranch_vccnz Lx50_%=\n\t"
        "s_branch Ld50_%=\n\t"
        "Lx60_%=:\n\t"
        "s_cmp_eq_u32 %[scn], 0\n\t"
        "s_cbranch_scc1 Ld60_%=\n\t"
        "s_sub_u32 %[scn], %[scn], 1\n\t"
        "buffer_load_dwordx4 v[64:67], v107, %[ssrd], %[sob4] offen offset:2048 sc1\n\t"
        "buffer_load_dwordx4 v[6:9], v108, %[ssrd], %[sob] offen offset:0 sc1\n\t"
        "buffer_load_dwordx4 v[70:73], v108, %[ssrd], %[sob] offen offset:1024 sc1\n\t"
        "s_waitcnt vmcnt(2)\n\t"
        "v_max3_u32 v17, v64, v65, v66\n\t"
        "v_max_u32 v17, v17, v67\n\t"
        "v_cmp_eq_u32 vcc, -1, v17\n\t"
        "s_cbranch_vccnz Lx60_%=\n\t"
        "s_branch Ld60_%=\n\t"
        "Lx70_%=:\n\t"
        "s_cmp_eq_u32 %[scn], 0\n\t"
        "s_cbranch_scc1 Ld70_%=\n\t"
        "s_sub_u32 %[scn], %[scn], 1\n\t"
        "buffer_load_dwordx4 v[6:9], v108, %[ssrd], %[sob] offen offset:0 sc1\n\t"
        "buffer_load_dwordx4 v[70:73], v108, %[ssrd], %[sob] offen offset:1024 sc1\n\t"
        "s_waitcnt vmcnt(1)\n\t"
        "v_max3_u32 v17, v6, v7, v8\n\t"
        "v_max_u32 v17, v17, v9\n\t"
        "v_cmp_eq_u32 vcc, -1, v17\n\t"
        "s_cbranch_vccnz Lx70_%=\n\t"
        "s_branch Ld70_%=\n\t"
        "Lx80_%=:\n\t"
        "s_cmp_eq_u32 %[scn], 0\n\t"
        "s_cbranch_scc1 Ld80_%=\n\t"
        "s_sub_u32 %[scn], %[scn], 1\n\t"
        "buffer_load_dwordx4 v[70:73], v108, %[ssrd], %[sob] offen offset:1024 sc1\n\t"
        "s_waitcnt vmcnt(0)\n\t"
        "v_max3_u32 v17, v70, v71, v72\n\t"
        "v_max_u32 v17, v17, v73\n\t"
        "v_cmp_eq_u32 vcc, -1, v17\n\t"
        "s_cbranch_vccnz Lx80_%=\n\t"
        "s_branch Ld80_%=\n\t"
        "Lws00_%=:\n\t"
        "s_cmp_eq_u32 %[scn], 0\n\t"
        "s_cbranch_scc1 Lwg00_%=\n\t"
        "s_sub_u32 %[scn], %[scn], 1\n\t"
        "s_sleep 1\n\t"
        "ds_read_b32 v16, v15 offset:56\n\t"
        "s_waitcnt lgkmcnt(0)\n\t"
        "s_branch Lwc00_%=\n\t"
        "Lcw_%=:\n\t"
        "s_cmp_eq_u32 %[scn], 0\n\t"
        "s_cbranch_scc1 Lcg_%=\n\t"
        "s_sub_u32 %[scn], %[scn], 1\n\t"
        "ds_read_b32 v13, v12 offset:112\n\t"
        "s_waitcnt lgkmcnt(0)\n\t"
        "v_readfirstlane_b32 %[sc], v13\n\t"
        "s_branch Lc_%=\n\t"
        "Lk0_%=:\n\t"
        "buffer_load_dwordx4 v[40:43], v107, %[ssrd], %[sob] offen offset:0 sc1\n\t"
        "v_lshlrev_b32 v20, 16, v88\n\t"
        "v_and_b32 v24, 0xffff0000, v88\n\t"
        "buffer_load_dwordx4 v[44:47], v107, %[ssrd], %[sob] offen offset:1024 sc1\n\t"
        "v_lshlrev_b32 v21, 16, v89\n\t"
        "v_and_b32 v25, 0xffff0000, v89\n\t"
        "buffer_load_dwordx4 v[48:51], v107, %[ssrd], %[sob] offen offset:2048 sc1\n\t"
        "v_lshlrev_b32 v22, 16, v90\n\t"
        "v_and_b32 v26, 0xffff0000, v90\n\t"
        "buffer_load_dwordx4 v[52:55], v107, %[ssrd], %[sob] offen offset:3072 sc1\n\t"
        "v_lshlrev_b32 v23, 16, v91\n\t"
        "v_and_b32 v27, 0xffff0000, v91\n\t"
        "buffer_load_dwordx4 v[56:59], v107, %[ssrd], %[sob4] offen offset:0 sc1\n\t"
        "v_lshlrev_b32 v28, 16, v92\n\t"
        "v_and_b32 v32, 0xffff0000, v92\n\t"
        "buffer_load_dwordx4 v[60:63], v107, %[ssrd], %[sob4] offen offset:1024 sc1\n\t"
        "v_lshlrev_b32 v29, 16, v93\n\t"
        "v_and_b32 v33, 0xffff0000, v93\n\t"
        "buffer_load_dwordx4 v[64:67], v107, %[ssrd], %[sob4] offen offset:2048 sc1\n\t"
        "v_lshlrev_b32 v30, 16, v94\n\t"
        "v_and_b32 v34, 0xffff0000, v94\n\t"
        "buffer_load_dwordx4 v[6:9], v108, %[ssrd], %[sob] offen offset:0 sc1\n\t"
        "v_lshlrev_b32 v31, 16, v95\n\t"
        "v_and_b32 v35, 0xffff0000, v95\n\t"
        "buffer_load_dwordx4 v[70:73], v108, %[ssrd], %[sob] offen offset:1024 sc1\n\t"
        "v_lshlrev_b32 v36, 16, v96\n\t"
        "v_and_b32 v37, 0xffff0000, v96\n\t"
        "v_lshlrev_b32 v38, 16, v97\n\t"
        "v_and_b32 v39, 0xffff0000, v97\n\t"
        "v_mov_b32 v15, 153984\n\t"
        "v_mov_b32 v16, 1\n\t"
        "v_add_u32 v18, 33536, v107\n\t"
        "v_add_u32 v14, 33536, v106\n\t"
        "ds_read_b128 v[76:79], v18 offset:0\n\t"
        "ds_read_b128 v[80:83], v18 offset:1024\n\t"
        "ds_read_b128 v[84:87], v18 offset:2048\n\t"
        "ds_read_b128 v[88:91], v18 offset:3072\n\t"
        "ds_read_b128 v[92:95], v14 offset:0\n\t"
        "ds_read_b128 v[96:99], v18 offset:4608\n\t"
        "s_waitcnt vmcnt(9)\n\t"
        "s_mov_b64 exec, 1\n\t"
        "ds_add_u32 v15, v16 offset:56\n\t"
        "s_mov_b64 exec, -1\n\t"
        "s_waitcnt vmcnt(8)\n\t"
        "v_max3_u32 v17, v40, v41, v42\n\t"
        "v_max_u32 v17, v17, v43\n\t"
        "v_cmp_eq_u32 vcc, -1, v17\n\t"
        "s_cbranch_vccnz Lx00_%=\n\t"
        "Ld00_%=:\n\t"
        "s_waitcnt lgkmcnt(6)\n\t"
        "v_mfma_f32_16x16x32_bf16 v[20:23], v[76:79], v[40:43], v[20:23]\n\t"
        "ds_read_b128 v[76:79], v18 offset:5632\n\t"
        "s_waitcnt lgkmcnt(6)\n\t"
        "v_mfma_f32_16x16x32_bf16 v[24:27], v[80:83], v[40:43], v[24:27]\n\t"
        "ds_read_b128 v[80:83], v18 offset:6656\n\t"
        "s_waitcnt lgkmcnt(6)\n\t"
        "v_mfma_f32_16x16x32_bf16 v[28:31], v[84:87], v[40:43], v[28:31]\n\t"
        "s_waitcnt vmcnt(7)\n\t"
        "v_max3_u32 v17, v44, v45, v46\n\t"
        "v_max_u32 v17, v17, v47\n\t"
        "v_cmp_eq_u32 vcc, -1, v17\n\t"
        "ds_read_b128 v[84:87], v18 offset:7680\n\t"
        "s_waitcnt lgkmcnt(6)\n\t"
        "v_mfma_f32_16x16x32_bf16 v[32:35], v[88:91], v[40:43], v[32:35]\n\t"
        "ds_read_b128 v[88:91], v14 offset:4608\n\t"
        "s_waitcnt lgkmcnt(6)\n\t"
        "v_mfma_f32_16x16x32_bf16 v[36:39], v[92:95], v[40:43], v[36:39]\n\t"
        "s_cbranch_vccnz Lx10_%=\n\t"
        "Ld10_%=:\n\t"
        "ds_read_b32 v16, v15 offset:56\n\t"
        "ds_read_b128 v[92:95], v18 offset:9216\n\t"
        "ds_read_b128 v[40:43], v18 offset:10240\n\t"
        "s_waitcnt lgkmcnt(8)\n\t"
        "v_mfma_f32_16x16x32_bf16 v[20:23], v[96:99], v[44:47], v[20:23]\n\t"
        "ds_read_b128 v[96:99], v18 offset:11264\n\t"
        "s_waitcnt lgkmcnt(7)\n\t"
        "v_mfma_f32_16x16x32_bf16 v[24:27], v[76:79], v[44:47], v[24:27]\n\t"
        "ds_read_b128 v[76:79], v18 offset:12288\n\t"
        "s_waitcnt lgkmcnt(7)\n\t"
        "v_mfma_f32_16x16x32_bf16 v[28:31], v[80:83], v[44:47], v[28:31]\n\t"
        "s_waitcnt vmcnt(6)\n\t"
        "v_max3_u32 v17, v48, v49, v50\n\t"
        "v_max_u32 v17, v17, v51\n\t"
        "v_cmp_eq_u32 vcc, -1, v17\n\t"
        "ds_read_b128 v[80:83], v14 offset:9216\n\t"
        "s_waitcnt lgkmcnt(7)\n\t"
        "v_mfma_f32_16x16x32_bf16 v[32:35], v[84:87], v[44:47], v[32:35]\n\t"
        "s_waitcnt lgkmcnt(5)\n\t"
        "Lwc00_%=:\n\t"
        "v_readfirstlane_b32 %[sob4x], v16\n\t"
        "s_cmp_lt_u32 %[sob4x], %[swt]\n\t"
        "s_cbranch_scc1 Lws00_%=\n\t"
        "Lwg00_%=:\n\t"
        "ds_read_b128 v[84:87], v18 offset:13824\n\t"
        "v_mfma_f32_16x16x32_bf16 v[36:39], v[88:91], v[44:47], v[36:39]\n\t"
        "s_cbranch_vccnz Lx20_%=\n\t"
        "Ld20_%=:\n\t"
        "ds_read_b128 v[88:91], v18 offset:14848\n\t"
        "ds_read_b128 v[44:47], v18 offset:15872\n\t"
        "s_waitcnt lgkmcnt(7)\n\t"
        "v_mfma_f32_16x16x32_bf16 v[20:23], v[92:95], v[48:51], v[20:23]\n\t"
        "ds_read_b128 v[92:95], v18 offset:16896\n\t"
        "s_waitcnt lgkmcnt(7)\n\t"
        "v_mfma_f32_16x16x32_bf16 v[24:27], v[40:43], v[48:51], v[24:27]\n\t"
        "ds_read_b128 v[40:43], v14 offset:13824\n\t"
        "s_waitcnt lgkmcnt(7)\n\t"
        "v_mfma_f32_16x16x32_bf16 v[28:31], v[96:99], v[48:51], v[28:31]\n\t"
        "s_waitcnt vmcnt(5)\n\t"
        "v_max3_u32 v17, v52, v53, v54\n\t"
        "v_max_u32 v17, v17, v55\n\t"
        "v_cmp_eq_u32 vcc, -1, v17\n\t"
        "ds_read_b128 v[96:99], v18 offset:18432\n\t"
        "s_waitcnt lgkmcnt(7)\n\t"
        "v_mfma_f32_16x16x32_bf16 v[32:35], v[76:79], v[48:51], v[32:35]\n\t"
        "ds_read_b128 v[76:79], v18 offset:19456\n\t"
        "s_waitcnt lgkmcnt(7)\n\t"
        "v_mfma_f32_16x16x32_bf16 v[36:39], v[80:83], v[48:51], v[36:39]\n\t"
        "s_cbranch_vccnz Lx30_%=\n\t"
        "Ld30_%=:\n\t"
        "ds_read_b128 v[80:83], v18 offset:20480\n\t"
        "ds_read_b128 v[48:51], v18 offset:21504\n\t"
        "s_waitcnt lgkmcnt(8)\n\t"
        "v_mfma_f32_16x16x32_bf16 v[20:23], v[84:87], v[52:55], v[20:23]\n\t"
        "ds_read_b128 v[84:87], v14 offset:18432\n\t"
        "s_waitcnt lgkmcnt(8)\n\t"
        "v_mfma_f32_16x16x32_bf16 v[24:27], v[88:91], v[52:55], v[24:27]\n\t"
        "ds_read_b128 v[88:91], v18 offset:23040\n\t"
        "s_waitcnt lgkmcnt(8)\n\t"
        "v_mfma_f32_16x16x32_bf16 v[28:31], v[44:47], v[52:55], v[28:31]\n\t"
        "s_waitcnt vmcnt(4)\n\t"
        "v_max3_u32 v17, v56, v57, v58\n\t"
        "v_max_u32 v17, v17, v59\n\t"
        "v_cmp_eq_u32 vcc, -1, v17\n\t"
        "ds_read_b128 v[44:47], v18 offset:24064\n\t"
        "s_waitcnt lgkmcnt(8)\n\t"
        "v_mfma_f32_16x16x32_bf16 v[32:35], v[92:95], v[52:55], v[32:35]\n\t"
        "ds_read_b128 v[92:95], v18 offset:25088\n\t"
        "s_waitcnt lgkmcnt(8)\n\t"
        "v_mfma_f32_16x16x32_bf16 v[36:39], v[40:43], v[52:55], v[36:39]\n\t"
        "s_cbranch_vccnz Lx40_%=\n\t"
        "Ld40_%=:\n\t"
        "ds_read_b128 v[40:43], v18 offset:26112\n\t"
        "s_waitcnt lgkmcnt(8)\n\t"
        "v_mfma_f32_16x16x32_bf16 v[20:23], v[96:99], v[56:59], v[20:23]\n\t"
        "ds_read_b128 v[52:55], v14 offset:23040\n\t"
        "s_waitcnt lgkmcnt(8)\n\t"
        "v_mfma_f32_16x16x32_bf16 v[24:27], v[76:79], v[56:59], v[24:27]\n\t"
        "ds_read_b128 v[96:99], v18 offset:27648\n\t"
        "s_waitcnt lgkmcnt(8)\n\t"
        "v_mfma_f32_16x16x32_bf16 v[28:31], v[80:83], v[56:59], v[28:31]\n\t"
        "s_waitcnt vmcnt(3)\n\t"
        "v_max3_u32 v17, v60, v61, v62\n\t"
        "v_max_u32 v17, v17, v63\n\t"
        "v_cmp_eq_u32 vcc, -1, v17\n\t"
        "ds_read_b128 v[76:79], v18 offset:28672\n\t"
        "s_waitcnt lgkmcnt(8)\n\t"
        "v_mfma_f32_16x16x32_bf16 v[32:35], v[48:51], v[56:59], v[32:35]\n\t"
        "ds_read_b128 v[80:83], v18 offset:29696\n\t"
        "s_waitcnt lgkmcnt(8)\n\t"
        "v_mfma_f32_16x16x32_bf16 v[36:39], v[84:87], v[56:59], v[36:39]\n\t"
        "s_cbranch_vccnz Lx50_%=\n\t"
        "Ld50_%=:\n\t"
        "ds_read_b128 v[48:51], v18 offset:30720\n\t"
        "s_waitcnt lgkmcnt(8)\n\t"
        "v_mfma_f32_16x16x32_bf16 v[20:23], v[88:91], v[60:63], v[20:23]\n\t"
        "ds_read_b128 v[84:87], v14 offset:27648\n\t"
        "s_waitcnt lgkmcnt(8)\n\t"
        "v_mfma_f32_16x16x32_bf16 v[24:27], v[44:47], v[60:63], v[24:27]\n\t"
        "ds_read_b128 v[56:59], v18 offset:32256\n\t"
        "s_waitcnt lgkmcnt(8)\n\t"
        "v_mfma_f32_16x16x32_bf16 v[28:31], v[92:95], v[60:63], v[28:31]\n\t"
        "s_waitcnt vmcnt(2)\n\t"
        "v_max3_u32 v17, v64, v65, v66\n\t"
        "v_max_u32 v17, v17, v67\n\t"
        "v_cmp_eq_u32 vcc, -1, v17\n\t"
        "ds_read_b128 v[88:91], v18 offset:33280\n\t"
        "s_waitcnt lgkmcnt(8)\n\t"
        "v_mfma_f32_16x16x32_bf16 v[32:35], v[40:43], v[60:63], v[32:35]\n\t"
        "ds_read_b128 v[44:47], v18 offset:34304\n\t"
        "s_waitcnt lgkmcnt(8)\n\t"
        "v_mfma_f32_16x16x32_bf16 v[36:39], v[52:55], v[60:63], v[36:39]\n\t"
        "s_cbranch_vccnz Lx60_%=\n\t"
        "Ld60_%=:\n\t"
        "ds_read_b128 v[92:95], v18 offset:35328\n\t"
        "s_waitcnt lgkmcnt(8)\n\t"
        "v_mfma_f32_16x16x32_bf16 v[20:23], v[96:99], v[64:67], v[20:23]\n\t"
        "ds_read_b128 v[40:43], v14 offset:32256\n\t"
        "s_waitcnt lgkmcnt(8)\n\t"
        "v_mfma_f32_16x16x32_bf16 v[24:27], v[76:79], v[64:67], v[24:27]\n\t"
        "ds_read_b128 v[52:55], v18 offset:36864\n\t"
        "s_waitcnt lgkmcnt(8)\n\t"
        "v_mfma_f32_16x16x32_bf16 v[28:31], v[80:83], v[64:67], v[28:31]\n\t"
        "s_waitcnt vmcnt(1)\n\t"
        "v_max3_u32 v17, v6, v7, v8\n\t"
        "v_max_u32 v17, v17, v9\n\t"
        "v_cmp_eq_u32 vcc, -1, v17\n\t"
        "ds_read_b128 v[60:63], v18 offset:37888\n\t"
        "s_waitcnt lgkmcnt(8)\n\t"
        "v_mfma_f32_16x16x32_bf16 v[32:35], v[48:51], v[64:67], v[32:35]\n\t"
        "ds_read_b128 v[96:99], v18 offset:38912\n\t"
        "s_waitcnt lgkmcnt(8)\n\t"
        "v_mfma_f32_16x16x32_bf16 v[36:39], v[84:87], v[64:67], v[36:39]\n\t"
        "s_cbranch_vccnz Lx70_%=\n\t"
        "Ld70_%=:\n\t"
        "ds_read_b128 v[76:79], v18 offset:39936\n\t"
        "s_waitcnt lgkmcnt(8)\n\t"
        "v_mfma_f32_16x16x32_bf16 v[20:23], v[56:59], v[6:9], v[20:23]\n\t"
        "ds_read_b128 v[80:83], v14 offset:36864\n\t"
        "s_waitcnt lgkmcnt(8)\n\t"
        "v_mfma_f32_16x16x32_bf16 v[24:27], v[88:91], v[6:9], v[24:27]\n\t"
        "s_waitcnt lgkmcnt(7)\n\t"
        "v_mfma_f32_16x16x32_bf16 v[28:31], v[44:47], v[6:9], v[28:31]\n\t"
        "s_waitcnt vmcnt(0)\n\t"
        "v_max3_u32 v17, v70, v71, v72\n\t"
        "v_max_u32 v17, v17, v73\n\t"
        "v_cmp_eq_u32 vcc, -1, v17\n\t"
        "s_waitcnt lgkmcnt(6)\n\t"
        "v_mfma_f32_16x16x32_bf16 v[32:35], v[92:95], v[6:9], v[32:35]\n\t"
        "s_waitcnt lgkmcnt(5)\n\t"
        "v_mfma_f32_16x16x32_bf16 v[36:39], v[40:43], v[6:9], v[36:39]\n\t"
        "s_cbranch_vccnz Lx80_%=\n\t"
        "Ld80_%=:\n\t"
        "s_waitcnt lgkmcnt(4)\n\t"
        "v_mfma_f32_16x16x32_bf16 v[20:23], v[52:55], v[70:73], v[20:23]\n\t"
        "s_waitcnt lgkmcnt(3)\n\t"
        "v_mfma_f32_16x16x32_bf16 v[24:27], v[60:63], v[70:73], v[24:27]\n\t"
        "v_add_u32 v18, %[sscr], v107\n\t"
        "s_waitcnt lgkmcnt(2)\n\t"
        "v_mfma_f32_16x16x32_bf16 v[28:31], v[96:99], v[70:73], v[28:31]\n\t"
        "s_waitcnt lgkmcnt(1)\n\t"
        "v_mfma_f32_16x16x32_bf16 v[32:35], v[76:79], v[70:73], v[32:35]\n\t"
        "s_waitcnt lgkmcnt(0)\n\t"
        "v_mfma_f32_16x16x32_bf16 v[36:39], v[80:83], v[70:73], v[36:39]\n\t"
        "s_lshr_b32 %[sc], %[sstat], 9\n\t"
        "s_and_b32 %[sc], %[sc], 12\n\t"
        "v_add_u32 v12, %[sc], v15\n\t"
        "Lp_%=:\n\t"
        "ds_read_b32 v16, v15 offset:24\n\t"
        "ds_read_b128 v[52:55], v18 offset:0\n\t"
        "ds_read_b128 v[56:59], v18 offset:1024\n\t"
        "ds_read_b128 v[60:63], v18 offset:2048\n\t"
        "ds_read_b128 v[64:67], v18 offset:3072\n\t"
        "ds_read_b128 v[68:71], v18 offset:4096\n\t"
        "v_lshrrev_b32 v19, 8, v107\n\t"
        "v_mov_b32 v17, 163232\n\t"
        "v_lshl_add_u32 v19, v19, 4, v17\n\t"
        "v_add_u32 v17, 192, v17\n\t"
        "s_mov_b32 vcc_lo, 0x22222222\n\t"
        "s_mov_b32 vcc_hi, 0x22222222\n\t"
        "v_cndmask_b32 v19, v17, v19, vcc\n\t"
        "ds_read_b128 v[40:43], v19 offset:0\n\t"
        "ds_read_b128 v[44:47], v19 offset:64\n\t"
        "ds_read_b128 v[48:51], v19 offset:128\n\t"
        "ds_read_b32 v13, v12 offset:112\n\t"
        "v_mov_b32 v98, 0\n\t"
        "v_mov_b32 v99, 0\n\t"
        "v_and_b32 v84, 0xf0, v107\n\t"
        "v_mov_b32 v83, 0\n\t"
        "s_waitcnt lgkmcnt(9)\n\t"
        "v_readfirstlane_b32 %[sob4x], v16\n\t"
        "s_cmp_ge_u32 %[sob4x], %[swt]\n\t"
        "s_cbranch_scc1 Lq_%=\n\t"
        "s_cmp_eq_u32 %[scn], 0\n\t"
        "s_cbranch_scc1 Lq_%=\n\t"
        "s_sub_u32 %[scn], %[scn], 1\n\t"
        "s_waitcnt lgkmcnt(0)\n\t"
        "s_branch Lp_%=\n\t"
        "Lq_%=:\n\t"
        "s_waitcnt lgkmcnt(7)\n\t"
        "v_add_f32 v20, v20, v52\n\t"
        "v_add_f32 v21, v21, v53\n\t"
        "v_add_f32 v22, v22, v54\n\t"
        "v_add_f32 v23, v23, v55\n\t"
        "v_add_f32 v24, v24, v56\n\t"
        "v_add_f32 v25, v25, v57\n\t"
        "v_add_f32 v26, v26, v58\n\t"
        "v_add_f32 v27, v27, v59\n\t"
        "v_cvt_pk_bf16_f32 v88, v20, v24\n\t"
        "v_cvt_pk_bf16_f32 v89, v21, v25\n\t"
        "v_cvt_pk_bf16_f32 v90, v22, v26\n\t"
        "v_cvt_pk_bf16_f32 v91, v23, v27\n\t"
        "s_waitcnt lgkmcnt(5)\n\t"
        "v_add_f32 v28, v28, v60\n\t"
        "v_add_f32 v29, v29, v61\n\t"
        "v_add_f32 v30, v30, v62\n\t"
        "v_add_f32 v31, v31, v63\n\t"
        "v_add_f32 v32, v32, v64\n\t"
        "v_add_f32 v33, v33, v65\n\t"
        "v_add_f32 v34, v34, v66\n\t"
        "v_add_f32 v35, v35, v67\n\t"
        "v_cvt_pk_bf16_f32 v92, v28, v32\n\t"
        "v_cvt_pk_bf16_f32 v93, v29, v33\n\t"
        "v_cvt_pk_bf16_f32 v94, v30, v34\n\t"
        "v_cvt_pk_bf16_f32 v95, v31, v35\n\t"
        "s_waitcnt lgkmcnt(0)\n\t"
        "v_mfma_f32_16x16x32_bf16 v[72:75], v[40:43], v[88:91], 0\n\t"
        "v_mfma_f32_16x16x32_bf16 v[76:79], v[88:91], v[88:91], 0\n\t"
        "s_waitcnt lgkmcnt(4)\n\t"
        "v_add_f32 v36, v36, v68\n\t"
        "v_add_f32 v37, v37, v69\n\t"
        "v_add_f32 v38, v38, v70\n\t"
        "v_add_f32 v39, v39, v71\n\t"
        "v_cvt_pk_bf16_f32 v96, v36, v37\n\t"
        "v_cvt_pk_bf16_f32 v97, v38, v39\n\t"
        "s_mov_b32 exec_lo, 0\n\t"
        "v_mov_b32 v96, 0\n\t"
        "v_mov_b32 v97, 0\n\t"
        "s_mov_b32 exec_lo, -1\n\t"
        "v_mfma_f32_16x16x32_bf16 v[72:75], v[44:47], v[92:95], v[72:75]\n\t"
        "v_mfma_f32_16x16x32_bf16 v[76:79], v[92:95], v[92:95], v[76:79]\n\t"
        "v_mfma_f32_16x16x32_bf16 v[72:75], v[48:51], v[96:99], v[72:75]\n\t"
        "v_mfma_f32_16x16x32_bf16 v[76:79], v[96:99], v[96:99], v[76:79]\n\t"
        "v_readfirstlane_b32 %[sc], v13\n\t"
        "v_and_b32 v17, 0x300, v107\n\t"
        "v_lshrrev_b32 v14, 5, v17\n\t"
        "v_lshrrev_b32 v18, 4, v17\n\t"
        "v_add_u32 v19, v14, v84\n\t"
        "v_min_u32 v88, -2, v88\n\t"
        "v_min_u32 v89, -2, v89\n\t"
        "v_min_u32 v90, -2, v90\n\t"
        "v_min_u32 v91, -2, v91\n\t"
        "v_min_u32 v92, -2, v92\n\t"
        "v_min_u32 v93, -2, v93\n\t"
        "v_min_u32 v94, -2, v94\n\t"
        "v_min_u32 v95, -2, v95\n\t"
        "v_min_u32 v96, -2, v96\n\t"
        "v_min_u32 v97, -2, v97\n\t"
        "Lc_%=:\n\t"
        "s_lshr_b32 %[sob4x], %[sc], 6\n\t"
        "s_cmp_lt_u32 %[sob4x], %[swt]\n\t"
        "s_cbranch_scc1 Lcw_%=\n\t"
        "Lcg_%=:\n\t"
        "s_andn2_b32 %[sob4], 15, %[sc]\n\t"
        "s_lshl_b32 exec_lo, 0x10001, %[sob4]\n\t"
        "s_mov_b32 exec_hi, exec_lo\n\t"
        "buffer_store_dwordx4 v[88:91], v18, %[ssrd], %[scr] offen sc1\n\t"
        "buffer_store_dwordx4 v[92:95], v18, %[ssrd], %[scr] offen offset:64 sc1\n\t"
        "s_mov_b32 exec_hi, 0\n\t"
        "buffer_store_dwordx2 v[96:97], v14, %[ssrd], %[scr] offen offset:128 sc1\n\t"
        "s_mov_b64 exec, -1\n\t"
        "v_min_u32 v80, -2, v72\n\t"
        "v_min_u32 v82, -2, v73\n\t"
        "v_cndmask_b32 v76, v76, v77, s[92:93]\n\t"
        "v_cndmask_b32 v78, v78, v79, s[92:93]\n\t"
        "v_cndmask_b32 v76, v76, v78, s[94:95]\n\t"
        "v_min_u32 v81, -2, v76\n\t"
        "v_mov_b32 v16, 1\n\t"
        "s_mov_b32 exec_lo, 0xf0000f\n\t"
        "s_mov_b32 exec_hi, 0xf0000f00\n\t"
        "buffer_store_dwordx4 v[80:83], v84, %[ssrd], %[sstat] offen sc1\n\t"
        "s_mov_b64 exec, 1\n\t"
        "ds_add_u32 v15, v16 offset:12\n\t"
        "s_mov_b64 exec, -1\n\t"
        "s_cmp_lg_u32 %[swt], 4\n\t"
        "s_cbranch_scc1 Lx_%=\n\t"
        "buffer_store_dwordx4 v[88:91], v107, %[ssrd], %[sx1] offen sc1\n\t"
        "buffer_store_dwordx4 v[92:95], v107, %[ssrd], %[sx1] offen offset:1024 sc1\n\t"
        "s_mov_b32 exec_hi, 0\n\t"
        "buffer_store_dwordx2 v[96:97], v19, %[ssrd], %[sx1] offen offset:2048 sc1\n\t"
        "s_mov_b32 exec_hi, -1\n\t"
        "Lx_%=:\n\t"
        "s_cmp_lg_u32 %[swt], 4\n\t"
        "s_cbranch_scc1 Lend_%=\n\t"
        "v_mov_b32 v17, %[stag]\n\t"
        "s_mov_b64 exec, 1\n\t"
        "s_waitcnt vmcnt(0)\n\t"
        "buffer_store_dword v17, off, %[ssrd], %[sxf] sc1\n\t"
        "s_mov_b64 exec, -1\n\t"
        "Lend_%=:\n\t"
        "Lend2_%=:\n\t"
        : [sob4] "=&s"(ob4), [sob4x] "=&s"(ob4x), [sc] "=&s"(cd), [scn] "+s"(cn)
        : [ssrd] "s"(a.srd), [sst] "s"(a.st), [stt] "s"(a.tt), [sc0] "s"(a.c0), [swx] "s"(a.wx), [sldx] "s"(a.ldx), [sskip] "s"(a.skip), [smsk] "s"(a.msk),
          [sob] "s"(a.ob), [swi] "s"(a.wi), [sld] "s"(a.ld), [sscr] "s"(a.scr), [sstat] "s"(a.stat), [sx1] "s"(a.x1), [scr] "s"(a.cr), [sxf] "s"(a.xf),
          [swt] "s"(wt), [stag] "s"(tag), [sgb] "s"(gb), [sgl] "s"(gl)
        : "memory", "scc", "vcc", "s92", "s93", "s94", "s95", "s96", "s97", "s98", "s99", "v2", "v3", "v4", "v5", "v6", "v7", "v8", "v9", "v10", "v11", "v12", "v13", "v14", "v15", "v16", "v17", "v18", "v19", "v20", "v21", "v22", "v23", "v24", "v25", "v26", "v27", "v28", "v29", "v30", "v31", "v32", "v33", "v34", "v35", "v36", "v37", "v38", "v39", "v40", "v41", "v42", "v43", "v44", "v45", "v46", "v47");
}

DEV void dload(u32x4 srd, u32 a2, u32 so) {
    asm volatile(
        "s_nop 4\n\t"
        "buffer_load_dwordx4 v[112:115], v107, %[ssrd], %[so] offen offset:0" NTD "\n\t"
        "buffer_load_dwordx4 v[116:119], v107, %[ssrd], %[so] offen offset:1152" NTD "\n\t"
        "buffer_load_dwordx4 v[120:123], v107, %[ssrd], %[so] offen offset:2304" NTD "\n\t"
        "buffer_load_ushort v220, %[a2], %[ssrd], %[so] offen offset:1024" NTD "\n\t"
        "buffer_load_ushort v221, %[a2], %[ssrd], %[so] offen offset:2176" NTD "\n\t"
        "buffer_load_ushort v222, %[a2], %[ssrd], %[so] offen offset:3328" NTD "\n\t"
        "s_add_u32 %[so], %[so], 3456\n\t"
        "buffer_load_dwordx4 v[124:127], v107, %[ssrd], %[so] offen offset:0" NTD "\n\t"
        "buffer_load_dwordx4 v[128:131], v107, %[ssrd], %[so] offen offset:1152" NTD "\n\t"
        "buffer_load_dwordx4 v[132:135], v107, %[ssrd], %[so] offen offset:2304" NTD "\n\t"
        "buffer_load_ushort v223, %[a2], %[ssrd], %[so] offen offset:1024" NTD "\n\t"
        "buffer_load_ushort v224, %[a2], %[ssrd], %[so] offen offset:2176" NTD "\n\t"
        "buffer_load_ushort v225, %[a2], %[ssrd], %[so] offen offset:3328" NTD "\n\t"
        "s_add_u32 %[so], %[so], 3456\n\t"
        "buffer_load_dwordx4 v[136:139], v107, %[ssrd], %[so] offen offset:0" NTD "\n\t"
        "buffer_load_dwordx4 v[140:143], v107, %[ssrd], %[so] offen offset:1152" NTD "\n\t"
        "buffer_load_dwordx4 v[144:147], v107, %[ssrd], %[so] offen offset:2304" NTD "\n\t"
        "buffer_load_ushort v226, %[a2], %[ssrd], %[so] offen offset:1024" NTD "\n\t"
        "buffer_load_ushort v227, %[a2], %[ssrd], %[so] offen offset:2176" NTD "\n\t"
        "buffer_load_ushort v228, %[a2], %[ssrd], %[so] offen offset:3328" NTD "\n\t"
        "s_add_u32 %[so], %[so], 3456\n\t"
        "buffer_load_dwordx4 v[148:151], v107, %[ssrd], %[so] offen offset:0" NTD "\n\t"
        "buffer_load_dwordx4 v[152:155], v107, %[ssrd], %[so] offen offset:1152" NTD "\n\t"
        "buffer_load_dwordx4 v[156:159], v107, %[ssrd], %[so] offen offset:2304" NTD "\n\t"
        "buffer_load_ushort v229, %[a2], %[ssrd], %[so] offen offset:1024" NTD "\n\t"
        "buffer_load_ushort v230, %[a2], %[ssrd], %[so] offen offset:2176" NTD "\n\t"
        "buffer_load_ushort v231, %[a2], %[ssrd], %[so] offen offset:3328" NTD "\n\t"
        "s_add_u32 %[so], %[so], 3456\n\t"
        "buffer_load_dwordx4 v[160:163], v107, %[ssrd], %[so] offen offset:0" NTD "\n\t"
        "buffer_load_dwordx4 v[164:167], v107, %[ssrd], %[so] offen offset:1152" NTD "\n\t"
        "buffer_load_dwordx4 v[168:171], v107, %[ssrd], %[so] offen offset:2304" NTD "\n\t"
        "buffer_load_ushort v232, %[a2], %[ssrd], %[so] offen offset:1024" NTD "\n\t"
        "buffer_load_ushort v233, %[a2], %[ssrd], %[so] offen offset:2176" NTD "\n\t"
        "buffer_load_ushort v234, %[a2], %[ssrd], %[so] offen offset:3328" NTD "\n\t"
        "s_add_u32 %[so], %[so], 3456\n\t"
        "buffer_load_dwordx4 v[172:175], v107, %[ssrd], %[so] offen offset:0" NTD "\n\t"
        "buffer_load_dwordx4 v[176:179], v107, %[ssrd], %[so] offen offset:1152" NTD "\n\t"
        "buffer_load_dwordx4 v[180:183], v107, %[ssrd], %[so] offen offset:2304" NTD "\n\t"
        "buffer_load_ushort v235, %[a2], %[ssrd], %[so] offen offset:1024" NTD "\n\t"
        "buffer_load_ushort v236, %[a2], %[ssrd], %[so] offen offset:2176" NTD "\n\t"
        "buffer_load_ushort v237, %[a2], %[ssrd], %[so] offen offset:3328" NTD "\n\t"
        "s_add_u32 %[so], %[so], 3456\n\t"
        "buffer_load_dwordx4 v[184:187], v107, %[ssrd], %[so] offen offset:0" NTD "\n\t"
        "buffer_load_dwordx4 v[188:191], v107, %[ssrd], %[so] offen offset:1152" NTD "\n\t"
        "buffer_load_dwordx4 v[192:195], v107, %[ssrd], %[so] offen offset:2304" NTD "\n\t"
        "buffer_load_ushort v238, %[a2], %[ssrd], %[so] offen offset:1024" NTD "\n\t"
        "buffer_load_ushort v239, %[a2], %[ssrd], %[so] offen offset:2176" NTD "\n\t"
        "buffer_load_ushort v240, %[a2], %[ssrd], %[so] offen offset:3328" NTD "\n\t"
        "s_add_u32 %[so], %[so], 3456\n\t"
        "buffer_load_dwordx4 v[196:199], v107, %[ssrd], %[so] offen offset:0" NTD "\n\t"
        "buffer_load_dwordx4 v[200:203], v107, %[ssrd], %[so] offen offset:1152" NTD "\n\t"
        "buffer_load_dwordx4 v[204:207], v107, %[ssrd], %[so] offen offset:2304" NTD "\n\t"
        "buffer_load_ushort v241, %[a2], %[ssrd], %[so] offen offset:1024" NTD "\n\t"
        "buffer_load_ushort v242, %[a2], %[ssrd], %[so] offen offset:2176" NTD "\n\t"
        "buffer_load_ushort v243, %[a2], %[ssrd], %[so] offen offset:3328" NTD "\n\t"
        "s_add_u32 %[so], %[so], 3456\n\t"
        : [so] "+s"(so) : [ssrd] "s"(srd), [a2] "v"(a2) : "memory", "scc");
}

DEV void xstats(int xa, float (&sx)[2], float (&sq)[2]) {
    int xb; u32 sc;
    asm volatile(
        "v_mov_b32 v24, 0x3f803f80\n\t"
        "v_mov_b32 v25, 0x3f803f80\n\t"
        "v_mov_b32 v26, 0x3f803f80\n\t"
        "v_mov_b32 v27, 0x3f803f80\n\t"
        "v_mov_b32 v48, 0\n\t"
        "v_mov_b32 v49, 0\n\t"
        "v_mov_b32 v50, 0\n\t"
        "v_mov_b32 v51, 0\n\t"
        "v_mov_b32 v52, 0\n\t"
        "v_mov_b32 v53, 0\n\t"
        "v_mov_b32 v54, 0\n\t"
        "v_mov_b32 v55, 0\n\t"
        "v_mov_b32 v56, 0\n\t"
        "v_mov_b32 v57, 0\n\t"
        "v_mov_b32 v58, 0\n\t"
        "v_mov_b32 v59, 0\n\t"
        "v_mov_b32 v60, 0\n\t"
        "v_mov_b32 v61, 0\n\t"
        "v_mov_b32 v62, 0\n\t"
        "v_mov_b32 v63, 0\n\t"
        "v_xor_b32 %[xb], 64, %[xa]\n\t"
        "s_mov_b32 %[sc], 3\n\t"
        "Ll_%=:\n\t"
        "ds_read_b128 v[208:211], %[xa] offset:0\n\t"
        "ds_read_b128 v[212:215], %[xa] offset:6144\n\t"
        "ds_read_b128 v[216:219], %[xb] offset:0\n\t"
        "ds_read_b128 v[220:223], %[xb] offset:6144\n\t"
        "ds_read_b128 v[224:227], %[xa] offset:10240\n\t"
        "ds_read_b128 v[228:231], %[xa] offset:16384\n\t"
        "ds_read_b128 v[232:235], %[xb] offset:10240\n\t"
        "ds_read_b128 v[236:239], %[xb] offset:16384\n\t"
        "ds_read_b128 v[240:243], %[xa] offset:20480\n\t"
        "ds_read_b128 v[244:247], %[xa] offset:26624\n\t"
        "ds_read_b128 v[248:251], %[xb] offset:20480\n\t"
        "ds_read_b128 v[252:255], %[xb] offset:26624\n\t"
        "v_add_u32 %[xa], 30720, %[xa]\n\t"
        "v_add_u32 %[xb], 30720, %[xb]\n\t"
        "s_waitcnt lgkmcnt(11)\n\t"
        "v_mfma_f32_16x16x32_bf16 v[48:51], v[24:27], v[208:211], v[48:51]\n\t"
        "v_mfma_f32_16x16x32_bf16 v[56:59], v[208:211], v[208:211], v[56:59]\n\t"
        "s_waitcnt lgkmcnt(10)\n\t"
        "v_mfma_f32_16x16x32_bf16 v[52:55], v[24:27], v[212:215], v[52:55]\n\t"
        "v_mfma_f32_16x16x32_bf16 v[60:63], v[212:215], v[212:215], v[60:63]\n\t"
        "s_waitcnt lgkmcnt(9)\n\t"
        "v_mfma_f32_16x16x32_bf16 v[48:51], v[24:27], v[216:219], v[48:51]\n\t"
        "v_mfma_f32_16x16x32_bf16 v[56:59], v[216:219], v[216:219], v[56:59]\n\t"
        "s_waitcnt lgkmcnt(8)\n\t"
        "v_mfma_f32_16x16x32_bf16 v[52:55], v[24:27], v[220:223], v[52:55]\n\t"
        "v_mfma_f32_16x16x32_bf16 v[60:63], v[220:223], v[220:223], v[60:63]\n\t"
        "s_waitcnt lgkmcnt(7)\n\t"
        "v_mfma_f32_16x16x32_bf16 v[48:51], v[24:27], v[224:227], v[48:51]\n\t"
        "v_mfma_f32_16x16x32_bf16 v[56:59], v[224:227], v[224:227], v[56:59]\n\t"
        "s_waitcnt lgkmcnt(6)\n\t"
        "v_mfma_f32_16x16x32_bf16 v[52:55], v[24:27], v[228:231], v[52:55]\n\t"
        "v_mfma_f32_16x16x32_bf16 v[60:63], v[228:231], v[228:231], v[60:63]\n\t"
        "s_waitcnt lgkmcnt(5)\n\t"
        "v_mfma_f32_16x16x32_bf16 v[48:51], v[24:27], v[232:235], v[48:51]\n\t"
        "v_mfma_f32_16x16x32_bf16 v[56:59], v[232:235], v[232:235], v[56:59]\n\t"
        "s_waitcnt lgkmcnt(4)\n\t"
        "v_mfma_f32_16x16x32_bf16 v[52:55], v[24:27], v[236:239], v[52:55]\n\t"
        "v_mfma_f32_16x16x32_bf16 v[60:63], v[236:239], v[236:239], v[60:63]\n\t"
        "s_waitcnt lgkmcnt(3)\n\t"
        "v_mfma_f32_16x16x32_bf16 v[48:51], v[24:27], v[240:243], v[48:51]\n\t"
        "v_mfma_f32_16x16x32_bf16 v[56:59], v[240:243], v[240:243], v[56:59]\n\t"
        "s_waitcnt lgkmcnt(2)\n\t"
        "v_mfma_f32_16x16x32_bf16 v[52:55], v[24:27], v[244:247], v[52:55]\n\t"
        "v_mfma_f32_16x16x32_bf16 v[60:63], v[244:247], v[244:247], v[60:63]\n\t"
        "s_waitcnt lgkmcnt(1)\n\t"
        "v_mfma_f32_16x16x32_bf16 v[48:51], v[24:27], v[248:251], v[48:51]\n\t"
        "v_mfma_f32_16x16x32_bf16 v[56:59], v[248:251], v[248:251], v[56:59]\n\t"
        "s_waitcnt lgkmcnt(0)\n\t"
        "v_mfma_f32_16x16x32_bf16 v[52:55], v[24:27], v[252:255], v[52:55]\n\t"
        "v_mfma_f32_16x16x32_bf16 v[60:63], v[252:255], v[252:255], v[60:63]\n\t"
        "s_sub_u32 %[sc], %[sc], 1\n\t"
        "s_cmp_lg_u32 %[sc], 0\n\t"
        "s_cbranch_scc1 Ll_%=\n\t"
        "s_nop 7\n\t"
        "v_mov_b32 %[x0], v48\n\t"
        "v_cndmask_b32 v56, v56, v57, %[spar]\n\t"
        "v_cndmask_b32 v58, v58, v59, %[spar]\n\t"
        "v_cndmask_b32 %[q0], v56, v58, %[sl2]\n\t"
        "v_mov_b32 %[x1], v52\n\t"
        "v_cndmask_b32 v60, v60, v61, %[spar]\n\t"
        "v_cndmask_b32 v62, v62, v63, %[spar]\n\t"
        "v_cndmask_b32 %[q1], v60, v62, %[sl2]\n\t"
        : [xa] "+v"(xa), [xb] "=&v"(xb), [sc] "=&s"(sc), [x0] "=&v"(sx[0]), [q0] "=&v"(sq[0]), [x1] "=&v"(sx[1]), [q1] "=&v"(sq[1])
        : [spar] "s"(0xAAAAAAAAAAAAAAAAull), [sl2] "s"(0xCCCCCCCCCCCCCCCCull)
        : "memory", "scc", "v24", "v25", "v26", "v27");
}

DEV void uv_load(u32x4 sr, u32x4 so, u32 ra, u32 oa, u32 ob) {
    asm volatile(
        "s_nop 4\n\t"
        "buffer_load_dwordx4 v[48:51], %[ra], %[sr], 0 offen offset:0\n\t"
        "buffer_load_dwordx4 v[52:55], %[ra], %[sr], 0 offen offset:256\n\t"
        "buffer_load_dwordx4 v[56:59], %[ra], %[sr], 0 offen offset:512\n\t"
        "buffer_load_dwordx4 v[60:63], %[ra], %[sr], 0 offen offset:768\n\t"
        "buffer_load_dwordx4 v[64:67], %[ra], %[sr], 0 offen offset:1024\n\t"
        "buffer_load_dwordx4 v[68:71], %[oa], %[so], 0 offen offset:0\n\t"
        "buffer_load_dwordx4 v[72:75], %[oa], %[so], 0 offen offset:256\n\t"
        "buffer_load_dwordx4 v[76:79], %[oa], %[so], 0 offen offset:512\n\t"
        "buffer_load_dwordx4 v[80:83], %[oa], %[so], 0 offen offset:768\n\t"
        "buffer_load_dwordx4 v[84:87], %[ob], %[so], 0 offen offset:1024\n\t"
        :: [sr] "s"(sr), [so] "s"(so), [ra] "v"(ra), [oa] "v"(oa), [ob] "v"(ob) : "memory");
}
DEV void uv_dots(float (&o)[4]) {
    asm volatile(
        "v_mov_b32 %[o0], 0\n\t"
        "v_mov_b32 %[o1], 0\n\t"
        "v_mov_b32 %[o2], 0\n\t"
        "v_mov_b32 %[o3], 0\n\t"
        "v_dot2c_f32_bf16 %[o0], %[sone], v48\n\t"
        "v_dot2c_f32_bf16 %[o1], v48, v68\n\t"
        "v_dot2c_f32_bf16 %[o2], %[sone], v68\n\t"
        "v_dot2c_f32_bf16 %[o3], v68, v68\n\t"
        "v_dot2c_f32_bf16 %[o0], %[sone], v49\n\t"
        "v_dot2c_f32_bf16 %[o1], v49, v69\n\t"
        "v_dot2c_f32_bf16 %[o2], %[sone], v69\n\t"
        "v_dot2c_f32_bf16 %[o3], v69, v69\n\t"
        "v_dot2c_f32_bf16 %[o0], %[sone], v50\n\t"
        "v_dot2c_f32_bf16 %[o1], v50, v70\n\t"
        "v_dot2c_f32_bf16 %[o2], %[sone], v70\n\t"
        "v_dot2c_f32_bf16 %[o3], v70, v70\n\t"
        "v_dot2c_f32_bf16 %[o0], %[sone], v51\n\t"
        "v_dot2c_f32_bf16 %[o1], v51, v71\n\t"
        "v_dot2c_f32_bf16 %[o2], %[sone], v71\n\t"
        "v_dot2c_f32_bf16 %[o3], v71, v71\n\t"
        "v_dot2c_f32_bf16 %[o0], %[sone], v52\n\t"
        "v_dot2c_f32_bf16 %[o1], v52, v72\n\t"
        "v_dot2c_f32_bf16 %[o2], %[sone], v72\n\t"
        "v_dot2c_f32_bf16 %[o3], v72, v72\n\t"
        "v_dot2c_f32_bf16 %[o0], %[sone], v53\n\t"
        "v_dot2c_f32_bf16 %[o1], v53, v73\n\t"
        "v_dot2c_f32_bf16 %[o2], %[sone], v73\n\t"
        "v_dot2c_f32_bf16 %[o3], v73, v73\n\t"
        "v_dot2c_f32_bf16 %[o0], %[sone], v54\n\t"
        "v_dot2c_f32_bf16 %[o1], v54, v74\n\t"
        "v_dot2c_f32_bf16 %[o2], %[sone], v74\n\t"
        "v_dot2c_f32_bf16 %[o3], v74, v74\n\t"
        "v_dot2c_f32_bf16 %[o0], %[sone], v55\n\t"
        "v_dot2c_f32_bf16 %[o1], v55, v75\n\t"
        "v_dot2c_f32_bf16 %[o2], %[sone], v75\n\t"
        "v_dot2c_f32_bf16 %[o3], v75, v75\n\t"
        "v_dot2c_f32_bf16 %[o0], %[sone], v56\n\t"
        "v_dot2c_f32_bf16 %[o1], v56, v76\n\t"
        "v_dot2c_f32_bf16 %[o2], %[sone], v76\n\t"
        "v_dot2c_f32_bf16 %[o3], v76, v76\n\t"
        "v_dot2c_f32_bf16 %[o0], %[sone], v57\n\t"
        "v_dot2c_f32_bf16 %[o1], v57, v77\n\t"
        "v_dot2c_f32_bf16 %[o2], %[sone], v77\n\t"
        "v_dot2c_f32_bf16 %[o3], v77, v77\n\t"
        "v_dot2c_f32_bf16 %[o0], %[sone], v58\n\t"
        "v_dot2c_f32_bf16 %[o1], v58, v78\n\t"
        "v_dot2c_f32_bf16 %[o2], %[sone], v78\n\t"
        "v_dot2c_f32_bf16 %[o3], v78, v78\n\t"
        "v_dot2c_f32_bf16 %[o0], %[sone], v59\n\t"
        "v_dot2c_f32_bf16 %[o1], v59, v79\n\t"
        "v_dot2c_f32_bf16 %[o2], %[sone], v79\n\t"
        "v_dot2c_f32_bf16 %[o3], v79, v79\n\t"
        "v_dot2c_f32_bf16 %[o0], %[sone], v60\n\t"
        "v_dot2c_f32_bf16 %[o1], v60, v80\n\t"
        "v_dot2c_f32_bf16 %[o2], %[sone], v80\n\t"
        "v_dot2c_f32_bf16 %[o3], v80, v80\n\t"
        "v_dot2c_f32_bf16 %[o0], %[sone], v61\n\t"
        "v_dot2c_f32_bf16 %[o1], v61, v81\n\t"
        "v_dot2c_f32_bf16 %[o2], %[sone], v81\n\t"
        "v_dot2c_f32_bf16 %[o3], v81, v81\n\t"
        "v_dot2c_f32_bf16 %[o0], %[sone], v62\n\t"
        "v_dot2c_f32_bf16 %[o1], v62, v82\n\t"
        "v_dot2c_f32_bf16 %[o2], %[sone], v82\n\t"
        "v_dot2c_f32_bf16 %[o3], v82, v82\n\t"
        "v_dot2c_f32_bf16 %[o0], %[sone], v63\n\t"
        "v_dot2c_f32_bf16 %[o1], v63, v83\n\t"
        "v_dot2c_f32_bf16 %[o2], %[sone], v83\n\t"
        "v_dot2c_f32_bf16 %[o3], v83, v83\n\t"
        "v_dot2c_f32_bf16 %[o0], %[sone], v64\n\t"
        "v_dot2c_f32_bf16 %[o1], v64, v84\n\t"
        "v_dot2c_f32_bf16 %[o2], %[sone], v84\n\t"
        "v_dot2c_f32_bf16 %[o3], v84, v84\n\t"
        "v_dot2c_f32_bf16 %[o0], %[sone], v65\n\t"
        "v_dot2c_f32_bf16 %[o1], v65, v85\n\t"
        "v_dot2c_f32_bf16 %[o2], %[sone], v85\n\t"
        "v_dot2c_f32_bf16 %[o3], v85, v85\n\t"
        "v_dot2c_f32_bf16 %[o0], %[sone], v66\n\t"
        "v_dot2c_f32_bf16 %[o1], v66, v86\n\t"
        "v_dot2c_f32_bf16 %[o2], %[sone], v86\n\t"
        "v_dot2c_f32_bf16 %[o3], v86, v86\n\t"
        "v_dot2c_f32_bf16 %[o0], %[sone], v67\n\t"
        "v_dot2c_f32_bf16 %[o1], v67, v87\n\t"
        "v_dot2c_f32_bf16 %[o2], %[sone], v87\n\t"
        "v_dot2c_f32_bf16 %[o3], v87, v87\n\t"
        "s_nop 2\n\t"
        : [o0] "=&v"(o[0]), [o1] "=&v"(o[1]), [o2] "=&v"(o[2]), [o3] "=&v"(o[3]) : [sone] "s"(ONES2) : "memory");
}

DEV void ffn_help(u32x4 srd, u32 c0, int g0, int g1, int g2, int pa, u32 fa, int ct, u32 l1, u32& cn) {
    u32 cr;
    asm volatile(
        "s_waitcnt vmcnt(0)\n\t"
        "ds_read_b64_tr_b16 v[40:41], %[g0] offset:0\n\t"
        "ds_read_b64_tr_b16 v[42:43], %[g0] offset:384\n\t"
        "ds_read_b64_tr_b16 v[44:45], %[g1] offset:0\n\t"
        "ds_read_b64_tr_b16 v[46:47], %[g1] offset:384\n\t"
        "ds_read_b64_tr_b16 v[48:49], %[g2] offset:0\n\t"
        "ds_read_b64_tr_b16 v[50:51], %[g2] offset:384\n\t"
        "ds_read_b64_tr_b16 v[52:53], %[g0] offset:1536\n\t"
        "ds_read_b64_tr_b16 v[54:55], %[g0] offset:1920\n\t"
        "ds_read_b64_tr_b16 v[56:57], %[g1] offset:1536\n\t"
        "ds_read_b64_tr_b16 v[58:59], %[g1] offset:1920\n\t"
        "ds_read_b64_tr_b16 v[60:61], %[g2] offset:1536\n\t"
        "ds_read_b64_tr_b16 v[62:63], %[g2] offset:1920\n\t"
        "ds_read_b64_tr_b16 v[64:65], %[g0] offset:3072\n\t"
        "ds_read_b64_tr_b16 v[66:67], %[g0] offset:3456\n\t"
        "ds_read_b64_tr_b16 v[68:69], %[g1] offset:3072\n\t"
        "ds_read_b64_tr_b16 v[70:71], %[g1] offset:3456\n\t"
        "ds_read_b64_tr_b16 v[72:73], %[g2] offset:3072\n\t"
        "ds_read_b64_tr_b16 v[74:75], %[g2] offset:3456\n\t"
        "ds_read_b64_tr_b16 v[76:77], %[g0] offset:4608\n\t"
        "ds_read_b64_tr_b16 v[78:79], %[g0] offset:4992\n\t"
        "ds_read_b64_tr_b16 v[80:81], %[g1] offset:4608\n\t"
        "ds_read_b64_tr_b16 v[82:83], %[g1] offset:4992\n\t"
        "ds_read_b64_tr_b16 v[84:85], %[g2] offset:4608\n\t"
        "ds_read_b64_tr_b16 v[86:87], %[g2] offset:4992\n\t"
        "ds_read_b64_tr_b16 v[88:89], %[g0] offset:6144\n\t"
        "ds_read_b64_tr_b16 v[90:91], %[g0] offset:6528\n\t"
        "ds_read_b64_tr_b16 v[92:93], %[g1] offset:6144\n\t"
        "ds_read_b64_tr_b16 v[94:95], %[g1] offset:6528\n\t"
        "ds_read_b64_tr_b16 v[96:97], %[g2] offset:6144\n\t"
        "ds_read_b64_tr_b16 v[98:99], %[g2] offset:6528\n\t"
        "v_mov_b32 v20, 0x3f803f80\n\t"
        "v_mov_b32 v21, 0x3f803f80\n\t"
        "v_mov_b32 v22, 0x3f803f80\n\t"
        "v_mov_b32 v23, 0x3f803f80\n\t"
        "v_mov_b32 v24, 0x3f803f80\n\t"
        "v_mov_b32 v25, 0x3f803f80\n\t"
        "v_mov_b32 v26, 0x3f803f80\n\t"
        "v_mov_b32 v27, 0x3f803f80\n\t"
        "v_mov_b32 v28, 0x3f803f80\n\t"
        "v_mov_b32 v29, 0x3f803f80\n\t"
        "v_mov_b32 v30, 0x3f803f80\n\t"
        "v_mov_b32 v31, 0x3f803f80\n\t"
        "v_mov_b32 v32, 0x3f803f80\n\t"
        "v_mov_b32 v33, 0x3f803f80\n\t"
        "v_mov_b32 v34, 0x3f803f80\n\t"
        "v_mov_b32 v35, 0x3f803f80\n\t"
        "v_mov_b32 v36, 0x3f803f80\n\t"
        "v_mov_b32 v37, 0x3f803f80\n\t"
        "v_mov_b32 v38, 0x3f803f80\n\t"
        "v_mov_b32 v39, 0x3f803f80\n\t"
        "v_mov_b32 %[g1], %[sl1]\n\t"
        "v_mov_b32 %[g2], %[sfa]\n\t"
        "Lw_%=:\n\t"
        "s_sleep 1\n\t"
        "ds_read_b32 %[g0], %[ct]\n\t"
        "s_sub_u32 %[scn], %[scn], 1\n\t"
        "s_waitcnt lgkmcnt(0)\n\t"
        "v_readfirstlane_b32 %[scr], %[g0]\n\t"
        "s_cmp_lt_i32 %[scn], 1\n\t"
        "s_cbranch_scc1 Lq_%=\n\t"
        "s_cmp_eq_u32 %[scr], 0\n\t"
        "s_cbranch_scc1 Lw_%=\n\t"
        "Lq_%=:\n\t"
        "s_setprio 3\n\t"
        "v_add_u32 %[c0], %[c0], %[g0]\n\t"
        "Lr_%=:\n\t"
        "s_mov_b64 exec, %[sm7]\n\t"
        "buffer_load_dwordx4 v[20:23], %[c0], %[ssrd], 0 offen offset:0 sc1\n\t"
        "buffer_load_dwordx4 v[24:27], %[c0], %[ssrd], 0 offen offset:64 sc1\n\t"
        "buffer_load_dwordx4 v[28:31], %[c0], %[ssrd], 0 offen offset:128 sc1\n\t"
        "buffer_load_dwordx4 v[32:35], %[c0], %[ssrd], 0 offen offset:192 sc1\n\t"
        "buffer_load_dwordx4 v[36:39], %[c0], %[ssrd], 0 offen offset:256 sc1\n\t"
        "s_mov_b64 exec, -1\n\t"
        "v_mov_b32 %[ct], 153984\n\t"
        "s_waitcnt vmcnt(4)\n\t"
        "ds_read_b32 v7, %[ct] offset:12\n\t"
        "v_mfma_f32_16x16x32_bf16 v[8:11], v[20:23], v[40:43], 0\n\t"
        "v_max3_u32 %[g0], v22, v20, v21\n\t"
        "v_mfma_f32_16x16x32_bf16 v[12:15], v[20:23], v[44:47], 0\n\t"
        "v_max3_u32 %[g0], %[g0], v22, v23\n\t"
        "v_mfma_f32_16x16x32_bf16 v[16:19], v[20:23], v[48:51], 0\n\t"
        "s_waitcnt vmcnt(3)\n\t"
        "v_mfma_f32_16x16x32_bf16 v[8:11], v[24:27], v[52:55], v[8:11]\n\t"
        "v_max3_u32 %[g0], %[g0], v24, v25\n\t"
        "v_mfma_f32_16x16x32_bf16 v[12:15], v[24:27], v[56:59], v[12:15]\n\t"
        "v_max3_u32 %[g0], %[g0], v26, v27\n\t"
        "v_mfma_f32_16x16x32_bf16 v[16:19], v[24:27], v[60:63], v[16:19]\n\t"
        "s_waitcnt vmcnt(2)\n\t"
        "v_mfma_f32_16x16x32_bf16 v[8:11], v[28:31], v[64:67], v[8:11]\n\t"
        "v_max3_u32 %[g0], %[g0], v28, v29\n\t"
        "v_mfma_f32_16x16x32_bf16 v[12:15], v[28:31], v[68:71], v[12:15]\n\t"
        "v_max3_u32 %[g0], %[g0], v30, v31\n\t"
        "v_mfma_f32_16x16x32_bf16 v[16:19], v[28:31], v[72:75], v[16:19]\n\t"
        "s_waitcnt vmcnt(1)\n\t"
        "v_mfma_f32_16x16x32_bf16 v[8:11], v[32:35], v[76:79], v[8:11]\n\t"
        "v_max3_u32 %[g0], %[g0], v32, v33\n\t"
        "v_mfma_f32_16x16x32_bf16 v[12:15], v[32:35], v[80:83], v[12:15]\n\t"
        "v_max3_u32 %[g0], %[g0], v34, v35\n\t"
        "v_mfma_f32_16x16x32_bf16 v[16:19], v[32:35], v[84:87], v[16:19]\n\t"
        "s_waitcnt vmcnt(0)\n\t"
        "v_mfma_f32_16x16x32_bf16 v[8:11], v[36:39], v[88:91], v[8:11]\n\t"
        "v_max3_u32 %[g0], %[g0], v36, v37\n\t"
        "v_mfma_f32_16x16x32_bf16 v[12:15], v[36:39], v[92:95], v[12:15]\n\t"
        "v_max3_u32 %[g0], %[g0], v38, v39\n\t"
        "v_mfma_f32_16x16x32_bf16 v[16:19], v[36:39], v[96:99], v[16:19]\n\t"
        "v_cmp_eq_u32 vcc, -1, %[g0]\n\t"
        "s_cbranch_vccz Ld_%=\n\t"
        "s_cmp_lt_i32 %[scn], 1\n\t"
        "s_cbranch_scc1 Ld_%=\n\t"
        "s_sub_u32 %[scn], %[scn], 1\n\t"
        "s_branch Lr_%=\n\t"
        "Ld_%=:\n\t"
        "Lk_%=:\n\t"
        "s_waitcnt lgkmcnt(0)\n\t"
        "v_lshrrev_b32 v7, 2, v7\n\t"
        "v_cmp_gt_u32 vcc, %[sl1], v7\n\t"
        "s_cbranch_vccz Lj_%=\n\t"
        "ds_read_b32 v7, %[ct] offset:12\n\t"
        "s_sub_u32 %[scn], %[scn], 1\n\t"
        "s_cmp_gt_i32 %[scn], 0\n\t"
        "s_cbranch_scc1 Lk_%=\n\t"
        "s_waitcnt lgkmcnt(0)\n\t"
        "Lj_%=:\n\t"
        "s_nop 1\n\t"
        "s_mov_b32 exec_hi, 0\n\t"
        "ds_write_b128 %[pa], v[8:11] offset:0\n\t"
        "ds_write_b128 %[pa], v[12:15] offset:512\n\t"
        "ds_write_b128 %[pa], v[16:19] offset:1024\n\t"
        "ds_write_b32 %[g2], %[g1]\n\t"
        "s_mov_b32 exec_hi, -1\n\t"
        : [scr] "=&s"(cr), [scn] "+s"(cn), [g0] "+v"(g0), [g1] "+v"(g1), [g2] "+v"(g2), [c0] "+v"(c0), [ct] "+v"(ct)
        : [ssrd] "s"(srd), [pa] "v"(pa), [sfa] "s"(fa), [sl1] "s"(l1), [sm7] "s"(0x007F007F007F007Full)
        : "memory", "scc", "vcc", "v7", "v8", "v9", "v10", "v11", "v12", "v13", "v14", "v15", "v16", "v17", "v18", "v19", "v20", "v21", "v22", "v23", "v24", "v25", "v26", "v27", "v28", "v29", "v30", "v31", "v32", "v33", "v34", "v35", "v36", "v37", "v38", "v39", "v40", "v41", "v42", "v43", "v44", "v45", "v46", "v47");
}

DEV void ffn_a(u32x4 srd, u32 ra, u32 sa, float (&o)[3], u32& cn) {
    u32 t;
    asm volatile(
        "s_nop 4\n\t"
        "Lr_%=:\n\t"
        "buffer_load_dwordx4 v[72:75], %[va], %[ssrd], 0 offen sc1\n\t"
        "buffer_load_dwordx4 v[76:79], %[va], %[ssrd], 0 offen offset:1024 sc1\n\t"
        "s_mov_b64 exec, %[sm7]\n\t"
        "buffer_load_dwordx4 v[60:63], %[ra], %[ssrd], 0 offen offset:0 sc1\n\t"
        "buffer_load_dwordx4 v[64:67], %[ra], %[ssrd], 0 offen offset:64 sc1\n\t"
        "buffer_load_dwordx4 v[68:71], %[ra], %[ssrd], 0 offen offset:128 sc1\n\t"
        "s_mov_b64 exec, -1\n\t"
        "s_sub_u32 %[scn], %[scn], 1\n\t"
        "s_waitcnt vmcnt(3)\n\t"
        "v_max3_u32 %[t], v72, v73, v74\n\t"
        "v_max3_u32 %[t], %[t], v75, v76\n\t"
        "v_max3_u32 %[t], %[t], v77, v78\n\t"
        "v_max_u32 %[t], %[t], v79\n\t"
        "v_cmp_eq_u32 vcc, -1, %[t]\n\t"
        "v_add_f32 %[o0], v72, v76\n\t"
        "v_add_f32 %[o1], v73, v77\n\t"
        "v_add_f32 %[o2], v74, v78\n\t"
        "s_cbranch_vccz Le_%=\n\t"
        "s_cmp_gt_i32 %[scn], 0\n\t"
        "s_cbranch_scc1 Lr_%=\n\t"
        "Le_%=:\n\t"
        : [o0] "=&v"(o[0]), [o1] "=&v"(o[1]), [o2] "=&v"(o[2]), [t] "=&v"(t), [scn] "+s"(cn)
        : [ssrd] "s"(srd), [ra] "v"(ra), [va] "v"(sa), [sm7] "s"(0x007F007F007F007Full)
        : "memory", "scc", "vcc");
}
DEV void ffn_ones() {
    asm volatile(
        "v_mov_b32 v60, 0x3f803f80\n\t"
        "v_mov_b32 v61, 0x3f803f80\n\t"
        "v_mov_b32 v62, 0x3f803f80\n\t"
        "v_mov_b32 v63, 0x3f803f80\n\t"
        "v_mov_b32 v64, 0x3f803f80\n\t"
        "v_mov_b32 v65, 0x3f803f80\n\t"
        "v_mov_b32 v66, 0x3f803f80\n\t"
        "v_mov_b32 v67, 0x3f803f80\n\t"
        "v_mov_b32 v68, 0x3f803f80\n\t"
        "v_mov_b32 v69, 0x3f803f80\n\t"
        "v_mov_b32 v70, 0x3f803f80\n\t"
        "v_mov_b32 v71, 0x3f803f80\n\t");
}

DEV void ffn(u32x4 srd, u32 cr, u32 ra, u32 c1, int pa, int ff, int pg, int pu, u32 wo, float m, float r, u32 l1, u32 ya, u32 ye, u32 yb, u32& cn) {
    u32 ap[12];
    asm volatile(
        "v_mov_b32 v45, 153984\n\t"
        "Lr_%=:\n\t"
        "s_waitcnt vmcnt(2)\n\t"
        "ds_read_b32 v46, v45 offset:12\n\t"
        "v_mfma_f32_16x16x32_bf16 v[84:87], v[60:63], v[220:223], 0\n\t"
        "v_max3_u32 v40, v62, v60, v61\n\t"
        "v_mfma_f32_16x16x32_bf16 v[88:91], v[60:63], v[224:227], 0\n\t"
        "v_max3_u32 v40, v40, v62, v63\n\t"
        "v_mfma_f32_16x16x32_bf16 v[92:95], v[60:63], v[228:231], 0\n\t"
        "s_waitcnt vmcnt(1)\n\t"
        "v_mfma_f32_16x16x32_bf16 v[84:87], v[64:67], v[232:235], v[84:87]\n\t"
        "v_max3_u32 v40, v40, v64, v65\n\t"
        "v_mfma_f32_16x16x32_bf16 v[88:91], v[64:67], v[236:239], v[88:91]\n\t"
        "v_max3_u32 v40, v40, v66, v67\n\t"
        "v_mfma_f32_16x16x32_bf16 v[92:95], v[64:67], v[240:243], v[92:95]\n\t"
        "s_waitcnt vmcnt(0)\n\t"
        "v_mfma_f32_16x16x32_bf16 v[84:87], v[68:71], v[244:247], v[84:87]\n\t"
        "v_max3_u32 v40, v40, v68, v69\n\t"
        "v_mfma_f32_16x16x32_bf16 v[88:91], v[68:71], v[248:251], v[88:91]\n\t"
        "v_max3_u32 v40, v40, v70, v71\n\t"
        "v_mfma_f32_16x16x32_bf16 v[92:95], v[68:71], v[252:255], v[92:95]\n\t"
        "v_cmp_eq_u32 vcc, -1, v40\n\t"
        "s_cbranch_vccnz Lx_%=\n\t"
        "Ld_%=:\n\t"
        "buffer_load_dwordx4 v[24:27], v107, %[ssrd], %[scr] offen sc1\n\t"
        "buffer_load_dwordx4 v[28:31], %[c1], %[ssrd], %[scr] offen sc1\n\t"
        "v_add_u32 v42, %[swo], %[pg]\n\t"
        "v_add_u32 v43, %[swo], %[pu]\n\t"
        "Lk_%=:\n\t"
        "s_waitcnt lgkmcnt(0)\n\t"
        "v_lshrrev_b32 v46, 2, v46\n\t"
        "v_cmp_gt_u32 vcc, %[sl1], v46\n\t"
        "s_cbranch_vccz Lj_%=\n\t"
        "ds_read_b32 v46, v45 offset:12\n\t"
        "s_sub_u32 %[scn], %[scn], 1\n\t"
        "s_cmp_gt_i32 %[scn], 0\n\t"
        "s_cbranch_scc1 Lk_%=\n\t"
        "s_waitcnt lgkmcnt(0)\n\t"
        "Lj_%=:\n\t"
        "s_mov_b32 exec_hi, 0\n\t"
        "ds_write_b128 %[pa], v[84:87] offset:0\n\t"
        "ds_write_b128 %[pa], v[88:91] offset:512\n\t"
        "ds_write_b128 %[pa], v[92:95] offset:1024\n\t"
        "s_mov_b32 exec_hi, -1\n\t"
        "Lw_%=:\n\t"
        "ds_read_b128 v[76:79], %[ff]\n\t"
        "ds_read2st64_b32 v[60:61], v42 offset0:0 offset1:6\n\t"
        "ds_read2st64_b32 v[62:63], %[pg] offset0:0 offset1:6\n\t"
        "ds_read2st64_b32 v[64:65], v43 offset0:0 offset1:6\n\t"
        "ds_read2st64_b32 v[66:67], %[pu] offset0:0 offset1:6\n\t"
        "ds_read2st64_b32 v[68:69], v42 offset0:12 offset1:18\n\t"
        "ds_read2st64_b32 v[70:71], %[pg] offset0:12 offset1:18\n\t"
        "ds_read2st64_b32 v[72:73], v43 offset0:12 offset1:18\n\t"
        "ds_read2st64_b32 v[74:75], %[pu] offset0:12 offset1:18\n\t"
        "s_sub_u32 %[scn], %[scn], 1\n\t"
        "s_waitcnt lgkmcnt(8)\n\t"
        "v_min3_u32 v40, v76, v77, v78\n\t"
        "v_cmp_gt_u32 vcc, %[sl1], v40\n\t"
        "s_cmp_lt_i32 %[scn], 1\n\t"
        "s_cbranch_scc1 Lg_%=\n\t"
        "s_cbranch_vccnz Lw_%=\n\t"
        "Lg_%=:\n\t"
        "s_waitcnt lgkmcnt(0)\n\t"
        "v_add_f32 v60, v60, v61\n\t"
        "v_add_f32 v62, v62, v63\n\t"
        "v_add_f32 v64, v64, v65\n\t"
        "v_add_f32 v66, v66, v67\n\t"
        "v_add_f32 v68, v68, v69\n\t"
        "v_add_f32 v70, v70, v71\n\t"
        "v_add_f32 v72, v72, v73\n\t"
        "v_add_f32 v74, v74, v75\n\t"
        "v_add_f32 v60, v60, v68\n\t"
        "v_add_f32 v62, v62, v70\n\t"
        "v_add_f32 v64, v64, v72\n\t"
        "v_add_f32 v66, v66, v74\n\t"
        "v_fma_f32 v42, -%[sm], v62, v60\n\t"
        "v_fma_f32 v43, -%[sm], v66, v64\n\t"
        "v_mul_f32 v42, %[sr], v42\n\t"
        "v_mul_f32 v43, %[sr], v43\n\t"
        "v_mul_f32 v44, 0xbfb8aa3b, v42\n\t"
        "v_exp_f32 v44, v44\n\t"
        "v_mul_f32 v42, v42, v43\n\t"
        "v_add_f32 v44, 1.0, v44\n\t"
        "v_rcp_f32 v44, v44\n\t"
        "v_mov_b32 v98, %[sm]\n\t"
        "v_mov_b32 v99, %[sr]\n\t"
        "v_mul_f32 v42, v42, v44\n\t"
        "v_mov_b32 v60, 0\n\t"
        "v_mov_b32 v61, 0\n\t"
        "v_mov_b32_dpp v43, v42 row_shl:1 row_mask:0xf bank_mask:0xf bound_ctrl:1\n\t"
        "v_cvt_pk_bf16_f32 v42, v42, v43\n\t"
        "v_mov_b32 v62, 0\n\t"
        "v_readlane_b32 %[sa0], v42, 0\n\t"
        "v_mov_b32 v63, 0\n\t"
        "v_readlane_b32 %[sa1], v42, 2\n\t"
        "v_mov_b32 v64, 0\n\t"
        "v_readlane_b32 %[sa2], v42, 4\n\t"
        "v_mov_b32 v65, 0\n\t"
        "v_readlane_b32 %[sa3], v42, 6\n\t"
        "v_mov_b32 v66, 0\n\t"
        "v_readlane_b32 %[sa4], v42, 8\n\t"
        "v_mov_b32 v67, 0\n\t"
        "v_readlane_b32 %[sa5], v42, 10\n\t"
        "v_mov_b32 v68, 0\n\t"
        "v_readlane_b32 %[sa6], v42, 12\n\t"
        "v_readlane_b32 %[sa7], v42, 14\n\t"
        "v_readlane_b32 %[sa8], v42, 16\n\t"
        "v_readlane_b32 %[sa9], v42, 18\n\t"
        "v_readlane_b32 %[sa10], v42, 20\n\t"
        "v_readlane_b32 %[sa11], v42, 22\n\t"
        "v_dot2c_f32_bf16 v60, %[sa0], v112\n\t"
        "v_dot2c_f32_bf16 v61, %[sa0], v116\n\t"
        "v_dot2c_f32_bf16 v62, %[sa0], v113\n\t"
        "v_dot2c_f32_bf16 v63, %[sa0], v117\n\t"
        "v_dot2c_f32_bf16 v60, %[sa1], v120\n\t"
        "v_dot2c_f32_bf16 v61, %[sa1], v124\n\t"
        "v_dot2c_f32_bf16 v62, %[sa1], v121\n\t"
        "v_dot2c_f32_bf16 v63, %[sa1], v125\n\t"
        "v_dot2c_f32_bf16 v60, %[sa2], v128\n\t"
        "v_dot2c_f32_bf16 v61, %[sa2], v132\n\t"
        "v_dot2c_f32_bf16 v62, %[sa2], v129\n\t"
        "v_dot2c_f32_bf16 v63, %[sa2], v133\n\t"
        "v_dot2c_f32_bf16 v60, %[sa3], v136\n\t"
        "v_dot2c_f32_bf16 v61, %[sa3], v140\n\t"
        "v_dot2c_f32_bf16 v62, %[sa3], v137\n\t"
        "v_dot2c_f32_bf16 v63, %[sa3], v141\n\t"
        "v_dot2c_f32_bf16 v60, %[sa4], v144\n\t"
        "v_dot2c_f32_bf16 v61, %[sa4], v148\n\t"
        "v_dot2c_f32_bf16 v62, %[sa4], v145\n\t"
        "v_dot2c_f32_bf16 v63, %[sa4], v149\n\t"
        "v_dot2c_f32_bf16 v60, %[sa5], v152\n\t"
        "v_dot2c_f32_bf16 v61, %[sa5], v156\n\t"
        "v_dot2c_f32_bf16 v62, %[sa5], v153\n\t"
        "v_dot2c_f32_bf16 v63, %[sa5], v157\n\t"
        "v_dot2c_f32_bf16 v60, %[sa6], v160\n\t"
        "v_dot2c_f32_bf16 v61, %[sa6], v164\n\t"
        "v_dot2c_f32_bf16 v62, %[sa6], v161\n\t"
        "v_dot2c_f32_bf16 v63, %[sa6], v165\n\t"
        "v_dot2c_f32_bf16 v60, %[sa7], v168\n\t"
        "v_dot2c_f32_bf16 v61, %[sa7], v172\n\t"
        "v_dot2c_f32_bf16 v62, %[sa7], v169\n\t"
        "v_dot2c_f32_bf16 v63, %[sa7], v173\n\t"
        "v_dot2c_f32_bf16 v60, %[sa8], v176\n\t"
        "v_dot2c_f32_bf16 v61, %[sa8], v180\n\t"
        "v_dot2c_f32_bf16 v62, %[sa8], v177\n\t"
        "v_dot2c_f32_bf16 v63, %[sa8], v181\n\t"
        "v_dot2c_f32_bf16 v60, %[sa9], v184\n\t"
        "v_dot2c_f32_bf16 v61, %[sa9], v188\n\t"
        "v_dot2c_f32_bf16 v62, %[sa9], v185\n\t"
        "v_dot2c_f32_bf16 v63, %[sa9], v189\n\t"
        "v_dot2c_f32_bf16 v60, %[sa10], v192\n\t"
        "v_dot2c_f32_bf16 v61, %[sa10], v196\n\t"
        "v_dot2c_f32_bf16 v62, %[sa10], v193\n\t"
        "v_dot2c_f32_bf16 v63, %[sa10], v197\n\t"
        "v_dot2c_f32_bf16 v60, %[sa11], v200\n\t"
        "v_dot2c_f32_bf16 v61, %[sa11], v204\n\t"
        "v_dot2c_f32_bf16 v62, %[sa11], v201\n\t"
        "v_dot2c_f32_bf16 v63, %[sa11], v205\n\t"
        "v_dot2c_f32_bf16 v64, %[sa0], v114\n\t"
        "v_dot2c_f32_bf16 v65, %[sa0], v118\n\t"
        "v_dot2c_f32_bf16 v66, %[sa0], v115\n\t"
        "v_dot2c_f32_bf16 v67, %[sa0], v119\n\t"
        "v_dot2c_f32_bf16 v68, %[sa0], v208\n\t"
        "v_min_u32 v60, -2, v60\n\t"
        "v_min_u32 v61, -2, v61\n\t"
        "v_min_u32 v62, -2, v62\n\t"
        "v_min_u32 v63, -2, v63\n\t"
        "buffer_store_dwordx4 v[60:63], %[ya], %[ssrd], %[syb] offen sc1\n\t"
        "v_dot2c_f32_bf16 v64, %[sa1], v122\n\t"
        "v_dot2c_f32_bf16 v65, %[sa1], v126\n\t"
        "v_dot2c_f32_bf16 v66, %[sa1], v123\n\t"
        "v_dot2c_f32_bf16 v67, %[sa1], v127\n\t"
        "v_dot2c_f32_bf16 v68, %[sa1], v209\n\t"
        "v_dot2c_f32_bf16 v64, %[sa2], v130\n\t"
        "v_dot2c_f32_bf16 v65, %[sa2], v134\n\t"
        "v_dot2c_f32_bf16 v66, %[sa2], v131\n\t"
        "v_dot2c_f32_bf16 v67, %[sa2], v135\n\t"
        "v_dot2c_f32_bf16 v68, %[sa2], v210\n\t"
        "v_dot2c_f32_bf16 v64, %[sa3], v138\n\t"
        "v_dot2c_f32_bf16 v65, %[sa3], v142\n\t"
        "v_dot2c_f32_bf16 v66, %[sa3], v139\n\t"
        "v_dot2c_f32_bf16 v67, %[sa3], v143\n\t"
        "v_dot2c_f32_bf16 v68, %[sa3], v211\n\t"
        "v_dot2c_f32_bf16 v64, %[sa4], v146\n\t"
        "v_dot2c_f32_bf16 v65, %[sa4], v150\n\t"
        "v_dot2c_f32_bf16 v66, %[sa4], v147\n\t"
        "v_dot2c_f32_bf16 v67, %[sa4], v151\n\t"
        "v_dot2c_f32_bf16 v68, %[sa4], v212\n\t"
        "v_dot2c_f32_bf16 v64, %[sa5], v154\n\t"
        "v_dot2c_f32_bf16 v65, %[sa5], v158\n\t"
        "v_dot2c_f32_bf16 v66, %[sa5], v155\n\t"
        "v_dot2c_f32_bf16 v67, %[sa5], v159\n\t"
        "v_dot2c_f32_bf16 v68, %[sa5], v213\n\t"
        "v_dot2c_f32_bf16 v64, %[sa6], v162\n\t"
        "v_dot2c_f32_bf16 v65, %[sa6], v166\n\t"
        "v_dot2c_f32_bf16 v66, %[sa6], v163\n\t"
        "v_dot2c_f32_bf16 v67, %[sa6], v167\n\t"
        "v_dot2c_f32_bf16 v68, %[sa6], v214\n\t"
        "v_dot2c_f32_bf16 v64, %[sa7], v170\n\t"
        "v_dot2c_f32_bf16 v65, %[sa7], v174\n\t"
        "v_dot2c_f32_bf16 v66, %[sa7], v171\n\t"
        "v_dot2c_f32_bf16 v67, %[sa7], v175\n\t"
        "v_dot2c_f32_bf16 v68, %[sa7], v215\n\t"
        "v_dot2c_f32_bf16 v64, %[sa8], v178\n\t"
        "v_dot2c_f32_bf16 v65, %[sa8], v182\n\t"
        "v_dot2c_f32_bf16 v66, %[sa8], v179\n\t"
        "v_dot2c_f32_bf16 v67, %[sa8], v183\n\t"
        "v_dot2c_f32_bf16 v68, %[sa8], v216\n\t"
        "v_dot2c_f32_bf16 v64, %[sa9], v186\n\t"
        "v_dot2c_f32_bf16 v65, %[sa9], v190\n\t"
        "v_dot2c_f32_bf16 v66, %[sa9], v187\n\t"
        "v_dot2c_f32_bf16 v67, %[sa9], v191\n\t"
        "v_dot2c_f32_bf16 v68, %[sa9], v217\n\t"
        "v_dot2c_f32_bf16 v64, %[sa10], v194\n\t"
        "v_dot2c_f32_bf16 v65, %[sa10], v198\n\t"
        "v_dot2c_f32_bf16 v66, %[sa10], v195\n\t"
        "v_dot2c_f32_bf16 v67, %[sa10], v199\n\t"
        "v_dot2c_f32_bf16 v68, %[sa10], v218\n\t"
        "v_dot2c_f32_bf16 v64, %[sa11], v202\n\t"
        "v_dot2c_f32_bf16 v65, %[sa11], v206\n\t"
        "v_dot2c_f32_bf16 v66, %[sa11], v203\n\t"
        "v_dot2c_f32_bf16 v67, %[sa11], v207\n\t"
        "v_dot2c_f32_bf16 v68, %[sa11], v219\n\t"
        "ds_write_b32 %[ff], %[ff] offset:12\n\t"
        "v_min_u32 v64, -2, v64\n\t"
        "v_min_u32 v65, -2, v65\n\t"
        "v_min_u32 v66, -2, v66\n\t"
        "v_min_u32 v67, -2, v67\n\t"
        "v_min_u32 v68, -2, v68\n\t"
        "buffer_store_dwordx4 v[64:67], %[ya], %[ssrd], %[syc] offen sc1\n\t"
        "buffer_store_dword v68, %[ye], %[ssrd], %[syb] offen sc1\n\t"
        "v_mul_f32 v98, -v98, v99\n\t"
        "s_waitcnt vmcnt(3)\n\t"
        "Lc_%=:\n\t"
        "v_max3_u32 v40, v24, v25, v26\n\t"
        "v_max3_u32 v40, v40, v27, v28\n\t"
        "v_max3_u32 v40, v40, v29, v30\n\t"
        "v_max_u32 v40, v40, v31\n\t"
        "v_cmp_eq_u32 vcc, -1, v40\n\t"
        "s_cbranch_vccnz Ly_%=\n\t"
        "Lz_%=:\n\t"
        "v_lshlrev_b32 v40, 16, v24\n\t"
        "v_and_b32 v41, 0xffff0000, v24\n\t"
        "v_fma_f32 v40, v40, v99, v98\n\t"
        "v_fma_f32 v41, v41, v99, v98\n\t"
        "v_cvt_pk_bf16_f32 v32, v40, v41\n\t"
        "v_lshlrev_b32 v40, 16, v25\n\t"
        "v_and_b32 v41, 0xffff0000, v25\n\t"
        "v_fma_f32 v40, v40, v99, v98\n\t"
        "v_fma_f32 v41, v41, v99, v98\n\t"
        "v_cvt_pk_bf16_f32 v33, v40, v41\n\t"
        "v_lshlrev_b32 v40, 16, v26\n\t"
        "v_and_b32 v41, 0xffff0000, v26\n\t"
        "v_fma_f32 v40, v40, v99, v98\n\t"
        "v_fma_f32 v41, v41, v99, v98\n\t"
        "v_cvt_pk_bf16_f32 v34, v40, v41\n\t"
        "v_lshlrev_b32 v40, 16, v27\n\t"
        "v_and_b32 v41, 0xffff0000, v27\n\t"
        "v_fma_f32 v40, v40, v99, v98\n\t"
        "v_fma_f32 v41, v41, v99, v98\n\t"
        "v_cvt_pk_bf16_f32 v35, v40, v41\n\t"
        "v_lshlrev_b32 v40, 16, v28\n\t"
        "v_and_b32 v41, 0xffff0000, v28\n\t"
        "v_fma_f32 v40, v40, v99, v98\n\t"
        "v_fma_f32 v41, v41, v99, v98\n\t"
        "v_cvt_pk_bf16_f32 v36, v40, v41\n\t"
        "v_lshlrev_b32 v40, 16, v29\n\t"
        "v_and_b32 v41, 0xffff0000, v29\n\t"
        "v_fma_f32 v40, v40, v99, v98\n\t"
        "v_fma_f32 v41, v41, v99, v98\n\t"
        "v_cvt_pk_bf16_f32 v37, v40, v41\n\t"
        "v_lshlrev_b32 v40, 16, v30\n\t"
        "v_and_b32 v41, 0xffff0000, v30\n\t"
        "v_fma_f32 v40, v40, v99, v98\n\t"
        "v_fma_f32 v41, v41, v99, v98\n\t"
        "v_cvt_pk_bf16_f32 v38, v40, v41\n\t"
        "v_lshlrev_b32 v40, 16, v31\n\t"
        "v_and_b32 v41, 0xffff0000, v31\n\t"
        "v_fma_f32 v40, v40, v99, v98\n\t"
        "v_fma_f32 v41, v41, v99, v98\n\t"
        "v_cvt_pk_bf16_f32 v39, v40, v41\n\t"
        "v_add_u32 v40, 155168, v107\n\t"
        "ds_write_b128 v40, v[32:35]\n\t"
        "ds_write_b128 v40, v[24:27] offset:1152\n\t"
        "s_mov_b64 exec, 0xff\n\t"
        "ds_write_b128 v40, v[36:39] offset:1024\n\t"
        "ds_write_b128 v40, v[28:31] offset:2176\n\t"
        "s_mov_b64 exec, -1\n\t"
        "s_waitcnt lgkmcnt(0)\n\t"
        "s_branch Le_%=\n\t"
        "Lx_%=:\n\t"
        "s_cmp_lt_i32 %[scn], 1\n\t"
        "s_cbranch_scc1 Ld_%=\n\t"
        "s_sub_u32 %[scn], %[scn], 1\n\t"
        "s_mov_b64 exec, %[sm7]\n\t"
        "buffer_load_dwordx4 v[60:63], %[ra], %[ssrd], 0 offen offset:0 sc1\n\t"
        "buffer_load_dwordx4 v[64:67], %[ra], %[ssrd], 0 offen offset:64 sc1\n\t"
        "buffer_load_dwordx4 v[68:71], %[ra], %[ssrd], 0 offen offset:128 sc1\n\t"
        "s_mov_b64 exec, -1\n\t"
        "s_branch Lr_%=\n\t"
        "Ly_%=:\n\t"
        "s_cmp_lt_i32 %[scn], 1\n\t"
        "s_cbranch_scc1 Lz_%=\n\t"
        "s_sub_u32 %[scn], %[scn], 1\n\t"
        "buffer_load_dwordx4 v[24:27], v107, %[ssrd], %[scr] offen sc1\n\t"
        "buffer_load_dwordx4 v[28:31], %[c1], %[ssrd], %[scr] offen sc1\n\t"
        "s_waitcnt vmcnt(0)\n\t"
        "s_branch Lc_%=\n\t"
        "Le_%=:\n\t"
        : [sa0] "=&s"(ap[0]), [sa1] "=&s"(ap[1]), [sa2] "=&s"(ap[2]), [sa3] "=&s"(ap[3]), [sa4] "=&s"(ap[4]), [sa5] "=&s"(ap[5]), [sa6] "=&s"(ap[6]), [sa7] "=&s"(ap[7]), [sa8] "=&s"(ap[8]), [sa9] "=&s"(ap[9]), [sa10] "=&s"(ap[10]), [sa11] "=&s"(ap[11]), [scn] "+s"(cn)
        : [ssrd] "s"(srd), [scr] "s"(cr), [ra] "v"(ra), [c1] "v"(c1), [pa] "v"(pa), [ff] "v"(ff), [pg] "v"(pg), [pu] "v"(pu), [ya] "v"(ya), [ye] "v"(ye),
          [syb] "s"(yb), [syc] "s"(yb + 262144), [swo] "s"(wo), [sm] "s"(m), [sr] "s"(r), [sl1] "s"(l1), [sm7] "s"(0x007F007F007F007Full)
        : "memory", "scc", "vcc", "v24", "v25", "v26", "v27", "v28", "v29", "v30", "v31", "v32", "v33", "v34", "v35", "v36", "v37", "v38", "v39", "v40", "v41", "v42", "v43", "v44", "v45", "v46", "v47");
}

DEV void poll_stats(u32x4 srd, u32 va, u32 so, float (&o)[3], u32& cn) {
    u32 t;
    asm volatile(
        "s_nop 4\n\t"
        "s_sleep 12\n\t"
        "buffer_load_dwordx4 v[48:51], %[va], %[ssrd], %[so] offen offset:0 sc1\n\t"
        "buffer_load_dwordx4 v[52:55], %[va], %[ssrd], %[so] offen offset:256 sc1\n\t"
        "buffer_load_dwordx4 v[56:59], %[va], %[ssrd], %[so] offen offset:512 sc1\n\t"
        "buffer_load_dwordx4 v[60:63], %[va], %[ssrd], %[so] offen offset:768 sc1\n\t"
        "buffer_load_dwordx4 v[64:67], %[va], %[ssrd], %[so] offen offset:1024 sc1\n\t"
        "buffer_load_dwordx4 v[68:71], %[va], %[ssrd], %[so] offen offset:1280 sc1\n\t"
        "buffer_load_dwordx4 v[72:75], %[va], %[ssrd], %[so] offen offset:1536 sc1\n\t"
        "buffer_load_dwordx4 v[76:79], %[va], %[ssrd], %[so] offen offset:1792 sc1\n\t"
        "Lp_%=:\n\t"
        "s_waitcnt vmcnt(0)\n\t"
        "v_max_u32 %[t], v48, v49\n\t"
        "v_max3_u32 %[t], %[t], v50, v52\n\t"
        "v_max3_u32 %[t], %[t], v53, v54\n\t"
        "v_max3_u32 %[t], %[t], v56, v57\n\t"
        "v_max3_u32 %[t], %[t], v58, v60\n\t"
        "v_max3_u32 %[t], %[t], v61, v62\n\t"
        "v_max3_u32 %[t], %[t], v64, v65\n\t"
        "v_max3_u32 %[t], %[t], v66, v68\n\t"
        "v_max3_u32 %[t], %[t], v69, v70\n\t"
        "v_max3_u32 %[t], %[t], v72, v73\n\t"
        "v_max3_u32 %[t], %[t], v74, v76\n\t"
        "v_max3_u32 %[t], %[t], v77, v78\n\t"
        "v_cmp_eq_u32 vcc, -1, %[t]\n\t"
        "s_cbranch_vccz Lf0_%=\n\t"
        "buffer_load_dwordx4 v[48:51], %[va], %[ssrd], %[so] offen offset:0 sc1\n\t"
        "buffer_load_dwordx4 v[52:55], %[va], %[ssrd], %[so] offen offset:256 sc1\n\t"
        "buffer_load_dwordx4 v[56:59], %[va], %[ssrd], %[so] offen offset:512 sc1\n\t"
        "buffer_load_dwordx4 v[60:63], %[va], %[ssrd], %[so] offen offset:768 sc1\n\t"
        "buffer_load_dwordx4 v[64:67], %[va], %[ssrd], %[so] offen offset:1024 sc1\n\t"
        "buffer_load_dwordx4 v[68:71], %[va], %[ssrd], %[so] offen offset:1280 sc1\n\t"
        "buffer_load_dwordx4 v[72:75], %[va], %[ssrd], %[so] offen offset:1536 sc1\n\t"
        "buffer_load_dwordx4 v[76:79], %[va], %[ssrd], %[so] offen offset:1792 sc1\n\t"
        "s_sub_u32 %[scn], %[scn], 1\n\t"
        "s_cmp_lg_u32 %[scn], 0\n\t"
        "s_cbranch_scc1 Lp_%=\n\t"
        "s_waitcnt vmcnt(0)\n\t"
        "Lf0_%=:\n\t"
        "v_add_f32 v48, v48, v64\n\t"
        "v_add_f32 v49, v49, v65\n\t"
        "v_add_f32 v50, v50, v66\n\t"
        "v_add_f32 v52, v52, v68\n\t"
        "v_add_f32 v53, v53, v69\n\t"
        "v_add_f32 v54, v54, v70\n\t"
        "v_add_f32 v56, v56, v72\n\t"
        "v_add_f32 v57, v57, v73\n\t"
        "v_add_f32 v58, v58, v74\n\t"
        "v_add_f32 v60, v60, v76\n\t"
        "v_add_f32 v61, v61, v77\n\t"
        "v_add_f32 v62, v62, v78\n\t"
        "v_add_f32 v48, v48, v52\n\t"
        "v_add_f32 v49, v49, v53\n\t"
        "v_add_f32 v50, v50, v54\n\t"
        "v_add_f32 v56, v56, v60\n\t"
        "v_add_f32 v57, v57, v61\n\t"
        "v_add_f32 v58, v58, v62\n\t"
        "v_add_f32 %[o0], v48, v56\n\t"
        "v_add_f32 %[o1], v49, v57\n\t"
        "v_add_f32 %[o2], v50, v58\n\t"
        "Le_%=:\n\t"
        : [o0] "=&v"(o[0]), [o1] "=&v"(o[1]), [o2] "=&v"(o[2]), [t] "=&v"(t), [scn] "+s"(cn)
        : [ssrd] "s"(srd), [va] "v"(va), [so] "s"(so)
        : "memory", "scc", "vcc");
}
DEV void poll_keye(u32x4 srd, u32 so, u32 tag, u32 layer, int g0, int g1, int g2, u32 (&o)[2], u32& cn) {
    u32 t;
    asm volatile(
        "s_nop 4\n\t"
        "s_sleep 2\n\t"
        "buffer_load_dwordx4 v[84:87], v107, %[ssrd], %[so] offen sc1\n\t"
        "s_cmp_lg_u32 %[slay], 0\n\t"
        "s_cbranch_scc1 Lq_%=\n\t"
        "v_perm_b32 %[t], v116, v112, %[slo]\n\t"
        "v_perm_b32 v116, v116, v112, %[shi]\n\t"
        "v_mov_b32 v112, %[t]\n\t"
        "v_perm_b32 %[t], v117, v113, %[slo]\n\t"
        "v_perm_b32 v117, v117, v113, %[shi]\n\t"
        "v_mov_b32 v113, %[t]\n\t"
        "v_perm_b32 %[t], v118, v114, %[slo]\n\t"
        "v_perm_b32 v118, v118, v114, %[shi]\n\t"
        "v_mov_b32 v114, %[t]\n\t"
        "v_perm_b32 %[t], v119, v115, %[slo]\n\t"
        "v_perm_b32 v119, v119, v115, %[shi]\n\t"
        "v_mov_b32 v115, %[t]\n\t"
        "v_perm_b32 v208, v221, v220, %[slo]\n\t"
        "v_perm_b32 %[t], v124, v120, %[slo]\n\t"
        "v_perm_b32 v124, v124, v120, %[shi]\n\t"
        "v_mov_b32 v120, %[t]\n\t"
        "v_perm_b32 %[t], v125, v121, %[slo]\n\t"
        "v_perm_b32 v125, v125, v121, %[shi]\n\t"
        "v_mov_b32 v121, %[t]\n\t"
        "v_perm_b32 %[t], v126, v122, %[slo]\n\t"
        "v_perm_b32 v126, v126, v122, %[shi]\n\t"
        "v_mov_b32 v122, %[t]\n\t"
        "v_perm_b32 %[t], v127, v123, %[slo]\n\t"
        "v_perm_b32 v127, v127, v123, %[shi]\n\t"
        "v_mov_b32 v123, %[t]\n\t"
        "v_perm_b32 v209, v223, v222, %[slo]\n\t"
        "v_perm_b32 %[t], v132, v128, %[slo]\n\t"
        "v_perm_b32 v132, v132, v128, %[shi]\n\t"
        "v_mov_b32 v128, %[t]\n\t"
        "v_perm_b32 %[t], v133, v129, %[slo]\n\t"
        "v_perm_b32 v133, v133, v129, %[shi]\n\t"
        "v_mov_b32 v129, %[t]\n\t"
        "v_perm_b32 %[t], v134, v130, %[slo]\n\t"
        "v_perm_b32 v134, v134, v130, %[shi]\n\t"
        "v_mov_b32 v130, %[t]\n\t"
        "v_perm_b32 %[t], v135, v131, %[slo]\n\t"
        "v_perm_b32 v135, v135, v131, %[shi]\n\t"
        "v_mov_b32 v131, %[t]\n\t"
        "v_perm_b32 v210, v225, v224, %[slo]\n\t"
        "v_perm_b32 %[t], v140, v136, %[slo]\n\t"
        "v_perm_b32 v140, v140, v136, %[shi]\n\t"
        "v_mov_b32 v136, %[t]\n\t"
        "v_perm_b32 %[t], v141, v137, %[slo]\n\t"
        "v_perm_b32 v141, v141, v137, %[shi]\n\t"
        "v_mov_b32 v137, %[t]\n\t"
        "v_perm_b32 %[t], v142, v138, %[slo]\n\t"
        "v_perm_b32 v142, v142, v138, %[shi]\n\t"
        "v_mov_b32 v138, %[t]\n\t"
        "v_perm_b32 %[t], v143, v139, %[slo]\n\t"
        "v_perm_b32 v143, v143, v139, %[shi]\n\t"
        "v_mov_b32 v139, %[t]\n\t"
        "v_perm_b32 v211, v227, v226, %[slo]\n\t"
        "v_perm_b32 %[t], v148, v144, %[slo]\n\t"
        "v_perm_b32 v148, v148, v144, %[shi]\n\t"
        "v_mov_b32 v144, %[t]\n\t"
        "v_perm_b32 %[t], v149, v145, %[slo]\n\t"
        "v_perm_b32 v149, v149, v145, %[shi]\n\t"
        "v_mov_b32 v145, %[t]\n\t"
        "v_perm_b32 %[t], v150, v146, %[slo]\n\t"
        "v_perm_b32 v150, v150, v146, %[shi]\n\t"
        "v_mov_b32 v146, %[t]\n\t"
        "v_perm_b32 %[t], v151, v147, %[slo]\n\t"
        "v_perm_b32 v151, v151, v147, %[shi]\n\t"
        "v_mov_b32 v147, %[t]\n\t"
        "v_perm_b32 v212, v229, v228, %[slo]\n\t"
        "v_perm_b32 %[t], v156, v152, %[slo]\n\t"
        "v_perm_b32 v156, v156, v152, %[shi]\n\t"
        "v_mov_b32 v152, %[t]\n\t"
        "v_perm_b32 %[t], v157, v153, %[slo]\n\t"
        "v_perm_b32 v157, v157, v153, %[shi]\n\t"
        "v_mov_b32 v153, %[t]\n\t"
        "v_perm_b32 %[t], v158, v154, %[slo]\n\t"
        "v_perm_b32 v158, v158, v154, %[shi]\n\t"
        "v_mov_b32 v154, %[t]\n\t"
        "v_perm_b32 %[t], v159, v155, %[slo]\n\t"
        "v_perm_b32 v159, v159, v155, %[shi]\n\t"
        "v_mov_b32 v155, %[t]\n\t"
        "v_perm_b32 v213, v231, v230, %[slo]\n\t"
        "v_perm_b32 %[t], v164, v160, %[slo]\n\t"
        "v_perm_b32 v164, v164, v160, %[shi]\n\t"
        "v_mov_b32 v160, %[t]\n\t"
        "v_perm_b32 %[t], v165, v161, %[slo]\n\t"
        "v_perm_b32 v165, v165, v161, %[shi]\n\t"
        "v_mov_b32 v161, %[t]\n\t"
        "v_perm_b32 %[t], v166, v162, %[slo]\n\t"
        "v_perm_b32 v166, v166, v162, %[shi]\n\t"
        "v_mov_b32 v162, %[t]\n\t"
        "v_perm_b32 %[t], v167, v163, %[slo]\n\t"
        "v_perm_b32 v167, v167, v163, %[shi]\n\t"
        "v_mov_b32 v163, %[t]\n\t"
        "v_perm_b32 v214, v233, v232, %[slo]\n\t"
        "v_perm_b32 %[t], v172, v168, %[slo]\n\t"
        "v_perm_b32 v172, v172, v168, %[shi]\n\t"
        "v_mov_b32 v168, %[t]\n\t"
        "v_perm_b32 %[t], v173, v169, %[slo]\n\t"
        "v_perm_b32 v173, v173, v169, %[shi]\n\t"
        "v_mov_b32 v169, %[t]\n\t"
        "v_perm_b32 %[t], v174, v170, %[slo]\n\t"
        "v_perm_b32 v174, v174, v170, %[shi]\n\t"
        "v_mov_b32 v170, %[t]\n\t"
        "v_perm_b32 %[t], v175, v171, %[slo]\n\t"
        "v_perm_b32 v175, v175, v171, %[shi]\n\t"
        "v_mov_b32 v171, %[t]\n\t"
        "v_perm_b32 v215, v235, v234, %[slo]\n\t"
        "v_perm_b32 %[t], v180, v176, %[slo]\n\t"
        "v_perm_b32 v180, v180, v176, %[shi]\n\t"
        "v_mov_b32 v176, %[t]\n\t"
        "v_perm_b32 %[t], v181, v177, %[slo]\n\t"
        "v_perm_b32 v181, v181, v177, %[shi]\n\t"
        "v_mov_b32 v177, %[t]\n\t"
        "v_perm_b32 %[t], v182, v178, %[slo]\n\t"
        "v_perm_b32 v182, v182, v178, %[shi]\n\t"
        "v_mov_b32 v178, %[t]\n\t"
        "v_perm_b32 %[t], v183, v179, %[slo]\n\t"
        "v_perm_b32 v183, v183, v179, %[shi]\n\t"
        "v_mov_b32 v179, %[t]\n\t"
        "v_perm_b32 v216, v237, v236, %[slo]\n\t"
        "v_perm_b32 %[t], v188, v184, %[slo]\n\t"
        "v_perm_b32 v188, v188, v184, %[shi]\n\t"
        "v_mov_b32 v184, %[t]\n\t"
        "v_perm_b32 %[t], v189, v185, %[slo]\n\t"
        "v_perm_b32 v189, v189, v185, %[shi]\n\t"
        "v_mov_b32 v185, %[t]\n\t"
        "v_perm_b32 %[t], v190, v186, %[slo]\n\t"
        "v_perm_b32 v190, v190, v186, %[shi]\n\t"
        "v_mov_b32 v186, %[t]\n\t"
        "v_perm_b32 %[t], v191, v187, %[slo]\n\t"
        "v_perm_b32 v191, v191, v187, %[shi]\n\t"
        "v_mov_b32 v187, %[t]\n\t"
        "v_perm_b32 v217, v239, v238, %[slo]\n\t"
        "v_perm_b32 %[t], v196, v192, %[slo]\n\t"
        "v_perm_b32 v196, v196, v192, %[shi]\n\t"
        "v_mov_b32 v192, %[t]\n\t"
        "v_perm_b32 %[t], v197, v193, %[slo]\n\t"
        "v_perm_b32 v197, v197, v193, %[shi]\n\t"
        "v_mov_b32 v193, %[t]\n\t"
        "v_perm_b32 %[t], v198, v194, %[slo]\n\t"
        "v_perm_b32 v198, v198, v194, %[shi]\n\t"
        "v_mov_b32 v194, %[t]\n\t"
        "v_perm_b32 %[t], v199, v195, %[slo]\n\t"
        "v_perm_b32 v199, v199, v195, %[shi]\n\t"
        "v_mov_b32 v195, %[t]\n\t"
        "v_perm_b32 v218, v241, v240, %[slo]\n\t"
        "v_perm_b32 %[t], v204, v200, %[slo]\n\t"
        "v_perm_b32 v204, v204, v200, %[shi]\n\t"
        "v_mov_b32 v200, %[t]\n\t"
        "v_perm_b32 %[t], v205, v201, %[slo]\n\t"
        "v_perm_b32 v205, v205, v201, %[shi]\n\t"
        "v_mov_b32 v201, %[t]\n\t"
        "v_perm_b32 %[t], v206, v202, %[slo]\n\t"
        "v_perm_b32 v206, v206, v202, %[shi]\n\t"
        "v_mov_b32 v202, %[t]\n\t"
        "v_perm_b32 %[t], v207, v203, %[slo]\n\t"
        "v_perm_b32 v207, v207, v203, %[shi]\n\t"
        "v_mov_b32 v203, %[t]\n\t"
        "v_perm_b32 v219, v243, v242, %[slo]\n\t"
        "v_mov_b32 %[o1], 153984\n\t"
        "Lw_%=:\n\t"
        "ds_read_b32 %[t], %[o1] offset:%[owc]\n\t"
        "s_sub_u32 %[scn], %[scn], 1\n\t"
        "s_waitcnt lgkmcnt(0)\n\t"
        "v_cmp_gt_u32 vcc, 4, %[t]\n\t"
        "s_cmp_lt_u32 %[scn], 2\n\t"
        "s_cbranch_scc1 Lu_%=\n\t"
        "s_cbranch_vccnz Lw_%=\n\t"
        "Lu_%=:\n\t"
        "ds_read_b64_tr_b16 v[220:221], %[g0] offset:0\n\t"
        "ds_read_b64_tr_b16 v[222:223], %[g0] offset:384\n\t"
        "ds_read_b64_tr_b16 v[224:225], %[g1] offset:0\n\t"
        "ds_read_b64_tr_b16 v[226:227], %[g1] offset:384\n\t"
        "ds_read_b64_tr_b16 v[228:229], %[g2] offset:0\n\t"
        "ds_read_b64_tr_b16 v[230:231], %[g2] offset:384\n\t"
        "ds_read_b64_tr_b16 v[232:233], %[g0] offset:1536\n\t"
        "ds_read_b64_tr_b16 v[234:235], %[g0] offset:1920\n\t"
        "ds_read_b64_tr_b16 v[236:237], %[g1] offset:1536\n\t"
        "ds_read_b64_tr_b16 v[238:239], %[g1] offset:1920\n\t"
        "ds_read_b64_tr_b16 v[240:241], %[g2] offset:1536\n\t"
        "ds_read_b64_tr_b16 v[242:243], %[g2] offset:1920\n\t"
        "ds_read_b64_tr_b16 v[244:245], %[g0] offset:3072\n\t"
        "ds_read_b64_tr_b16 v[246:247], %[g0] offset:3456\n\t"
        "ds_read_b64_tr_b16 v[248:249], %[g1] offset:3072\n\t"
        "ds_read_b64_tr_b16 v[250:251], %[g1] offset:3456\n\t"
        "ds_read_b64_tr_b16 v[252:253], %[g2] offset:3072\n\t"
        "ds_read_b64_tr_b16 v[254:255], %[g2] offset:3456\n\t"
        "Lq_%=:\n\t"
        "v_mov_b32 v60, 0x3f803f80\n\t"
        "v_mov_b32 v61, 0x3f803f80\n\t"
        "v_mov_b32 v62, 0x3f803f80\n\t"
        "v_mov_b32 v63, 0x3f803f80\n\t"
        "v_mov_b32 v64, 0x3f803f80\n\t"
        "v_mov_b32 v65, 0x3f803f80\n\t"
        "v_mov_b32 v66, 0x3f803f80\n\t"
        "v_mov_b32 v67, 0x3f803f80\n\t"
        "v_mov_b32 v68, 0x3f803f80\n\t"
        "v_mov_b32 v69, 0x3f803f80\n\t"
        "v_mov_b32 v70, 0x3f803f80\n\t"
        "v_mov_b32 v71, 0x3f803f80\n\t"
        "s_waitcnt lgkmcnt(0)\n\t"
        "s_setprio 3\n\t"
        "Lp_%=:\n\t"
        "s_waitcnt vmcnt(0)\n\t"
        "v_xor_b32 %[t], %[stag], v85\n\t"
        "v_xor_b32 %[o0], %[stag], v87\n\t"
        "v_or_b32 %[t], %[t], %[o0]\n\t"
        "v_cmp_ne_u32 vcc, 0, %[t]\n\t"
        "s_cbranch_vccz Lf0_%=\n\t"
        "buffer_load_dwordx4 v[84:87], v107, %[ssrd], %[so] offen sc1\n\t"
        "s_sub_u32 %[scn], %[scn], 1\n\t"
        "s_cmp_lg_u32 %[scn], 0\n\t"
        "s_cbranch_scc1 Lp_%=\n\t"
        "s_waitcnt vmcnt(0)\n\t"
        "Lf0_%=:\n\t"
        "v_mov_b32 %[o0], v84\n\t"
        "v_mov_b32 %[o1], v86\n\t"
        "Le_%=:\n\t"
        : [o0] "=&v"(o[0]), [o1] "=&v"(o[1]), [t] "=&v"(t), [scn] "+s"(cn)
        : [ssrd] "s"(srd), [so] "s"(so), [stag] "s"(tag), [g0] "v"(g0), [g1] "v"(g1), [g2] "v"(g2), [slay] "s"(layer), [slo] "s"(0x05040100u), [shi] "s"(0x07060302u), [owc] "n"(BC_WC)
        : "memory", "scc", "vcc");
}
DEV void poll_flag4(u32x4 srd, u32 so, u32 tag, u32& cn) {
    u32 t, d0;
    asm volatile(
        "s_nop 4\n\t"
        "buffer_load_dwordx4 v[84:87], v107, %[ssrd], %[so] offen sc1\n\t"
        "Lp_%=:\n\t"
        "s_waitcnt vmcnt(0)\n\t"
        "v_xor_b32 %[t], %[stag], v84\n\t"
        "v_xor_b32 v85, %[stag], v85\n\t"
        "v_xor_b32 v86, %[stag], v86\n\t"
        "v_xor_b32 v87, %[stag], v87\n\t"
        "v_or3_b32 %[t], %[t], v85, v86\n\t"
        "v_or_b32 %[t], %[t], v87\n\t"
        "v_cmp_ne_u32 vcc, 0, %[t]\n\t"
        "s_cbranch_vccz Lf0_%=\n\t"
        "buffer_load_dwordx4 v[84:87], v107, %[ssrd], %[so] offen sc1\n\t"
        "s_sub_u32 %[scn], %[scn], 1\n\t"
        "s_cmp_lg_u32 %[scn], 0\n\t"
        "s_cbranch_scc1 Lp_%=\n\t"
        "s_waitcnt vmcnt(0)\n\t"
        "Lf0_%=:\n\t"
        "s_nop 0\n\t"
        "Le_%=:\n\t"
        : [d0] "=&s"(d0), [t] "=&v"(t), [scn] "+s"(cn)
        : [ssrd] "s"(srd), [so] "s"(so), [stag] "s"(tag)
        : "memory", "scc", "vcc");
}
DEV void poll_flags(u32x4 srd, u32 fo, u32 tag, u32& cn) {
    u32 t, d0;
    asm volatile(
        "s_nop 4\n\t"
        "s_sleep 5\n\t"
        "buffer_load_dword v48, %[fo], %[ssrd], 0 offen sc1\n\t"
        "Lp_%=:\n\t"
        "s_waitcnt vmcnt(0)\n\t"
        "v_cmp_ne_u32 vcc, %[stag], v48\n\t"
        "s_cbranch_vccz Lf0_%=\n\t"
        "buffer_load_dword v48, %[fo], %[ssrd], 0 offen sc1\n\t"
        "s_sub_u32 %[scn], %[scn], 1\n\t"
        "s_cmp_lg_u32 %[scn], 0\n\t"
        "s_cbranch_scc1 Lp_%=\n\t"
        "s_waitcnt vmcnt(0)\n\t"
        "Lf0_%=:\n\t"
        "s_nop 0\n\t"
        "Le_%=:\n\t"
        : [d0] "=&s"(d0), [t] "=&v"(t), [scn] "+s"(cn)
        : [ssrd] "s"(srd), [fo] "v"(fo), [stag] "s"(tag)
        : "memory", "scc", "vcc");
}
DEV void poll_flagx(u32x4 srd, u32 fo, u32 tag, u32& cn) {
    u32 t, d0;
    asm volatile(
        "s_nop 4\n\t"
        "s_sleep 16\n\t"
        "buffer_load_dword v48, %[fo], %[ssrd], 0 offen sc1\n\t"
        "Lp_%=:\n\t"
        "s_waitcnt vmcnt(0)\n\t"
        "v_cmp_ne_u32 vcc, %[stag], v48\n\t"
        "s_cbranch_vccz Lf0_%=\n\t"
        "buffer_load_dword v48, %[fo], %[ssrd], 0 offen sc1\n\t"
        "s_sub_u32 %[scn], %[scn], 1\n\t"
        "s_cmp_lg_u32 %[scn], 0\n\t"
        "s_cbranch_scc1 Lp_%=\n\t"
        "s_waitcnt vmcnt(0)\n\t"
        "Lf0_%=:\n\t"
        "s_nop 0\n\t"
        "Le_%=:\n\t"
        : [d0] "=&s"(d0), [t] "=&v"(t), [scn] "+s"(cn)
        : [ssrd] "s"(srd), [fo] "v"(fo), [stag] "s"(tag)
        : "memory", "scc", "vcc");
}
DEV void poll_flagh(u32x4 srd, u32 fo, u32 tag, u32& cn) {
    u32 t, d0;
    asm volatile(
        "s_nop 4\n\t"
        "buffer_load_dword v48, %[fo], %[ssrd], 0 offen sc1\n\t"
        "Lp_%=:\n\t"
        "s_waitcnt vmcnt(0)\n\t"
        "v_cmp_ne_u32 vcc, %[stag], v48\n\t"
        "s_cbranch_vccz Lf0_%=\n\t"
        "buffer_load_dword v48, %[fo], %[ssrd], 0 offen sc1\n\t"
        "s_sub_u32 %[scn], %[scn], 1\n\t"
        "s_cmp_lg_u32 %[scn], 0\n\t"
        "s_cbranch_scc1 Lp_%=\n\t"
        "s_waitcnt vmcnt(0)\n\t"
        "Lf0_%=:\n\t"
        "s_nop 0\n\t"
        "Le_%=:\n\t"
        : [d0] "=&s"(d0), [t] "=&v"(t), [scn] "+s"(cn)
        : [ssrd] "s"(srd), [fo] "v"(fo), [stag] "s"(tag)
        : "memory", "scc", "vcc");
}
DEV void poll_ky(u32x4 srd, u32 vk, u32 tag, u32 va, u32 so, u32& k, float (&o)[4], u32& cn) {
    u32 t; u64 m;
    asm volatile(
        "s_nop 4\n\t"
        "s_sleep 8\n\t"
        "buffer_load_dwordx4 v[64:67], %[vk], %[ssrd], 0 offen sc1\n\t"
        "buffer_load_dwordx4 v[48:51], %[va], %[ssrd], %[so0] offen sc1\n\t"
        "buffer_load_dwordx4 v[52:55], %[va], %[ssrd], %[so1] offen sc1\n\t"
        "buffer_load_dwordx4 v[56:59], %[va], %[ssrd], %[so2] offen sc1\n\t"
        "buffer_load_dwordx4 v[60:63], %[va], %[ssrd], %[so3] offen sc1\n\t"
        "Lp_%=:\n\t"
        "s_waitcnt vmcnt(0)\n\t"
        "v_cmp_ne_u32 vcc, %[stag], v65\n\t"
        "v_cmp_ne_u32 %[sm], %[stag], v67\n\t"
        "s_or_b64 %[sm], %[sm], vcc\n\t"
        "v_max3_u32 %[t], v48, v49, v50\n\t"
        "v_max3_u32 %[t], %[t], v51, v52\n\t"
        "v_max3_u32 %[t], %[t], v53, v54\n\t"
        "v_max3_u32 %[t], %[t], v55, v56\n\t"
        "v_max3_u32 %[t], %[t], v57, v58\n\t"
        "v_max3_u32 %[t], %[t], v59, v60\n\t"
        "v_max3_u32 %[t], %[t], v61, v62\n\t"
        "v_max_u32 %[t], %[t], v63\n\t"
        "v_cmp_eq_u32 vcc, -1, %[t]\n\t"
        "s_or_b64 vcc, %[sm], vcc\n\t"
        "s_cbranch_vccz Lf0_%=\n\t"
        "buffer_load_dwordx4 v[64:67], %[vk], %[ssrd], 0 offen sc1\n\t"
        "buffer_load_dwordx4 v[48:51], %[va], %[ssrd], %[so0] offen sc1\n\t"
        "buffer_load_dwordx4 v[52:55], %[va], %[ssrd], %[so1] offen sc1\n\t"
        "buffer_load_dwordx4 v[56:59], %[va], %[ssrd], %[so2] offen sc1\n\t"
        "buffer_load_dwordx4 v[60:63], %[va], %[ssrd], %[so3] offen sc1\n\t"
        "s_sub_u32 %[scn], %[scn], 1\n\t"
        "s_cmp_lg_u32 %[scn], 0\n\t"
        "s_cbranch_scc1 Lp_%=\n\t"
        "s_waitcnt vmcnt(0)\n\t"
        "Lf0_%=:\n\t"
        "v_max_u32 %[k], v64, v66\n\t"
        "v_add_f32 v48, v48, v52\n\t"
        "v_add_f32 v49, v49, v53\n\t"
        "v_add_f32 v50, v50, v54\n\t"
        "v_add_f32 v51, v51, v55\n\t"
        "v_add_f32 v56, v56, v60\n\t"
        "v_add_f32 v57, v57, v61\n\t"
        "v_add_f32 v58, v58, v62\n\t"
        "v_add_f32 v59, v59, v63\n\t"
        "v_add_f32 %[o0], v48, v56\n\t"
        "v_add_f32 %[o1], v49, v57\n\t"
        "v_add_f32 %[o2], v50, v58\n\t"
        "v_add_f32 %[o3], v51, v59\n\t"
        "Le_%=:\n\t"
        : [k] "=&v"(k), [sm] "=&s"(m), [o0] "=&v"(o[0]), [o1] "=&v"(o[1]), [o2] "=&v"(o[2]), [o3] "=&v"(o[3]), [t] "=&v"(t), [scn] "+s"(cn)
        : [ssrd] "s"(srd), [vk] "v"(vk), [stag] "s"(tag), [va] "v"(va), [so0] "s"(so + 0), [so1] "s"(so + 4096), [so2] "s"(so + 8192), [so3] "s"(so + 12288)
        : "memory", "scc", "vcc");
}
DEV void poll_kr(u32x4 srd, u32 v1, u32 v2, int h0, int h1, u32 (&o)[6], u32& cn) {
    u32 t;
    asm volatile(
        "s_nop 4\n\t"
        "s_sleep 12\n\t"
        "buffer_load_dwordx2 v[48:49], %[v1], %[ssrd], 0 offen sc1\n\t"
        "buffer_load_dwordx2 v[50:51], %[v1], %[ssrd], 0 offen offset:2048 sc1\n\t"
        "buffer_load_dwordx2 v[52:53], %[v2], %[ssrd], 0 offen sc1\n\t"
        "Lp_%=:\n\t"
        "s_waitcnt vmcnt(0)\n\t"
        "v_max3_u32 %[t], v48, v49, v50\n\t"
        "v_max3_u32 %[t], %[t], v51, v52\n\t"
        "v_max_u32 %[t], %[t], v53\n\t"
        "v_cmp_eq_u32 vcc, -1, %[t]\n\t"
        "s_cbranch_vccz Lf0_%=\n\t"
        "buffer_load_dwordx2 v[48:49], %[v1], %[ssrd], 0 offen sc1\n\t"
        "buffer_load_dwordx2 v[50:51], %[v1], %[ssrd], 0 offen offset:2048 sc1\n\t"
        "buffer_load_dwordx2 v[52:53], %[v2], %[ssrd], 0 offen sc1\n\t"
        "s_sub_u32 %[scn], %[scn], 1\n\t"
        "s_cmp_lg_u32 %[scn], 0\n\t"
        "s_cbranch_scc1 Lp_%=\n\t"
        "s_waitcnt vmcnt(0)\n\t"
        "Lf0_%=:\n\t"
        "ds_write_b128 %[h0], v[48:51]\n\t"
        "ds_write_b64 %[h1], v[52:53]\n\t"
        "v_mov_b32 %[o0], v48\n\t"
        "v_mov_b32 %[o1], v49\n\t"
        "v_mov_b32 %[o2], v50\n\t"
        "v_mov_b32 %[o3], v51\n\t"
        "v_mov_b32 %[o4], v52\n\t"
        "v_mov_b32 %[o5], v53\n\t"
        "Le_%=:\n\t"
        : [o0] "=&v"(o[0]), [o1] "=&v"(o[1]), [o2] "=&v"(o[2]), [o3] "=&v"(o[3]), [o4] "=&v"(o[4]), [o5] "=&v"(o[5]), [t] "=&v"(t), [scn] "+s"(cn)
        : [ssrd] "s"(srd), [v1] "v"(v1), [v2] "v"(v2), [h0] "v"(h0), [h1] "v"(h1)
        : "memory", "scc", "vcc");
}

DEV int ffn_slice(int b, int& nu) {
    const int j = b >> 3, t = j - 20, t3 = (t * 43) >> 7, r = t - 3 * t3;
    nu = (j >= 20 && r < 2) ? 24 : 16;
    return (b & 7) * 576 + (j < 20 ? j * 16 : 320 + t3 * 64 + r * 24);
}
DEV int gu_grp(int i, int b) {
    const int gi = i + ((b >> 3) & 3) * 2;
    return gi >= (i < 9 ? 9 : 18) ? gi - 9 : gi;
}
DEV void gu_dma(const u16* wg, const u16* wu, int lane, int b, int i0, int i1, bool keep) {
    int nu;
    const int f0 = ffn_slice(b, nu);
    u32 gv[3];
#pragma unroll
    for (int m = 0; m < 3; ++m) {
        const int q = 64 * m + lane, sg = (q * 171) >> 9, part = q - 3 * sg;
        const int kin = 32 * (sg >> 5) + 16 * ((sg >> 4) & 1) + 8 * ((sg >> 2) & 1) + 4 * ((sg >> 3) & 1) + (sg & 3);
        gv[m] = (u32)kin * 9216u + f0 * 2u + ((part == 2 && nu == 16) ? 16u : part * 16u);
    }
    if (keep) sts128(L_HROW + lane * 16, (u32x4){gv[0], gv[1] - 1024, gv[2] - 2048, gv[0]});
#pragma nounroll
    for (int i = i0; i < i1; ++i) {
        int gi = gu_grp(i, b);
        const u32x4 sd = mk_srd(gi < 9 ? wg : wu, 5308416);
        asm volatile("" : "+s"(gi));
        int g9 = gi - 9 * ((gi * 57) >> 9);
        asm volatile("" : "+s"(g9));
        const u32 so = g9 * (64 * 9216), ld = L_G + gi * 3072;
#pragma unroll
        for (int m = 0; m < 3; ++m) dma16g(sd, gv[m], so, ld + m * 1024);
    }
}

struct RowS { float m0, var, m, rs; u32 key; };
DEV RowS row_key(const float (&t)[3], float wrsum, u32 idx) {
    RowS q;
    q.m0 = t[0] * (1.f / 576.f);
    q.var = fmaxf(__builtin_fmaf(t[1], 1.f / 576.f, -(q.m0 * q.m0)), 0.f);
    q.m = ln_m(q.m0); q.rs = ln_rs(q.var);
    const u32 sb = pk2(0.f, q.rs * __builtin_fmaf(-q.m, wrsum, t[2]));
    q.key = ((sb ^ ((u32)((int)sb >> 31) | 0x80000000u)) & 0xffff0000u) | idx | 0x800u;
    return q;
}
DEV u32 xt_update(u32 (&XT)[10], int lane, int w, u32 target) {
    xt_get(XT);
    LWAIT0(L_BC + BC_TF, target, 24);
    const u32x4 q = lds128(L_MX + (16 * w + (lane & 15)) * 16);
    const float qm = __uint_as_float(q.x), qr = __uint_as_float(q.y);
#pragma unroll
    for (int i = 0; i < 10; ++i) {
        const float x = lo16(XT[i]), y = hi16(XT[i]);
        const u32 h = pk2((x - qm) * qr, (y - qm) * qr);
        XT[i] = pk2(x + lo16(h), y + hi16(h));
    }
    return q.z;
}
DEV u32 wmax(u32 k) {
    k = rmax(k);
    return umax2(umax2(__builtin_amdgcn_readlane(k, 0), __builtin_amdgcn_readlane(k, 16)), umax2(__builtin_amdgcn_readlane(k, 32), __builtin_amdgcn_readlane(k, 48)));
}

DEV void gu_pf(const u16* wg, const u16* wu, int lane, int b, int i) {
    const int j = b >> 3;
    const u32x4 sg = mk_srd(i < 3 ? wg : wu, 5308416);
    const int r = (lane * 57) >> 9;
    const u32 o = lane < 54 ? (u32)(j * 18 + (i < 3 ? i : i - 3) * 6 + r) * 9216u + (b & 7) * 1152u + (lane - 9 * r) * 128u : (u32)OOB;
    asm volatile("s_nop 4\n\tbuffer_load_dword v98, %0, %1, 0 offen" :: "v"(o), "s"(sg) : "memory");
}

DEV u32 w_voff(int wp, int r16, int g, int cb) {
    int ch = 16 * wp + r16; if (ch > 107) ch = 107;
    const int sec = (ch * 57) >> 11, c = ch - 36 * sec;
    return (u32)(8 * g) * 3456u + (sec == 2 ? 0u : (u32)(sec + 1) * 1152u) + cb * 144u + c * 4u;
}

__global__ void NOPK __attribute__((amdgpu_num_vgpr(24))) __launch_bounds__(512, 2)
mega(const u16* __restrict__ x0, const u16* __restrict__ wqkv, const u16* __restrict__ wo, const u16* __restrict__ wr,
     const u16* __restrict__ wg, const u16* __restrict__ wu, const u16* __restrict__ wd, u16* __restrict__ out, char* __restrict__ ws) {
    const int w_ = __builtin_amdgcn_readfirstlane(threadIdx.x >> 6);
    const int b_ = blockIdx.x;
    {
        u64 pc; u32 off;
        asm volatile("s_getpc_b64 %0\n"
                     "Lcode0:\n\t"
                     "v_lshlrev_b32 %1, 7, %2\n\t"
                     "v_and_b32 v99, 3, %2\n\t"
                     "v_cmp_gt_u32 vcc, Lcode1-Lcode0, %1\n\t"
                     "s_mov_b64 exec, vcc\n\t"
                     "v_cmp_eq_u32 vcc, %3, v99\n\t"
                     "s_and_b64 exec, exec, vcc\n\t"
                     "s_cbranch_execz Lcode2\n\t"
                     "global_load_dword v99, %1, %0\n"
                     "Lcode2:\n\t"
                     "s_mov_b64 exec, -1"
                     : "=&s"(pc), "=&v"(off) : "v"(threadIdx.x), "s"((b_ >> 3) & 3) : "vcc", "scc", "memory");
    }
    const __amdgpu_buffer_rsrc_t rs = __builtin_amdgcn_make_buffer_rsrc((void*)ws, 0, WS_BYTES, 0x00020000);
    asm volatile("v_mov_b32 v109, 0");
    u32 tag_;
    {
        IDS
        int u = rg * 4 + (w & 3);
        const bool uok = u < 126;
        if (u > 125) u = 125;
        const int su = (u * 147) >> 10, wp = u - 7 * su;
        u32 wv = w_voff(wp, r16, g, cb), so = (u32)su * 110592u;
        sts32(L_BC + lane * 4, 0u);
        asm volatile("" : "+v"(wv), "+s"(so));
        asm volatile("" :: "s"(x0), "s"(wqkv), "s"(wo), "s"(ws));
        wld8p(mk_srd(wqkv, 1990656), wv, so, w);
        bar();
        { const char* ep_ = ws; asm volatile("" : "+s"(ep_)); tag_ = *(const __attribute__((address_space(4))) u32*)(ep_ + OFF_EPOCH) + 1u; }
        wunit_st(mk_srd(ws, WS_BYTES), (uok && !(wp == 6 && r16 >= 12)) ? OFF_WI + ((cb * 7 + wp) * 18 + su) * 2048 + (wp == 6 ? g * 12 + r16 : lane) * 16 : OOB, w);
        x_dma(mk_srd(x0 - 9216, 2359296 + 18432), (u32)((rg * 64 + (lane >> 3)) * 1152) + (u32)(((lane ^ (lane >> 4)) & 7) << 4), rg == 0 ? OOB : 0,
              ((XCI >> (4 * w)) & 15) + cb, (XCN >> (4 * w)) & 15);
        if (w < 4) {
            const int fa = lane < WREP ? OFF_FLAG + ((lane * 8 + cb) * 32 + rg) * FST : OOB;
            if (w > 0) { VMWAIT(10); } else { VMWAIT(0); }
            BST32(tag_, lds_inc_rtn(L_BC + BC_FC) == 3u ? fa : OOB, 0);
            __builtin_amdgcn_s_setprio(0);
        }
#define PFI(w) ((w) >= 4 ? (w) - 4 : (w) + 2)
#define PFON(w) ((w) > 0 && (w) < 7 && PFI(w) < PFA)
        if (PFON(w)) gu_pf(wg, wu, lane, b, PFI(w));
        if (w == 6) {
#pragma unroll
            for (int i = 0; i < 3; ++i) {
                const int q = lane + 64 * i;
                if (q < 144) sts128((q >> 4) * XCH + (q & 15) * 16, (u32x4){ONES2, ONES2, ONES2, ONES2});
            }
        }
        lc_set(lane_consts(w_, b_));
    }
    MidS a;
    {
        IDS
        const int rt4 = w & 3, kh = w >> 2, hp = 2 * cb + kh;
        const u32 obase = OFF_OT + (rg * 4 + rt4) * OTS;
        const u32 wl = OFF_WOI + cb * 82944;
        const u32 lo = w < 4 ? 16384 + w * 6144 : (w < 7 ? 64512 + (w - 4) * 6144 : 57344), eo = kh * 41472 + rt4 * 4096;
        a.ob = obase + kh * 9216; a.st = obase + hp * 1024; a.tt = obase + 16384 + hp * 256;
        a.c0 = (rg == 0 && rt4 == 0) ? 0xff61b1e6u : 0u;
        a.xf = kh ? OFF_KEYE + cb * 4096 + (rg * 4 + rt4) * 8 : OFF_XFLAG + ((rg * 8 + cb) * 4 + w) * XFS;
        a.wi = wl + lo; a.ld = lo >= 64512 ? lo - 64512 : L_WOE + lo; a.wx = wl + eo; a.ldx = L_WOE + eo;
        a.msk = (u32)(kh - 1); a.scr = rt4 == 2 ? L_SCR3 : L_SCR + (rt4 == 3 ? 2 : rt4) * 4608; a.cr = OFF_CROW + (rg * 4 + rt4) * 1280 + cb * 144;
        a.stat = OFF_STATS + (rg * 4 + rt4) * 2048 + cb * 256;
        a.x1 = kh ? OFF_KEYE - 32768 + (b & 31) * 1024 : OFF_X1G + (rg * 4 + rt4) * 18432 + cb * 2304;
        asm volatile("" : "+s"(a.ob), "+s"(a.st), "+s"(a.tt), "+s"(a.c0), "+s"(a.wi), "+s"(a.ld), "+s"(a.wx), "+s"(a.ldx), "+s"(a.msk),
                          "+s"(a.scr), "+s"(a.stat), "+s"(a.x1), "+s"(a.cr), "+s"(a.xf));
    }
    asm volatile("" : "+s"(tag_));
#define VA_MASK (CSL(w) < 6 ? ~0ull : 0x0000FFFFFFFFFFFFull)
#define V0_MASK (CSL(w) < 4 ? ~0ull : (w == 4 ? 0xFFFFFFFFull : 0ull))
    a.skip = 0;
#pragma nounroll
    for (int layer = 0; layer < 2; ++layer) {
    u32 tag = tag_; asm volatile("" : "+s"(tag));
    u64 gub = 0; u32 gul = 0;
    if (layer == 0) {
    {
        IDS
        const int gi = gu_grp(GN1 + __builtin_popcount(GWM & ((1u << w) - 1u)), b);
        gub = ((u64)(gi < 9 ? wg : wu) + (gi < 9 ? gi : gi - 9) * (64 * 9216)) & 0xffffffffffffull;
        gul = (GWM >> w & 1) ? L_G + gi * 3072 : 0;
        asm volatile("" : "+s"(gub), "+s"(gul));
    }
    if (w_ < 7) {
        IDS
        const u32x4 sws = mk_srd(ws, WS_BYTES);
        const u32 fo = w > 0 ? OOB : OFF_GFLAG + (lane < 18 ? lane * 8 + cb : rg * 8 + (lane & 7)) * 16;
        int xt[3];
        {
            const int row = 16 + 16 * (w & 3) + r16;
#pragma unroll
            for (int p = 0; p < 3; ++p) {
                const int pc = 9 * cb + 4 * p + (p < 2 ? g : 0);
                xt[p] = (pc >> 3) * XCH + row * 128 + (((pc ^ (row >> 1)) & 7) << 4) + (p < 2 ? 0 : (g & 1) * 8);
            }
            asm volatile("" : "+v"(xt[0]), "+v"(xt[1]), "+v"(xt[2]));
        }
        if (w >= 1) {
            if (PFON(w)) { VMWAIT(1); } else { VMWAIT(0); }
            lds_inc(L_BC + BC_XC, lane);
            if (w < 4) { gu_dma(wg, wu, lane, b, w - 1, w, false); }
        }
        if (w >= 4) {
            LWAIT0(L_BC + BC_XC, 6, 22);
            float xs[2], xq[2];
            xstats((w - 4) * 2048 + r16 * 128 + (((g ^ (r16 >> 1)) & 7) << 4), xs, xq);
#pragma unroll
            for (int t = 0; t < 2; ++t) {
                const int row = 16 * (w - 4 + 3 * t) + r16;
                float m = xs[t] * (1.f / 576.f);
                const float xv = fmaxf(xq[t] * (1.f / 576.f) - m * m, 0.f);
                float rsd = ln_rs(xv);
                m = ln_m(m);
                if (g == (r16 >> 2) && row < 80 && row >= 16) sts32(L_VAR + (row - 16) * 4, __float_as_uint(xv));
                if (row < 2) { m = 0.f; rsd = 0.f; }
                if (g == (r16 >> 2) && row < 80) sts64(L_MR + row * 8, (u32x2){__float_as_uint(m), __float_as_uint(rsd)});
            }
            lds_inc(L_BC + BC_MC, lane);
        }
        if (w == 0) {
            u32 cn = SPIN_MAX;
            poll_flags(sws, OFF_FLAG + ((rg & (WREP - 1)) * 256 + cb * 32 + (lane & 31)) * FST, tag, cn);
            sts32v(L_BC + BC_GC, 1u);
            if (cn == 0) TMO(3);
        } else {
            LWAIT0(L_BC + BC_GC, 1, 23);
        }
        LWAIT0(L_BC + BC_XC, 6, 22);
        {
            const u32 ib = OFF_WI + (cb * 7 + CSL(w)) * 36864;
            u32 cn = SPIN_MAX;
            gemm0(sws, r16 * 128 + (((g ^ (r16 >> 1)) & 7) << 4), (CSL(w) == 6 ? g * 12 + (r16 < 12 ? r16 : 11) : lane) * 16, ib, xt, fo, w > 0 ? 0u : tag, L_CS + w * 128, VA_MASK, V0_MASK, cn);
            if ((int)cn < 1) TMO(25);
        }
    } else {
        IDS
        __builtin_amdgcn_s_setprio(3);
        {
            u32 U[3][8];
            const int ks = rg < 18 ? rg : 0;
            const u32 rb = rg < 18 ? (u32)(ks < 16 ? 36 * ks + 4 * g : 36 * (8 * (ks - 16) + 2 * g) + 16) * 1152u + cb * 144u : (u32)OOB;
#pragma unroll
            for (int p = 0; p < 3; ++p) old8(U[p], mk_srd(wo, 663552), rb + p * 64u + (p < 2 ? r16 * 4u : ((r16 & 7) >> 1) * 4u), (ks < 16 ? 20 : 36) * 1152);
            u32 sw_ = SENT; asm volatile("" : "+v"(sw_));
            const u32x4 sv = {sw_, sw_, sw_, sw_};
            const int ob = OFF_OT + rg * 4 * OTS;
            LWAIT(L_BC + BC_FC, 4, 43);
#pragma unroll
            for (int i = 0; i < 8; ++i) BST128(sv, lane * 16 + (i & 1) * 1024, ob + (i >> 1) * OTS + cb * 2048);
            BST128(sv, g * OTS + r16 * 16, ob + 16384 + cb * 512); BST128(sv, g * OTS + r16 * 16 + 256, ob + 16384 + cb * 512);
            BST128(sv, g * 2048 + r16 * 16, OFF_STATS + rg * 4 * 2048 + cb * 256);
            const int wb = rg < 18 ? OFF_WOI + cb * 82944 + ks * 4608 : OOB;
#pragma unroll
            for (int p = 0; p < 3; ++p) {
                vmwait8n(U[p], 11);
                u32x4 f0, f1;
                wfrag(U[p], f0, f1);
                if (p < 2) { BST128(f0, wb + p * 2048 + lane * 16, 0); BST128(f1, wb + p * 2048 + 1024 + lane * 16, 0); }
                else BST128((r16 & 1) ? f1 : f0, r16 < 8 ? wb + 4096 + (g * 8 + r16) * 16 : OOB, 0);
            }
        }
#define UV_LOAD() do { const u32 oa = rg < 18 ? (u32)(72 * cb + 4 * rg + g) * 1152u + r16 * 16u : (u32)OOB; \
            uv_load(mk_srd(wr, 1152), mk_srd(wo, 663552), r16 * 16u, oa, r16 < 8 ? oa : (u32)OOB); } while (0)
#define UV_DOTS() do { \
            float d[4]; \
            uv_dots(d); \
            const float wsum = rsum(d[0]), wmean = wsum * (1.f / 576.f); \
            const int q = rg >= 9 ? rg - 9 : rg; \
            const u32 u = __float_as_uint((rsum(d[1]) - wmean * rsum(d[2])) * (q == 4 ? 0.25f : 1.f)); \
            const u32 c = __float_as_uint(gsum(rsum(d[3])) * (36.f / 32.f / 2304.f)) & 0xffff0000u; \
            const u32 r0 = pk2(__uint_as_float(__builtin_amdgcn_readlane(u, 0)), __uint_as_float(__builtin_amdgcn_readlane(u, 16))); \
            const u32 r1 = pk2(__uint_as_float(__builtin_amdgcn_readlane(u, 32)), __uint_as_float(__builtin_amdgcn_readlane(u, 48))); \
            BST128(q < 4 ? ((u32x4){tag, c, r0, r1}) : ((u32x4){r0, r1, c, tag}), (rg < 18 && lane == 0) ? OFF_UV + (2 * cb + (rg >= 9)) * 256 + q * 16 : OOB, 0); \
            if (lane < 36) sts32(L_WR + 192 + lane * 4, ONES2); \
            if (lane == 0) { sts32(L_BC + BC_WRS, __float_as_uint(wsum)); sts32(L_WR + 336, __float_as_uint(wmean)); } \
        } while (0)
        UV_LOAD();
        u32 wq;
        bld32(wq, mk_srd(wr, 1152), lane < 32 ? cb * 144u + lane * 4u : ((lane < 40 && !(lane & 2)) ? cb * 144u + 128u + ((lane >> 2) & 1) * 8u + (lane & 1) * 4u : (u32)OOB), 0u, 0);
        { int nu; const int f0 = ffn_slice(b, nu); dload(mk_srd(wd, (f0 + nu) * 1152), lane * 2u, f0 * 1152u); }
        asm volatile("s_waitcnt vmcnt(48)" : "+v"(wq) :: "memory");
        if (lane == 0) BST32(tag, OFF_GFLAG + (rg * 8 + cb) * 16, 0);
        if (lane < 48) sts32(L_WR + lane * 4, wq);
        UV_DOTS();
        asm volatile("s_nop 4\n\tbuffer_load_dword v98, %0, %1, 0 offen sc1" :: "v"(lane < 4 ? (lane < 2 ? OFF_KEY : OFF_KEYE - 256) + b * 256 + lane * 128 : (lane == 4 ? OFF_XFLAG + b * 128 : (lane == 5 ? OFF_HX + (rg * 8 + cb) * 10240 : OOB))), "s"(mk_srd(ws, WS_BYTES)) : "memory");
        LWAIT(L_BC + BC_GC, 1, 31);
        __builtin_amdgcn_s_sleep(20);
        __builtin_amdgcn_s_setprio(3);
#pragma unroll
        for (int i = PFA; i < 6; ++i) gu_pf(wg, wu, lane, b, i);
        VMWAIT(0);
        __builtin_amdgcn_s_setprio(3);
#define UV_DMA() asm volatile("s_mov_b32 m0, %0\n\ts_mov_b32 exec_hi, 0\n\ts_nop 2\n\tbuffer_load_dwordx4 %1, %2, %3 offen sc1 lds\n\ts_mov_b32 exec_hi, -1" \
                              :: "s"(L_UV), "v"(lane * 16), "s"(mk_srd(ws, WS_BYTES)), "s"(OFF_UV + cb * 512) : "memory")
        UV_DMA();
        gu_dma(wg, wu, lane, b, 3, GN1, true);
        vmwaitn(3 * (GN1 - 3));
        {
            int it = 0;
            const int ta = L_UV + (g & 1) * 256 + (r16 < 4 ? r16 * 16 : (r16 < 9 ? r16 : 8) * 16 + 12);
#pragma nounroll
            while (__builtin_expect(__builtin_amdgcn_ballot_w64(lds32v(ta) != tag) != 0ull, 0)) {
                if (++it > SPIN_MAX) { TMO(19); break; }
                UV_DMA();
                VMWAIT(0);
            }
        }
        {
            u32 sw_ = SENT; asm volatile("" : "+v"(sw_));
            const u32x4 sv = {sw_, sw_, sw_, sw_};
            const int l16 = lane * 16;
#pragma unroll
            for (int l = 0; l < 2; ++l) {
                BST128(sv, (r16 < 9 ? g * 1280 + cb * 144 + r16 * 16 : OOB), OFF_CROW + (l * 128 + rg * 4) * 1280);
            }
            if (b < 144) {
#pragma unroll
                for (int i = 0; i < 4; ++i) BST128(sv, l16 + i * 1024, OFF_YP + b * 4096);
            }
            if (b >= 246) BST128(sv, l16, OFF_X2R + (b - 246) * 1024);
            const int ob = OFF_OT + TILES_B + rg * 4 * OTS;
#pragma unroll
            for (int i = 0; i < 8; ++i) BST128(sv, l16 + (i & 1) * 1024, ob + (i >> 1) * OTS + cb * 2048);
            BST128(sv, g * OTS + r16 * 16, ob + 16384 + cb * 512); BST128(sv, g * OTS + r16 * 16 + 256, ob + 16384 + cb * 512);
            BST128(sv, g * 2048 + r16 * 16, OFF_STATS + STATS_B + rg * 4 * 2048 + cb * 256);
        }
        __builtin_amdgcn_s_setprio(0);
    }
    }
#define XFA(r) (OFF_XFLAG + ((r) * 32 + (lane & 31)) * XFS)
#define FFN_ADDR(h) \
            const int q4 = r16 & 3; \
            const int sl = (16 * (g >> 1) + 4 * (g & 1) + (r16 >> 2)) * 48 + (h) * 7680; \
            int g0 = L_G + sl + q4 * 8, g1 = L_U + sl + q4 * 8, g2 = (q4 < 2 ? L_G + q4 * 8 : L_U + (q4 - 2) * 8) + sl + 32; \
            u32 c0 = TBIAS + (h) * 320 + g * 16; \
            int pa = L_FP + (h) * 1536 + lane * 16, ct = L_BC + BC_CT + layer * 64 + (r16 < 7 ? r16 : 7) * 8;
        {
            IDS
            u32 cn = LSPIN_MAX / 4;
            a.srd = mk_srd(ws, WS_BYTES);
            mid(a, tag, gub, gul, 4 * layer + 4, cn);
            a.ob += TILES_B; a.st += TILES_B; a.tt += TILES_B; a.stat += STATS_B; a.cr += 128 * 1280; a.xf += 32768;
            asm volatile("" : "+s"(a.ob), "+s"(a.st), "+s"(a.tt), "+s"(a.stat), "+s"(a.cr), "+s"(a.xf));
            if (cn == 0) TMO(4);
        }
        if (w_ >= 4) {
            IDS
            __builtin_amdgcn_s_setprio(0);
            if (w < 7) {
                if (layer == 0) LWAIT(L_BC + BC_WC, 4, 27);
                FFN_ADDR(w - 4)
                u32 cn = LSPIN_MAX;
                ffn_help(mk_srd(ws, WS_BYTES), c0, g0, g1, g2, pa, L_BC + BC_HF + (w - 4) * 4, ct, layer + 1, cn);
                if ((int)cn < 1) TMO(38);
                __builtin_amdgcn_s_setprio(0);
            }
        } else {
            IDS
            if (w == 0) {
                float wrsum = __uint_as_float(lds32(L_BC + BC_WRS));
                int ka = r16 < 4 ? OFF_KEY + (layer * 32 + cb * 4 + r16) * 1024 + (rg * 4 + g) * 8 : OOB;
                u32 idx = 2047 - rg * 64 - lane, tv = tag;
                asm volatile("" : "+v"(wrsum), "+v"(ka), "+v"(idx), "+v"(tv));
                float t[3];
                u32 cn = SPIN_MAX;
                poll_stats(mk_srd(ws, WS_BYTES), g * 2048 + r16 * 16, OFF_STATS + layer * STATS_B + rg * 8192, t, cn);
                const RowS q = row_key(t, wrsum, idx);
                const u32 tb = rmax(q.key);
                BST64(((u32x2){tb, tv}), ka, 0);
                CFENCE();
                sts128(L_MX + lane * 16, (u32x4){__float_as_uint(q.m), __float_as_uint(q.rs), tb, tb});
                CFENCE();
                sts32v(L_BC + BC_TF, layer + 1);
                sts64(L_MR + (16 + lane) * 8, ln_next(q.m0, q.var, q.m, q.rs));
                lds_inc(L_BC + BC_MC, lane);
                sts32(L_VAR + lane * 4, __float_as_uint((1.f + q.rs) * (1.f + q.rs) * q.var));
                if (cn == 0) TMO(5);
            }
            const u32x4 sws = mk_srd(ws, WS_BYTES);
            if (layer == 0 && w == 3) {
                u32 cn = SPIN_MAX;
                poll_flagx(sws, XFA(rg), tag, cn);
                gather(sws, OFF_X1G + rg * 73728);
                if (cn == 0) TMO(6);
                LWAIT(L_BC + BC_HF + 12, 1, 28);
            } else {
                {
                    u32 XT[10];
                    const u32 best = xt_update(XT, lane, w, layer + 1);
                    if (layer == 1) {
                        const int grow = (rg * 4 + w) * 16 + r16;
                        const bool isb = (best & 0x7ffu) == (u32)(2047 - grow);
                        const __amdgpu_buffer_rsrc_t ro = __builtin_amdgcn_make_buffer_rsrc((void*)out, 0, 2359296, 0x00020000);
                        const int ob = grow * 1152 + cb * 144;
                        const u32x4 v0 = {XT[0], XT[1], XT[2], XT[3]}, v1 = {XT[4], XT[5], XT[6], XT[7]};
                        const u32x2 v2 = {XT[8], XT[9]};
                        if (!isb) {
                            __builtin_amdgcn_raw_buffer_store_b128(v0, ro, ob + g * 16, 0, 0);
                            __builtin_amdgcn_raw_buffer_store_b128(v1, ro, ob + 64 + g * 16, 0, 0);
                            if (g < 2) __builtin_amdgcn_raw_buffer_store_b64(v2, ro, ob + 128 + g * 8, 0, 0);
                        }
                        LWAIT(L_BC + BC_WIN, 1, 17);
                        const bool held = isb && ((best ^ lds32v(L_BC + BC_WIN)) & 0x7ffu) == 0u;
                        if (isb && !held) {
                            __builtin_amdgcn_raw_buffer_store_b128(v0, ro, ob + g * 16, 0, 16);
                            __builtin_amdgcn_raw_buffer_store_b128(v1, ro, ob + 64 + g * 16, 0, 16);
                            if (g < 2) __builtin_amdgcn_raw_buffer_store_b64(v2, ro, ob + 128 + g * 8, 0, 16);
                        }
                        LWAIT(L_BC + BC_FIN, 1, 13);
                        if (__builtin_expect(__builtin_amdgcn_readfirstlane(lds32v(L_BC + BC_FIN)) == 1u, 0)) {
                            u32 X[10];
                            xt_get(X);
                            BST128(((u32x4){X[0], X[1], X[2], X[3]}), a.x1 + lane * 16, 0);
                            BST128(((u32x4){X[4], X[5], X[6], X[7]}), a.x1 + 1024 + lane * 16, 0);
                            BST64(((u32x2){X[8], X[9]}), g < 2 ? a.x1 + 2048 + r16 * 16 + g * 8 : OOB, 0);
                            VMWAIT(0);
                            u32 tg = tag; asm volatile("" : "+s"(tg));
                            BST32(tg, lane == 0 ? OFF_XF2 + ((rg * 8 + cb) * 4 + w) * XFS : OOB, 0);
                            if (held) {
                                __builtin_amdgcn_raw_buffer_store_b128(v0, ro, ob + g * 16, 0, 0);
                                __builtin_amdgcn_raw_buffer_store_b128(v1, ro, ob + 64 + g * 16, 0, 0);
                                if (g < 2) __builtin_amdgcn_raw_buffer_store_b64(v2, ro, ob + 128 + g * 8, 0, 0);
                            }
                        }
                        break;
                    }
                    xt_set(XT);
                }
            }
        }
        if (w_ < 7) {
            if (layer == 0) {
                IDS
                int xr = L_XR + cb * 144 + g * 16, xq = L_XR + cb * 144 + 128 + (g & 1) * 8, fb = L_BC;
                asm volatile("" : "+v"(xr), "+v"(xq), "+v"(fb));
                const u32 hx = OFF_HX + ((rg * 8 + cb) * 5 + CSL(w)) * 2048;
                LWAIT0(L_BC + BC_G0, 1, 16);
                u32 cn = LSPIN_MAX, go;
                gemm1a(mk_srd(ws, WS_BYTES), hx, rg > 0 ? hx - 81920 : hx, tag, fb, L_CS + w * 128, VA_MASK, V0_MASK, go, cn);
                a.skip = 3;
                if (w < 4) {
                    a.skip = 1;
                    if (w < 3) {
                        const u32x4 sws = mk_srd(ws, WS_BYTES);
                        const u32 wl = OFF_WOI + cb * 82944 + (5 + 2 * w) * 4096, dl = L_WOE + (5 + 2 * w) * 4096;
                        LWAIT0(L_BC + BC_C1, 7, 48);
                        dma16x4_sc1(sws, lane * 16u, wl, dl);
                        if (w < 2) dma16x4_sc1(sws, lane * 16u, wl + 4096, dl + 4096);
                    }
                    if (w == 3) { u32 XT[10]; xt_update(XT, lane, w, 1); xt_set(XT); }
                }
                if (go != 1u) gemm1b(fb, xr, xq, w + 1, VA_MASK, V0_MASK, cn);
                if ((int)cn < 1) TMO(16);
            }
        } else {
            u32 best = 0, yb = OFF_YP + layer * YP_B, xb = OFF_X2R;
#pragma nounroll
            for (;;) {
                IDS
                FFN_ADDR(3)
                u32 c1 = 1024 + (lane & 7) * 16;
                u32 ya = (lane >> 2) * 16384u + (lane & 3) * 16u, ye = (32 + g) * 16384u + r16 * 4u;
                int pg = L_FP + 268 + (g == 0 ? r16 * 16 : 1024 + (r16 & 7) * 16), pu = L_FP + 268 + (g == 0 ? 512 + r16 * 16 : 1152 + (r16 & 7) * 16), ff = L_BC + BC_HF;
                u32 sg = TBIAS + g * 256, e0 = OFF_CROW - TBIAS + (layer * 128 + 2 * lane) * 1280, e1 = OFF_STATS - TBIAS + layer * STATS_B + lane * 4096 + 240;
                float wrsum = __uint_as_float(lds32(L_BC + BC_WRS));
                asm volatile("" : "+v"(g0), "+v"(g1), "+v"(g2), "+v"(c0), "+v"(c1), "+v"(ya), "+v"(ye), "+v"(pa), "+v"(pg), "+v"(pu), "+v"(ff), "+v"(ct), "+v"(sg), "+v"(e0), "+v"(e1), "+v"(wrsum));
                const u32x4 sws = mk_srd(ws, WS_BYTES);
                if (best == 0u) {
                    u32 ke[2];
                    u32 cn = SPIN_MAX;
                    poll_keye(sws, OFF_KEYE + (layer * 32 + (b & 31)) * 1024, tag, layer, g0, g1, g2, ke, cn);
                    if (cn == 0) TMO(7);
                    const u32 top = wmax(umax2(ke[0], ke[1]));
                    u32 thr = umax2(top, KDEL) - KDEL;
                    u64 m0 = __builtin_amdgcn_ballot_w64(ke[0] >= thr && ke[0] != top), m1 = __builtin_amdgcn_ballot_w64(ke[1] >= thr && ke[1] != top);
#pragma nounroll
                    while (__builtin_expect(__builtin_popcountll(m0) + __builtin_popcountll(m1) > 6, 0)) {
                        thr += (top - thr + 7u) >> 3;
                        m0 &= __builtin_amdgcn_ballot_w64(ke[0] >= thr); m1 &= __builtin_amdgcn_ballot_w64(ke[1] >= thr);
                    }
                    const bool s0 = __builtin_amdgcn_inverse_ballot_w64(m0), s1 = __builtin_amdgcn_inverse_ballot_w64(m1);
                    const u32 p0 = __builtin_amdgcn_mbcnt_hi((u32)(m0 >> 32), __builtin_amdgcn_mbcnt_lo((u32)m0, 1u));
                    const u32 p1 = __builtin_amdgcn_mbcnt_hi((u32)(m1 >> 32), __builtin_amdgcn_mbcnt_lo((u32)m1, 1u + __builtin_popcountll(m0)));
                    const int cb_ = L_BC + BC_CT + layer * 64;
                    LWAIT0(L_BC + BC_PK, 4 * layer + 4, 33);
                    if (s0) sts64(cb_ + p0 * 8, (u32x2){e0, e1 - (ke[0] & 15u) * 16});
                    if (s1) sts64(cb_ + p1 * 8, (u32x2){e0 + 1280, e1 + 2048 - (ke[1] & 15u) * 16});
                    CFENCE();
                    const u32 tr = ~top & 0x7ffu;
                    sts64(cb_, (u32x2){OFF_CROW - TBIAS + (layer * 128 + (tr >> 4)) * 1280, OFF_STATS - TBIAS + layer * STATS_B + (tr >> 4) * 2048 + (tr & 15u) * 16});
                    CFENCE();
                }
                const u32x2 ce = lds64(ct);
                u32 cn = LSPIN_MAX, win;
                {
                    float t[3];
                    c0 += ce.x;
                    u32 idx = ~(((ce.y + TBIAS - OFF_STATS) >> 7 & 0x7f0u) | (ce.y >> 4 & 15u)) & 0x7ffu;
                    asm volatile("" : "+v"(idx));
                    ffn_a(sws, c0, sg + ce.y, t, cn);
                    gsum3(t);
                    const RowS q = row_key(t, wrsum, idx);
                    const u32 key = ce.x != 0u ? q.key : 0u;
                    win = rmax(key);
                    const int sl_ = __builtin_ctzll(__builtin_amdgcn_ballot_w64(key == win));
                    sts32v((layer == 1 && best == 0u) ? L_BC + BC_WIN : L_BC + BC_NIL, win);
                    win = __builtin_amdgcn_readfirstlane(win);
                    ffn(sws, __builtin_amdgcn_readlane(ce.x, sl_) + TBIAS, c0, c1, pa, ff, pg, pu, (sl_ >> 2) * 256 + (sl_ & 3) * 4 - 268,
                        __uint_as_float(__builtin_amdgcn_readlane(__float_as_uint(q.m), sl_)), __uint_as_float(__builtin_amdgcn_readlane(__float_as_uint(q.rs), sl_)), layer + 1,
                        ya, ye, yb + b * 64, cn);
                }
                if ((int)cn < 1) TMO(14);
                if (layer == 0) UV_DMA();
                if (best != 0u) {
                    VMWAIT(0);
                    u32 tg = tag; asm volatile("" : "+s"(tg));
                    BST32(tg, lane == 0 ? OFF_YF2 + (layer * 256 + b) * 4 : OOB, 0);
                    cn = SPIN_MAX;
                    if (b < 144) poll_flag4(sws, OFF_YF2 + layer * 1024, tag, cn);
                    if (cn == 0) TMO(9);
                }
                u32 k; int bad;
                const int prow = (int)(~win & 0x7ffu) - (rg * 64 - 16);
                const bool affected = prow >= (rg == 0 ? 16 : 2) && prow < 80;
                {
                    const int q = (b & 7) * 18 + (b >> 3), f0 = q < 128 ? 8 * (q & 63) + 4 * (q >> 6) : 4 * q;
                    const int cidx = f0 + 2 * (g & 1) + (g >> 1);
                    float xv = __uint_as_float((u32)*(const u16*)(smem + L_XROW + cidx * 2) << 16);
                    float hv = __uint_as_float((u32)*(const u16*)(smem + L_HROW + cidx * 2) << 16);
                    const u32 sbits = win >> 16;
                    const u32 sraw = (sbits & 0x8000u) ? (sbits & 0x7fffu) : (~sbits & 0xffffu);
                    const float sc = __uint_as_float(sraw << 16);
                    float sg_ = rbf(__builtin_amdgcn_rcpf(1.f + __builtin_amdgcn_exp2f(-sc * 1.4426950408889634f)));
                    const bool one = lane == 0 && b < 144;
                    int xo = (one && layer == 0) ? xb + 32 * q : OOB, oo = (one && layer == 1) ? (int)(~win & 0x7ffu) * 1152 + 2 * f0 : OOB;
                    int fa_ = layer == 1 ? (best == 0u ? L_BC + BC_FIN : L_BC + BC_NIL) : (affected ? L_BC + BC_NIL : L_BC + BC_GO);
                    asm volatile("" : "+v"(xv), "+v"(hv), "+v"(sg_), "+v"(xo), "+v"(oo), "+v"(fa_));
                    cn = SPIN_MAX;
                    float yf[4];
                    poll_ky(sws, OFF_KEY + (layer * 32 + (b & 31)) * 1024 + lane * 16, tag, b < 144 ? lane * 64u : (u32)OOB, yb + (q >> 2) * 16384 + (q & 3) * 16, k, yf, cn);
                    bad = (__builtin_amdgcn_ballot_w64(k > win) == 0ull || best != 0u) ? 0 : OOB;
                    sts32v(fa_, (bad ? 0u : 1u) + layer);
                    CFENCE();
                    const __amdgpu_buffer_rsrc_t ro = __builtin_amdgcn_make_buffer_rsrc((void*)out, 0, 2359296, 0x00020000);
                    const u32 ys[4] = {__float_as_uint(yf[0]), __float_as_uint(yf[1]), __float_as_uint(yf[2]), __float_as_uint(yf[3])};
                    auto p0 = __builtin_amdgcn_permlane32_swap(ys[0], ys[1], false, false);
                    auto p1 = __builtin_amdgcn_permlane32_swap(ys[2], ys[3], false, false);
                    const u32 yd = __float_as_uint(__uint_as_float(p0[0]) + __uint_as_float(p0[1])), yc = __float_as_uint(__uint_as_float(p1[0]) + __uint_as_float(p1[1]));
                    auto p2 = __builtin_amdgcn_permlane16_swap(yd, yc, false, false);
                    const float y = rsum(__uint_as_float(p2[0]) + __uint_as_float(p2[1]));
                    const u32 r1 = pk2(xv + rbf(hv + rbf(rbf(y) * sg_)), 0.f);
                    auto p3 = __builtin_amdgcn_permlane32_swap(r1, r1, false, false);
                    const u32 res = p3[0] | (p3[1] << 16);
                    auto p4 = __builtin_amdgcn_permlane16_swap(res, res, false, false);
                    BST64(((u32x2){san(p4[0]), san(p4[1])}), xo, 0);
                    __builtin_amdgcn_raw_buffer_store_b64(((u32x2){p4[0], p4[1]}), ro, oo | bad, 0, 0);
                    CFENCE();
                    if (cn == 0) TMO(10);
                }
                if (layer == 0 && bad == 0 && affected) {
                    u32 o[6], gw = 1u | (u32)prow << 8;
                    asm volatile("" : "+v"(gw));
                    cn = SPIN_MAX;
                    poll_kr(sws, xb + lane * 32, xb + 4096 + r16 * 32, L_XR + lane * 16, L_XR + 1024 + r16 * 8, o, cn);
                    sts32v(L_BC + BC_GO, gw);
                    CFENCE();
                    if (cn == 0) TMO(2);
                    {
                        if (lane >= 16) { o[4] = 0u; o[5] = 0u; }
                        float psx = 0.f, psq = 0.f;
#pragma unroll
                        for (int i = 0; i < 6; ++i) { psx = dot2(o[i], ONES2, psx); psq = dot2(o[i], o[i], psq); }
                        psx = gsum(rsum(psx)); psq = gsum(rsum(psq));
                        CFENCE();
                        const float pm0 = psx * (1.f / 576.f);
                        const float pv = fmaxf(psq * (1.f / 576.f) - pm0 * pm0, 0.f);
                        const float prs = ln_rs(pv), pm = ln_m(pm0);
                        sts32v(L_BC + BC_PM, __float_as_uint(pm)); sts32v(L_BC + BC_PM + 4, __float_as_uint(prs)); sts32v(L_BC + BC_PST, 255u);
                        if (prow >= 16) sts32v(L_VAR + (prow - 16) * 4, __float_as_uint(pv));
                    }
                }
                asm volatile("" : "+s"(bad));
                if (__builtin_expect(bad == 0, 1)) { best = win; break; }
                best = wmax(k);
                {
                    const u32 tr = ~best & 0x7ffu;
                    cn = SPIN_MAX;
                    poll_flagh(sws, XFA(tr >> 6) + layer * (OFF_XF2 - OFF_XFLAG), tag, cn);
                    if (cn == 0) TMO(11);
                    const int xs = OFF_X1G + (tr >> 4) * 18432 + (tr & 15u) * 16, cr = OFF_CROW2 + b * 1280;
                    const u32x4 r0 = __builtin_amdgcn_raw_buffer_load_b128(rs, xs + lane * 256, 0, 16), r1 = __builtin_amdgcn_raw_buffer_load_b128(rs, xs + (64 + (lane & 7)) * 256, 0, 16);
                    BST128(r0, cr + lane * 16, 0); BST128(r1, cr + 1024 + (lane & 7) * 16, 0);
                    VMWAIT(0);
                    const u32x2 e = {cr - TBIAS, OFF_STATS - TBIAS + layer * STATS_B + (tr >> 4) * 2048 + (tr & 15u) * 16};
                    if (lane < 8) sts64(ct, lane == 0 ? e : (u32x2){0u, 0u});
                    yb = OFF_YP2; xb = OFF_X2R + 5120;
                }
#pragma nounroll
                for (int h = 0; h < 3; ++h) {
                    IDS
                    FFN_ADDR(h)
                    cn = LSPIN_MAX;
                    ffn_help(mk_srd(ws, WS_BYTES), c0, g0, g1, g2, pa, L_BC + BC_HF + h * 4, ct, layer + 1, cn);
                    if ((int)cn < 1) TMO(12);
                }
                ffn_ones();
            }
            IDS
            a.skip = 1;
            if (layer == 0) BST32(tag, (b == 255 && lane == 0) ? OFF_EPOCH : OOB, 0);
            if (layer == 0 && b < 144) {
                u32 sw_ = SENT; asm volatile("" : "+v"(sw_));
                const u32x4 sv = {sw_, sw_, sw_, sw_};
#pragma unroll
                for (int i = 0; i < 4; ++i) BST128(sv, lane * 16 + i * 1024, OFF_YP + YP_B + b * 4096);
            }
            __builtin_amdgcn_s_setprio(0);
            if (layer == 0) {
                const u32x4 sws = mk_srd(ws, WS_BYTES);
                const u32 wl = OFF_WOI + cb * 82944;
                LWAIT(L_BC + BC_C2, 7, 48);
#pragma unroll
                for (int i = 0; i < 5; ++i) dma16x4_sc1(sws, lane * 16u, wl + i * 4096, L_WOE + i * 4096);
            }
        }
    }
    { u32 tmo; asm volatile("s_nop 1\n\tv_readlane_b32 %0, v109, 47" : "=s"(tmo)); if (tmo != 0u) BST32(tmo, OFF_TMO, 0); }
    asm volatile("Lcode1:");
}

extern "C" void kernel_launch(void* const* d_in, const int* in_sizes, int n_in, void* d_out, int out_size, void* d_ws, size_t ws_size,
                              hipStream_t stream) {
    (void)in_sizes; (void)n_in; (void)out_size; (void)ws_size;
    hipFuncSetAttribute((const void*)mega, hipFuncAttributeMaxDynamicSharedMemorySize, LDS_BYTES);
    mega<<<256, 512, LDS_BYTES, stream>>>((const u16*)d_in[0], (const u16*)d_in[1], (const u16*)d_in[2], (const u16*)d_in[4],
                                          (const u16*)d_in[5], (const u16*)d_in[6], (const u16*)d_in[7], (u16*)d_out, (char*)d_ws);
}
```
